```python
import torch

def _softmax(x, axis=-1):
    return torch.softmax(x, dim=axis)

def _gelu(x):
    return torch.nn.functional.gelu(x, approximate='tanh')

def _silu(x):
    return torch.nn.functional.silu(x)

def _sigmoid(x):
    return torch.sigmoid(x)

def _layernorm(x, axis=-1, eps=1e-05):
    m = x.mean(dim=axis, keepdim=True)
    v = x.var(dim=axis, keepdim=True, unbiased=False)
    return (x - m) / torch.sqrt(v + eps)

def _scatter_add(tgt, idx, src, axis=0):
    return tgt.clone().index_add_(axis, idx, src)

def _segment_sum(src, seg, n):
    out = torch.zeros((n,) + src.shape[1:], dtype=src.dtype)
    return out.index_add_(0, seg, src)

def _pad(x, pw):
    flat = tuple((v for pair in reversed(pw) for v in pair))
    return torch.nn.functional.pad(x, flat)
kernel_name = 'adaLN_cond__ssm_conv_h3__glu_gated_geglu__prompt'

def setup_inputs():

    _w = lambda a: (lambda t: t.to(torch.bfloat16) if t.is_floating_point() else t)(torch.as_tensor(a))

    def _A_setup():
        B, S, D = (1, 2048, 1024)
        ks = 4
        return {'q': _w((lambda *s: torch.randn(*s, dtype=torch.bfloat16) * 0.05)(B, S, D)), 'k': _w((lambda *s: torch.randn(*s, dtype=torch.bfloat16) * 1.6)(B, S, D)), 'v': _w((lambda *s: torch.randn(*s, dtype=torch.bfloat16) * 1.6)(B, S, D)), 'shift_k': _w((lambda *s: torch.randn(*s, dtype=torch.bfloat16) * 1.6)(D, ks) * 0.5), 'ssm_k': _w((lambda *s: torch.randn(*s, dtype=torch.bfloat16) * 1.6)(D, S) * (1.0 / S ** 0.5)), 'ssm_D': _w((lambda *s: torch.randn(*s, dtype=torch.bfloat16) * 1.6)(D))}
    _A_in = _A_setup()

    def _B_setup():
        B, S, Dm, Df = (1, 2048, 1024, 3584)
        return {'x': _w((lambda *s: torch.randn(*s, dtype=torch.bfloat16) * 0.05)(B, S, Dm)), 'w_gate': _w((lambda *s: torch.randn(*s, dtype=torch.bfloat16) * 3.2)(Dm, Df) * Dm ** (-0.5)), 'w_up': _w((lambda *s: torch.randn(*s, dtype=torch.bfloat16) * 3.2)(Dm, Df) * Dm ** (-0.5)), 'w_down': _w((lambda *s: torch.randn(*s, dtype=torch.bfloat16) * 3.2)(Df, Dm) * Df ** (-0.5))}
    _B_in = _B_setup()
    return {'A_q': _A_in['q'], 'A_k': _A_in['k'], 'A_v': _A_in['v'], 'A_shift_k': _A_in['shift_k'], 'A_ssm_k': _A_in['ssm_k'], 'A_ssm_D': _A_in['ssm_D'], 'B_x': _B_in['x'], 'B_w_gate': _B_in['w_gate'], 'B_w_up': _B_in['w_up'], 'B_w_down': _B_in['w_down'], 'c': _w((lambda *s: torch.randn(*s, dtype=torch.bfloat16) * 0.1)(1, 1024) * 8.0), 'w_mod': _w((lambda *s: torch.randn(*s, dtype=torch.bfloat16) * 0.1)(1024, 6 * 1024) * 1024 ** (-0.5) * 8.0)}

def reference(A_q, A_k, A_v, A_shift_k, A_ssm_k, A_ssm_D, B_x, B_w_gate, B_w_up, B_w_down, c, w_mod):

    def _A_ref(q, k, v, shift_k, ssm_k, ssm_D):
        B, S, D = q.shape
        ks = shift_k.shape[-1]
        k_pad = _pad(k, ((0, 0), (ks - 1, 0), (0, 0)))
        k_shift = torch.zeros_like(k)
        for t in range(ks):
            k_shift = k_shift + k_pad[:, t:t + S, :] * shift_k[:, ks - 1 - t][None, None, :]
        kv = k_shift * v
        L = 2 * S
        kv_pad = _pad(kv, ((0, 0), (0, S), (0, 0)))
        kf_pad = _pad(ssm_k, ((0, 0), (0, S)))
        kv_f = (lambda x, axes=(-2, -1): torch.fft.rfft2(x.float(), dim=axes))(kv_pad[:, None, :, :], axes=(1, 2))
        kf_f = (lambda x, axes=(-2, -1): torch.fft.rfft2(x.float(), dim=axes))(kf_pad[:, None, :], axes=(1, 2))
        kf_f = (lambda x, *d: x.permute(*d))(kf_f, 1, 2, 0)
        y_f = kv_f * kf_f[None, :, :, :]
        y = (lambda x, s, axes=(-2, -1): torch.fft.irfft2(x, s=s, dim=axes).to(torch.bfloat16))(y_f, (1, L), axes=(1, 2))[:, 0, :S, :]
        y = y + kv * ssm_D[None, None, :]
        return q * y

    def _B_ref(x, w_gate, w_up, w_down):
        g = torch.einsum('bsd,df->bsf', x, w_gate)
        u = torch.einsum('bsd,df->bsf', x, w_up)
        h = _gelu(g) * u
        return torch.einsum('bsf,fd->bsd', h, w_down)
    x = A_q
    mod = (_silu(c) @ w_mod).reshape(c.shape[0], 6, 1, -1)
    sa, ca, ga, sb, cb, gb = (mod[:, 0].clone(), mod[:, 1].clone(), mod[:, 2].clone(), mod[:, 3].clone(), mod[:, 4].clone(), mod[:, 5].clone())
    h = _layernorm(x, axis=-1) * (1 + ca) + sa
    x = x + ga * _A_ref(h, A_k, A_v, A_shift_k, A_ssm_k, A_ssm_D)
    h = _layernorm(x, axis=-1) * (1 + cb) + sb
    x = x + gb * _B_ref(h, B_w_gate, B_w_up, B_w_down)
    return x
```

```cpp
#include <hip/hip_runtime.h>
#include <hip/hip_bf16.h>
#include <stdint.h>
typedef unsigned short bf16_t;
__device__ __forceinline__ float b2f(bf16_t v){ return __uint_as_float(((unsigned)v)<<16); }
__device__ __forceinline__ bf16_t f2b(float f){ __hip_bfloat16 h = __float2bfloat16(f); return *(bf16_t*)&h; }
__device__ __forceinline__ float rb(float f){ return b2f(f2b(f)); }
constexpr int S=2048, D=1024, F=3584;

__global__ void k_mod(const bf16_t* c, const bf16_t* w, float* mod){
  int n = blockIdx.x*blockDim.x+threadIdx.x;
  float acc=0;
  for(int k=0;k<D;k++){ float cv=b2f(c[k]); float s = rb(cv/(1.f+expf(-cv))); acc += s*b2f(w[(size_t)k*6144+n]); }
  mod[n]=rb(acc);
}
__global__ void k_kv(const bf16_t* k, const bf16_t* v, const bf16_t* sk, bf16_t* kv){
  int d = blockIdx.x*blockDim.x+threadIdx.x; int t=blockIdx.y;
  float acc=0;
  for(int tt=0;tt<4;tt++){ int j=3-tt; int ts=t-j; float kk = ts>=0? b2f(k[(size_t)ts*D+d]):0.f; acc = rb(acc + rb(kk*b2f(sk[d*4+j]))); }
  kv[(size_t)d*S+t]=f2b(acc*b2f(v[(size_t)t*D+d]));
}
__global__ void k_conv(const bf16_t* kv, const bf16_t* h, const bf16_t* Dp, bf16_t* y){
  int d=blockIdx.y; int t=blockIdx.x*blockDim.x+threadIdx.x;
  __shared__ float su[S], sh[S];
  for(int i=threadIdx.x;i<S;i+=blockDim.x){ su[i]=b2f(kv[(size_t)d*S+i]); sh[i]=b2f(h[(size_t)d*S+i]); }
  __syncthreads();
  float acc=0;
  for(int s=0;s<=t;s++) acc += su[s]*sh[t-s];
  float yy = rb(acc);
  yy = rb(yy + rb(su[t]*b2f(Dp[d])));
  y[(size_t)t*D+d]=f2b(yy);
}
__device__ float blocksum(float v, float* sm){
  for(int o=32;o>0;o>>=1) v+=__shfl_xor(v,o);
  __syncthreads();
  if((threadIdx.x&63)==0) sm[threadIdx.x>>6]=v;
  __syncthreads();
  float r=0; for(int i=0;i<(int)(blockDim.x>>6);i++) r+=sm[i];
  return r;
}
__global__ void k_ln(const bf16_t* x, const bf16_t* y, const float* mod, bf16_t* x1, bf16_t* h2){
  int t=blockIdx.x; __shared__ float sm[16];
  float xv[4]; float s=0;
  for(int i=0;i<4;i++){ xv[i]=b2f(x[(size_t)t*D+threadIdx.x*4+i]); s+=xv[i]; }
  float m=blocksum(s,sm)/D; float q=0;
  for(int i=0;i<4;i++){ float d=xv[i]-m; q+=d*d; }
  float var=blocksum(q,sm)/D; float rs=1.f/sqrtf(var+1e-5f);
  float x1v[4]; s=0;
  for(int i=0;i<4;i++){ int d=threadIdx.x*4+i; float h=(xv[i]-m)*rs*(1.f+mod[1024+d])+mod[d]; h=rb(h);
    float a=rb(h*b2f(y[(size_t)t*D+d])); x1v[i]=rb(xv[i]+rb(mod[2048+d]*a)); s+=x1v[i]; }
  m=blocksum(s,sm)/D; q=0;
  for(int i=0;i<4;i++){ float d=x1v[i]-m; q+=d*d; }
  var=blocksum(q,sm)/D; rs=1.f/sqrtf(var+1e-5f);
  for(int i=0;i<4;i++){ int d=threadIdx.x*4+i; float h=(x1v[i]-m)*rs*(1.f+mod[4096+d])+mod[3072+d];
    h2[(size_t)t*D+d]=f2b(h); x1[(size_t)t*D+d]=f2b(x1v[i]); }
}
template<int MODE>
__global__ void k_gemm(const bf16_t* A, const bf16_t* B0, const bf16_t* B1, const bf16_t* x1, const float* mod, bf16_t* out, int K, int N){
  __shared__ float sa[16][17], sb0[16][17], sb1[16][17];
  int tx=threadIdx.x, ty=threadIdx.y; int row=blockIdx.y*16+ty, col=blockIdx.x*16+tx;
  float a0=0,a1=0;
  for(int k0=0;k0<K;k0+=16){
    sa[ty][tx]=b2f(A[(size_t)row*K+k0+tx]); sb0[ty][tx]=b2f(B0[(size_t)(k0+ty)*N+col]);
    if(MODE==0) sb1[ty][tx]=b2f(B1[(size_t)(k0+ty)*N+col]);
    __syncthreads();
    for(int k=0;k<16;k++){ a0+=sa[ty][k]*sb0[k][tx]; if(MODE==0) a1+=sa[ty][k]*sb1[k][tx]; }
    __syncthreads();
  }
  if(MODE==0){ float g=rb(a0), u=rb(a1); float ge=0.5f*g*(1.f+tanhf(0.7978845608f*(g+0.044715f*g*g*g))); out[(size_t)row*N+col]=f2b(rb(ge)*u); }
  else { float o=rb(a0); out[(size_t)row*N+col]=f2b(b2f(x1[(size_t)row*N+col])+rb(mod[5120+col]*o)); }
}
extern "C" void kernel_launch(void* const* d_in, const int* in_sizes, int n_in, void* d_out, int out_size, void* d_ws, size_t ws_size, hipStream_t stream){
  const bf16_t* q=(const bf16_t*)d_in[0]; const bf16_t* k=(const bf16_t*)d_in[1]; const bf16_t* v=(const bf16_t*)d_in[2];
  const bf16_t* sk=(const bf16_t*)d_in[3]; const bf16_t* ssmk=(const bf16_t*)d_in[4]; const bf16_t* ssmD=(const bf16_t*)d_in[5];
  const bf16_t* wg=(const bf16_t*)d_in[7]; const bf16_t* wu=(const bf16_t*)d_in[8]; const bf16_t* wd=(const bf16_t*)d_in[9];
  const bf16_t* c=(const bf16_t*)d_in[10]; const bf16_t* wm=(const bf16_t*)d_in[11];
  char* ws=(char*)d_ws;
  float* mod=(float*)ws; bf16_t* kv=(bf16_t*)(ws+(1<<20)); bf16_t* y=(bf16_t*)(ws+(8<<20)); bf16_t* x1=(bf16_t*)(ws+(16<<20)); bf16_t* h2=(bf16_t*)(ws+(24<<20)); bf16_t* H=(bf16_t*)(ws+(32<<20));
  k_mod<<<6144/256,256,0,stream>>>(c,wm,mod);
  k_kv<<<dim3(D/256,S),256,0,stream>>>(k,v,sk,kv);
  k_conv<<<dim3(S/256,D),256,0,stream>>>(kv,ssmk,ssmD,y);
  k_ln<<<S,256,0,stream>>>(q,y,mod,x1,h2);
  k_gemm<0><<<dim3(F/16,S/16),dim3(16,16),0,stream>>>(h2,wg,wu,nullptr,mod,H,D,F);
  k_gemm<1><<<dim3(D/16,S/16),dim3(16,16),0,stream>>>(H,wd,nullptr,x1,mod,(bf16_t*)d_out,F,D);
}
```

```cpp
#include <hip/hip_runtime.h>
#include <hip/hip_bf16.h>
#include <stdint.h>
typedef unsigned short bf16_t;
typedef unsigned u32x4 __attribute__((ext_vector_type(4)));
__device__ __forceinline__ u32x4 mksrd(const void* p, unsigned bytes){
  unsigned long long a=(unsigned long long)p;
  u32x4 r; r[0]=__builtin_amdgcn_readfirstlane((unsigned)a); r[1]=__builtin_amdgcn_readfirstlane((unsigned)(a>>32)&0xffffu); r[2]=bytes; r[3]=0x00020000u; return r;
}
#define CODE_ADDR(dst,sym) asm volatile("s_getpc_b64 s[40:41]\n s_add_u32 s40, s40, " sym "@rel32@lo+4\n s_addc_u32 s41, s41, " sym "@rel32@hi+12\n s_mov_b64 %0, s[40:41]":"=s"(dst)::"s40","s41","scc")
#define code_range(a,b) ({ unsigned long long cb_,ce_; CODE_ADDR(cb_,a); CODE_ADDR(ce_,b); mksrd((const void*)cb_,(unsigned)(ce_-cb_)); })
#define K0_NT 1
#define K0_PFNT 1
#define K1_GNT 1
#define K1_QNT 0
#define K1_PFROW0 0u
#define K2_PFROW0 0u
#define K2_PFROWS 4
#define K2_PFNT 0
#define K1_GDELAY 2
#define K1_SDELAY 2
#define WS_BASE 0
#define K3_EXTRA ,wd
#define K3_LDS 143360
#define RPT0 1
#define RPT1 1
#define RPT2 1
#define RPT3 1
#define RPT4 1
#define RPTE 0
#define K1_GMAP 0
#define K1_GPRIO 0
#define K1_SPRIO 0
#define HMUL 8
#define K0_SF 0
#define K2_SFX 1
#define K2_SFH 0
#define CPF 15
#define K2_PFEND 0
#define FUSE23 1
#define FUSE123 1
#define K4_W4 1
#define X1LATE 1
#define K0_PK 1
#define K0_XMAP 0
#define K0_BUF 1
#define K1_EARLY 1
#define K2_PRE 1
#define K0_PFLATE 0
#define K2_PK 1
#define YDMA 1
#define K2_TOUCH 0
#define K1_PPIPE 0
#define K0_PFM 0
#define K0_PFMNT 0
#define K1_PFW 0
#define K1_HPOS 1
#define K1_PDELAY 0
#define K1_WPOS 0
#define K1_PF2 3
#define K1_NREP 8
#define K1_FSTR 1
#define K1_PSLEEP 1
#define K1_REPX (blk>>1)
#define WS_X1 11
#define WS_H2 15
#define WS_HT 19
#define FUSE4 1
#define K4_PDELAY 0
#define K4_PSLEEP 1
#define CPF_FIRST "k123_gateup"
#define STAMP_BEGIN
#define STAMP_MID(i)
#define STAMP_MIDW(i)
#define STAMP_END
__device__ __forceinline__ float blo(unsigned u){ return __uint_as_float(u<<16); }
__device__ __forceinline__ float bhi(unsigned u){ return __uint_as_float(u&0xffff0000u); }
__device__ __forceinline__ unsigned pkbf(float a,float b){ unsigned r; asm("v_cvt_pk_bf16_f32 %0, %1, %2":"=v"(r):"v"(a),"v"(b)); return r; }
template<int CTRL> __device__ __forceinline__ float dppf(float x){ return __int_as_float(__builtin_amdgcn_update_dpp(0,__float_as_int(x),CTRL,0xf,0xf,true)); }
__device__ __forceinline__ float wave_sum(float x){
  x+=dppf<0xB1>(x);
  x+=dppf<0x4E>(x);
  x+=dppf<0x141>(x);
  x+=dppf<0x140>(x);
  { auto r=__builtin_amdgcn_permlane16_swap(__float_as_uint(x),__float_as_uint(x),false,false); x=__uint_as_float(r[0])+__uint_as_float(r[1]); }
  { auto r=__builtin_amdgcn_permlane32_swap(__float_as_uint(x),__float_as_uint(x),false,false); x=__uint_as_float(r[0])+__uint_as_float(r[1]); }
  return x;
}
__device__ __forceinline__ float red8(const float* p, unsigned lane){
  float a[4];
  #pragma unroll
  for(int j=0;j<4;j++){ auto r=__builtin_amdgcn_permlane32_swap(__float_as_uint(p[j]),__float_as_uint(p[j+4]),false,false); a[j]=__uint_as_float(r[0])+__uint_as_float(r[1]); }
  float b[2];
  #pragma unroll
  for(int j=0;j<2;j++){ auto r=__builtin_amdgcn_permlane16_swap(__float_as_uint(a[j]),__float_as_uint(a[j+2]),false,false); b[j]=__uint_as_float(r[0])+__uint_as_float(r[1]); }
  const bool o=(lane&8)!=0;
  float keep=o?b[1]:b[0], send=o?b[0]:b[1];
  float x=keep+dppf<0x128>(send);
  x+=dppf<0xB1>(x); x+=dppf<0x4E>(x); x+=dppf<0x141>(x);
  return x;
}
template<int NT> __device__ __forceinline__ void pf_lds(u32x4 srd, unsigned voff, unsigned soff, unsigned ldsaddr){
  if(NT) asm volatile("s_mov_b32 m0, %3\n s_nop 0\n buffer_load_dwordx4 %0, %1, %2 offen nt lds"::"v"(voff),"s"(srd),"s"(soff),"s"(ldsaddr):"memory");
  else asm volatile("s_mov_b32 m0, %3\n s_nop 0\n buffer_load_dwordx4 %0, %1, %2 offen lds"::"v"(voff),"s"(srd),"s"(soff),"s"(ldsaddr):"memory");
}
template<int NT> __device__ __forceinline__ void ld16(u32x4& d, const void* p){
  if(NT) asm volatile("global_load_dwordx4 %0, %1, off nt":"=v"(d):"v"(p):"memory");
  else asm volatile("global_load_dwordx4 %0, %1, off":"=v"(d):"v"(p):"memory");
}
template<int FL> __device__ __forceinline__ void st16(void* p, u32x4 v){
  if(FL==1) asm volatile("global_store_dwordx4 %0, %1, off sc1\n s_nop 1"::"v"(p),"v"(v):"memory");
  else if(FL==2) asm volatile("global_store_dwordx4 %0, %1, off sc0 sc1\n s_nop 1"::"v"(p),"v"(v):"memory");
  else if(FL==3) asm volatile("global_store_dwordx4 %0, %1, off nt\n s_nop 1"::"v"(p),"v"(v):"memory");
  else if(FL==4) asm volatile("global_store_dwordx4 %0, %1, off sc1 nt\n s_nop 1"::"v"(p),"v"(v):"memory");
  else *(u32x4*)p=v;
}
template<int FL> __device__ __forceinline__ void st8(void* p, uint2 v){
  typedef unsigned u32x2_t __attribute__((ext_vector_type(2)));
  u32x2_t x={v.x,v.y};
  if(FL==1) asm volatile("global_store_dwordx2 %0, %1, off sc1"::"v"(p),"v"(x):"memory");
  else if(FL==2) asm volatile("global_store_dwordx2 %0, %1, off sc0 sc1"::"v"(p),"v"(x):"memory");
  else if(FL==3) asm volatile("global_store_dwordx2 %0, %1, off nt"::"v"(p),"v"(x):"memory");
  else if(FL==4) asm volatile("global_store_dwordx2 %0, %1, off sc1 nt"::"v"(p),"v"(x):"memory");
  else *(uint2*)p=v;
}
#define LD_WAIT(n) asm volatile("s_waitcnt vmcnt(" #n ")":::"memory")
#define LD_PIN(x) asm volatile("":"+v"(x))
extern "C" __global__ __launch_bounds__(256) void k0_pre(const bf16_t* __restrict__ k, const bf16_t* __restrict__ v,
    const bf16_t* __restrict__ sk, const bf16_t* __restrict__ ssmk, bf16_t* __restrict__ kvTr, unsigned* __restrict__ flags
){
  STAMP_BEGIN
  __shared__ __attribute__((aligned(1024))) char junk[4096];
  const unsigned tid=threadIdx.x, lane=tid&63;
  const unsigned w=__builtin_amdgcn_readfirstlane(tid>>6);
  const unsigned blk=blockIdx.x;
  const int tt=blk>>4, ct=blk&15;
  if(tid<128) flags[blk*128+tid]=0u;
  const int c8=tid&7, tq=tid>>3;
  const int t0=tt*128+tq*4, ch0=ct*64+c8*8;
  u32x4 kr[7], vr[4], sr[4];
  const u32x4 srdK=mksrd(k,4194304u), srdV=mksrd(v,4194304u), srdS=mksrd(sk,8192u), srdO=mksrd(kvTr,4194304u);
  const unsigned vo=(unsigned)t0*2048u+(unsigned)ch0*2u, vp=vo-(t0?6144u:0u), vs=(unsigned)ch0*8u;
#define K0_LD(d,off,srd,so,im,fl) asm volatile("buffer_load_dwordx4 %0, %1, %2, %3 offen offset:" #im fl:"=v"(d):"v"(off),"s"(srd),"s"(so):"memory")
  K0_LD(kr[3],vo,srdK,0u,0," nt"); K0_LD(kr[4],vo,srdK,0u,2048," nt"); K0_LD(kr[5],vo,srdK,4096u,0," nt"); K0_LD(kr[6],vo,srdK,4096u,2048," nt");
  K0_LD(vr[0],vo,srdV,0u,0," nt"); K0_LD(vr[1],vo,srdV,0u,2048," nt"); K0_LD(vr[2],vo,srdV,4096u,0," nt"); K0_LD(vr[3],vo,srdV,4096u,2048," nt");
  K0_LD(kr[0],vp,srdK,0u,0," nt"); K0_LD(kr[1],vp,srdK,0u,2048," nt"); K0_LD(kr[2],vp,srdK,4096u,0," nt");
  K0_LD(sr[0],vs,srdS,0u,0,""); K0_LD(sr[1],vs,srdS,0u,16,""); K0_LD(sr[2],vs,srdS,0u,32,""); K0_LD(sr[3],vs,srdS,0u,48,"");
  auto pf0=[&]()
  {
    u32x4 srd=mksrd(ssmk,1024u*4096u);
    const unsigned jb=(unsigned)(size_t)junk+w*1024u;
    const unsigned row=(blk&7)*128u+(blk>>3)*4u+w;
    #pragma unroll
    for(int j=0;j<4;j++) pf_lds<K0_PFNT>(srd,row*4096u+lane*16u,j*1024u,jb);
    pf_lds<0>(code_range(CPF_FIRST,"k4_code_end"),((blk>>3)*4u+w)*1024u+lane*16u,0u,jb);
  };
  pf0();
  LD_WAIT(5);
  #pragma unroll
  for(int j=0;j<7;j++) LD_PIN(kr[j]);
  #pragma unroll
  for(int j=0;j<4;j++){ LD_PIN(vr[j]); LD_PIN(sr[j]); }
  STAMP_MID(1)
  if(t0==0){
    #pragma unroll
    for(int j=0;j<3;j++) kr[j]=(u32x4){0,0,0,0};
  }
  typedef float f2_t __attribute__((ext_vector_type(2)));
  typedef unsigned w2_t __attribute__((ext_vector_type(2)));
  #pragma unroll
  for(int cp=0;cp<4;cp++){
    const f2_t a0={blo(sr[cp][0]),blo(sr[cp][2])}, a1={bhi(sr[cp][0]),bhi(sr[cp][2])}, a2={blo(sr[cp][1]),blo(sr[cp][3])}, a3={bhi(sr[cp][1]),bhi(sr[cp][3])};
    f2_t kk[7], r[4];
    #pragma unroll
    for(int j=0;j<7;j++) kk[j]=(f2_t){blo(kr[j][cp]),bhi(kr[j][cp])};
    #pragma unroll
    for(int j=0;j<4;j++){
      f2_t s=kk[j]*a3; s=__builtin_elementwise_fma(kk[j+1],a2,s); s=__builtin_elementwise_fma(kk[j+2],a1,s); s=__builtin_elementwise_fma(kk[j+3],a0,s);
      r[j]=s*(f2_t){blo(vr[j][cp]),bhi(vr[j][cp])};
    }
    const w2_t o0={pkbf(r[3].x,r[2].x),pkbf(r[1].x,r[0].x)}, o1={pkbf(r[3].y,r[2].y),pkbf(r[1].y,r[0].y)};
    const unsigned oo=(unsigned)ch0*4096u+(unsigned)(2044-t0)*2u;
    asm volatile("buffer_store_dwordx2 %0, %1, %2, %3 offen"::"v"(o0),"v"(oo),"s"(srdO),"s"(cp*8192u):"memory");
    asm volatile("buffer_store_dwordx2 %0, %1, %2, %3 offen"::"v"(o1),"v"(oo),"s"(srdO),"s"(cp*8192u+4096u):"memory");
    __builtin_amdgcn_sched_barrier(0);
  }
  STAMP_MID(3)
  asm volatile("s_waitcnt vmcnt(0)":::"memory");
  STAMP_END
}
#define K1_QLAST 0
#define K1_PFROWS 0u
#define K23_EARLY 1
#define K4_ROTEXPR (((idx&3)+(idx>>2))&1)
#define K4_NAFT 24
#define K4_PH 1
#define K1_TGS 16512u
typedef __attribute__((address_space(3))) char* ldsp_t;
typedef short s16x4_t __attribute__((ext_vector_type(4)));
typedef __bf16 bf16x8_t __attribute__((ext_vector_type(8)));
typedef float f32x4_t __attribute__((ext_vector_type(4)));
typedef unsigned u32x2_t __attribute__((ext_vector_type(2)));
__device__ __forceinline__ u32x2_t lds_tr(unsigned a){ return __builtin_bit_cast(u32x2_t,__builtin_amdgcn_ds_read_tr16_b64_v4i16((__attribute__((address_space(3))) s16x4_t*)(size_t)a)); }
__device__ __forceinline__ void ld16sc(u32x4& d, const void* p){ asm volatile("global_load_dwordx4 %0, %1, off sc1":"=v"(d):"v"(p):"memory"); }
__device__ __forceinline__ float2 wave_stats(const float (&x)[8][4], unsigned lane){
  float ps[8];
  #pragma unroll
  for(int t=0;t<8;t++) ps[t]=(x[t][0]+x[t][1])+(x[t][2]+x[t][3]);
  const float mw=red8(ps,lane)*(1.f/256.f);
  float pq[8];
  #pragma unroll
  for(int t=0;t<8;t++){
    const float m=__uint_as_float(__builtin_amdgcn_readlane(__float_as_uint(mw),t*8));
    float s=0.f;
    #pragma unroll
    for(int e=0;e<4;e++){ float d=x[t][e]-m; s=fmaf(d,d,s); }
    pq[t]=s;
  }
  return make_float2(mw,red8(pq,lane));
}
typedef float f2v __attribute__((ext_vector_type(2)));
__device__ __forceinline__ f2v up2(unsigned v){ return (f2v){blo(v),bhi(v)}; }
__device__ __forceinline__ f2v asf2(unsigned a,unsigned b){ return (f2v){__uint_as_float(a),__uint_as_float(b)}; }
__device__ __forceinline__ float2 wave_stats2(const f2v (&x)[8][2], unsigned lane){
  float ps[8];
  #pragma unroll
  for(int t=0;t<8;t++){ const f2v p=x[t][0]+x[t][1]; ps[t]=p.x+p.y; }
  const float mw=red8(ps,lane)*(1.f/256.f);
  float pq[8];
  #pragma unroll
  for(int t=0;t<8;t++){
    const float m=__uint_as_float(__builtin_amdgcn_readlane(__float_as_uint(mw),t*8));
    const f2v mm={m,m}, d0=x[t][0]-mm, d1=x[t][1]-mm;
    const f2v s=__builtin_elementwise_fma(d1,d1,d0*d0);
    pq[t]=s.x+s.y;
  }
  return make_float2(mw,red8(pq,lane));
}
__device__ __forceinline__ float2 comb_stats(const float2* ldsS, unsigned lane){
  const unsigned t=lane&7;
  const float2 a=ldsS[t],b=ldsS[8+t],c=ldsS[16+t],d=ldsS[24+t];
  const float mean=0.25f*((a.x+b.x)+(c.x+d.x));
  const float da=a.x-mean,db=b.x-mean,dc=c.x-mean,dd=d.x-mean;
  const float M2=(a.y+b.y)+(c.y+d.y)+256.f*((da*da+db*db)+(dc*dc+dd*dd));
  return make_float2(mean,rsqrtf(M2*(1.f/1024.f)+1e-5f));
}
#define L_CONV 28672u
#define L_MODW 81920u
#define L_SV 147456u
#define L_PART 149504u
#define L_JUNK 150528u
extern "C" __global__ __launch_bounds__(256,1) void k123_gateup(const bf16_t* __restrict__ ssmk, const bf16_t* __restrict__ kvTr, const bf16_t* __restrict__ ssmD,
    char* yg, const bf16_t* __restrict__ cvec, const bf16_t* __restrict__ wmod, float* modp, float* __restrict__ modf, const bf16_t* __restrict__ q,
    bf16_t* __restrict__ x1t, bf16_t* h2t, unsigned* flags,
    const bf16_t* __restrict__ wg, const bf16_t* __restrict__ wu, bf16_t* Ht, const bf16_t* __restrict__ wd
  , bf16_t* __restrict__ out
){
  __shared__ __attribute__((aligned(1024))) char lds3[K3_LDS+18432+512];
  const unsigned tid=threadIdx.x, lane=tid&63;
  const unsigned w=__builtin_amdgcn_readfirstlane(tid>>6);
  const unsigned blk=blockIdx.x, xcd=blk&7, idx=blk>>3;
  {
    const unsigned d0=(xcd*32+idx)*4+w, b0=(unsigned)(size_t)lds3+L_CONV+w*12928u;
    const u32x4 sH=mksrd(ssmk,1024u*4096u), sU=mksrd(kvTr,1024u*4096u);
    const unsigned l0=lane*16u, l1=lane*16u+2u;
    asm volatile(
      "s_add_u32 m0, %5, 8320\n s_nop 0\n"
      "buffer_load_dwordx4 %0, %3, %4 offen offset:0 lds\n buffer_load_dwordx4 %0, %3, %4 offen offset:1024 lds\n"
      "buffer_load_dwordx4 %0, %3, %4 offen offset:2048 lds\n buffer_load_dwordx4 %0, %3, %4 offen offset:3072 lds\n"
      "s_add_u32 m0, %5, 32\n s_nop 0\n"
      "buffer_load_dwordx4 %0, %2, %4 offen offset:0 lds\n buffer_load_dwordx4 %0, %2, %4 offen offset:1024 lds\n"
      "buffer_load_dwordx4 %0, %2, %4 offen offset:2048 lds\n buffer_load_dwordx4 %0, %2, %4 offen offset:3072 lds\n"
      "s_add_u32 m0, %5, 4192\n s_nop 0\n"
      "buffer_load_dwordx4 %1, %2, %4 offen offset:0 lds\n buffer_load_dwordx4 %1, %2, %4 offen offset:1024 lds\n"
      "buffer_load_dwordx4 %1, %2, %4 offen offset:2048 lds\n buffer_load_dwordx4 %1, %2, %4 offen offset:3072 lds\n"
      ::"v"(l0),"v"(l1),"s"(sH),"s"(sU),"s"(d0*4096u),"s"(b0):"scc","memory");
    __builtin_amdgcn_sched_barrier(0);
  }
  const unsigned mb=idx&7, fb=xcd*4+(idx>>3);
  const unsigned T=mb*4+w;
  const unsigned odd=fb&1;
  const unsigned ldsb=(unsigned)(size_t)lds3;
  const unsigned dreg=ldsb+w*7168u;
  const unsigned tg=mb*32+fb;
  const unsigned cg=xcd*32+idx;
  float2* ldsS=(float2*)(lds3+K3_LDS+18432);
  const unsigned mu=cg&63u, msl=(mu>>2)*3u+(mu&3u), mcb=(msl>>3)*16u+(cg>>6)*4u+((msl&7u)>>1), mhalf=msl&1u;
  const bool has=(mu&3u)!=3u;
  u32x2_t qv[8], cv2;
  {
    const unsigned lb1=ldsb+L_CONV;
    const unsigned d=cg*4+w;
    const unsigned base=lb1+w*12928u;
    const unsigned r=lane&15, g=lane>>4;
    const unsigned aad=base+(((r+1)&1)?4160u:0u)+16u*g+4u*((r+1)>>1);
    const unsigned bad=base+8320u+480u-32u*r+16u*g;
    const unsigned kad=base+8320u+504u-32u*r-8u*g;
    const unsigned yw=base+160u*g+8u*r;
    const unsigned tgl=tid>>2, p=tid&3;
    const unsigned yr=lb1+640u*(tgl>>5)+160u*(2u*(tgl&1)+(p>>1))+8u*((tgl>>1)&15)+4u*(p&1);
    const unsigned st=tgl*K1_TGS+cg*64u+p*16u;
    const unsigned z0=base+(lane<8?4u*lane:8288u+4u*(lane&7));
    const unsigned msk=lane==7?0xffffffffu:0u;
    const unsigned zu=base+8320u+4096u+lane*8u;
    const unsigned pf=(K1_PFROW0+(idx*4+w)*K1_PFROWS)*7168u+xcd*896u+(lane<56?lane:55u)*16u;
    const bf16_t* dptr=ssmD+(d&~1u);
    u32x4 srdH=mksrd(ssmk,1024u*4096u), srdU=mksrd(kvTr,1024u*4096u), srdY=mksrd(yg,256u*K1_TGS), srdP=mksrd(wg,1024u*7168u), srdQ=mksrd(wu,1024u*7168u);
    u32x4 srdX=mksrd(q,2048u*2048u), srdM=mksrd(wmod,has?1024u*12288u:0u), srdC=mksrd(cvec,2048u);
    asm volatile(
  "s_nop 4\n"
  "s_mov_b32 s28, 0\n"
  "s_load_dword s22, s[62:63], 0\n"
  "buffer_load_ushort v48, off, s[8:11], s20\n"
  "v_mov_b32 v50, 0\n"
  "v_mov_b32 v51, 0\n"
  "ds_write_b64 v45, v[50:51]\n"
  "ds_write_b32 v43, v50\n"
  "s_waitcnt vmcnt(0)\n"
  "v_lshlrev_b32 v48, 16, v48\n"
  "v_and_b32 v48, v48, v44\n"
  "v_add_u32 v49, 4160, v43\n"
  "ds_write_b32 v49, v48\n"
  "s_waitcnt lgkmcnt(0)\n"
  "s_lshl_b32 s22, s22, s29\n"
  "s_and_b32 s22, s22, 0xffff0000\n"
  "ds_read2_b32 a[0:1], v34 offset0:0 offset1:1\n"
  "ds_read2_b32 a[2:3], v34 offset0:2 offset1:3\n"
  "ds_read_b128 a[64:67], v38 offset:3584\n"
  "ds_read_b128 a[68:71], v38 offset:3072\n"
  "ds_read_b128 a[72:75], v38 offset:2560\n"
  "ds_read_b128 a[76:79], v38 offset:2048\n"
  "ds_read_b128 a[80:83], v38 offset:1536\n"
  "ds_read_b128 a[84:87], v38 offset:1024\n"
  "ds_read_b128 a[88:91], v38 offset:512\n"
  "ds_read_b128 a[92:95], v38 offset:0\n"
  "ds_read2_b32 a[4:5], v34 offset0:128 offset1:129\n"
  "ds_read2_b32 a[6:7], v34 offset0:130 offset1:131\n"
  "ds_read2_b32 a[8:9], v35 offset0:0 offset1:1\n"
  "ds_read2_b32 a[10:11], v35 offset0:2 offset1:3\n"
  "ds_read2_b32 a[12:13], v35 offset0:128 offset1:129\n"
  "s_waitcnt lgkmcnt(14)\n"
  "ds_read2_b32 a[14:15], v35 offset0:130 offset1:131\n"
  "s_waitcnt lgkmcnt(14)\n"
  "ds_read2_b32 a[16:17], v36 offset0:0 offset1:1\n"
  "s_waitcnt lgkmcnt(14)\n"
  "ds_read2_b32 a[18:19], v36 offset0:2 offset1:3\n"
  "s_waitcnt lgkmcnt(14)\n"
  "ds_read2_b32 a[20:21], v36 offset0:128 offset1:129\n"
  "s_waitcnt lgkmcnt(14)\n"
  "ds_read2_b32 a[22:23], v36 offset0:130 offset1:131\n"
  "s_waitcnt lgkmcnt(14)\n"
  "ds_read2_b32 a[24:25], v37 offset0:0 offset1:1\n"
  "s_waitcnt lgkmcnt(14)\n"
  "ds_read2_b32 a[26:27], v37 offset0:2 offset1:3\n"
  "s_waitcnt lgkmcnt(14)\n"
  "ds_read2_b32 a[28:29], v37 offset0:128 offset1:129\n"
  "s_waitcnt lgkmcnt(14)\n"
  "ds_read2_b32 a[30:31], v37 offset0:130 offset1:131\n"
  "v_mfma_f32_16x16x32_bf16 v[0:3], a[0:3], a[64:67], 0\n"
  "v_mfma_f32_16x16x32_bf16 v[4:7], a[0:3], a[68:71], 0\n"
  "v_mfma_f32_16x16x32_bf16 v[8:11], a[0:3], a[72:75], 0\n"
  "s_mov_b32 m0, s61\n"
  "s_nop 0\n"
  "buffer_load_dwordx4 v32, s[68:71], s72 offen lds\n"
  "s_waitcnt lgkmcnt(14)\n"
  "ds_read2_b32 a[32:33], v34 offset0:16 offset1:17\n"
  "v_mfma_f32_16x16x32_bf16 v[12:15], a[0:3], a[76:79], 0\n"
  "s_waitcnt lgkmcnt(14)\n"
  "ds_read2_b32 a[34:35], v34 offset0:18 offset1:19\n"
  "v_mfma_f32_16x16x32_bf16 v[16:19], a[0:3], a[80:83], 0\n"
  "s_waitcnt lgkmcnt(14)\n"
  "ds_read_b128 a[96:99], v38 offset:3648\n"
  "v_mfma_f32_16x16x32_bf16 v[20:23], a[0:3], a[84:87], 0\n"
  "s_waitcnt lgkmcnt(14)\n"
  "ds_read_b128 a[100:103], v38 offset:3136\n"
  "v_mfma_f32_16x16x32_bf16 v[24:27], a[0:3], a[88:91], 0\n"
  "s_waitcnt lgkmcnt(14)\n"
  "ds_read_b128 a[104:107], v38 offset:2624\n"
  "v_mfma_f32_16x16x32_bf16 v[28:31], a[0:3], a[92:95], 0\n"
  "s_waitcnt lgkmcnt(14)\n"
  "ds_read_b128 a[108:111], v38 offset:2112\n"
  "v_mfma_f32_16x16x32_bf16 v[4:7], a[4:7], a[64:67], v[4:7]\n"
  "s_waitcnt lgkmcnt(14)\n"
  "ds_read_b128 a[112:115], v38 offset:1600\n"
  "v_mfma_f32_16x16x32_bf16 v[8:11], a[4:7], a[68:71], v[8:11]\n"
  "s_waitcnt lgkmcnt(14)\n"
  "ds_read_b128 a[116:119], v38 offset:1088\n"
  "v_mfma_f32_16x16x32_bf16 v[12:15], a[4:7], a[72:75], v[12:15]\n"
  "s_waitcnt lgkmcnt(14)\n"
  "ds_read_b128 a[120:123], v38 offset:576\n"
  "v_mfma_f32_16x16x32_bf16 v[16:19], a[4:7], a[76:79], v[16:19]\n"
  "s_waitcnt lgkmcnt(14)\n"
  "ds_read_b128 a[124:127], v38 offset:64\n"
  "v_mfma_f32_16x16x32_bf16 v[20:23], a[4:7], a[80:83], v[20:23]\n"
  "s_waitcnt lgkmcnt(14)\n"
  "ds_read2_b32 a[36:37], v34 offset0:144 offset1:145\n"
  "v_mfma_f32_16x16x32_bf16 v[24:27], a[4:7], a[84:87], v[24:27]\n"
  "s_mov_b32 s47, 0\n"
  "buffer_load_dwordx2 v[68:69], v86, s[52:55], s47 offen\n"
  "s_waitcnt lgkmcnt(14)\n"
  "ds_read2_b32 a[38:39], v34 offset0:146 offset1:147\n"
  "v_mfma_f32_16x16x32_bf16 v[28:31], a[4:7], a[88:91], v[28:31]\n"
  "s_waitcnt lgkmcnt(14)\n"
  "ds_read2_b32 a[40:41], v35 offset0:16 offset1:17\n"
  "v_mfma_f32_16x16x32_bf16 v[8:11], a[8:11], a[64:67], v[8:11]\n"
  "s_waitcnt lgkmcnt(14)\n"
  "ds_read2_b32 a[42:43], v35 offset0:18 offset1:19\n"
  "v_mfma_f32_16x16x32_bf16 v[12:15], a[8:11], a[68:71], v[12:15]\n"
  "s_waitcnt lgkmcnt(14)\n"
  "ds_read2_b32 a[44:45], v35 offset0:144 offset1:145\n"
  "v_mfma_f32_16x16x32_bf16 v[16:19], a[8:11], a[72:75], v[16:19]\n"
  "s_waitcnt lgkmcnt(14)\n"
  "ds_read2_b32 a[46:47], v35 offset0:146 offset1:147\n"
  "v_mfma_f32_16x16x32_bf16 v[20:23], a[8:11], a[76:79], v[20:23]\n"
  "s_waitcnt lgkmcnt(14)\n"
  "ds_read2_b32 a[48:49], v36 offset0:16 offset1:17\n"
  "v_mfma_f32_16x16x32_bf16 v[24:27], a[8:11], a[80:83], v[24:27]\n"
  "s_waitcnt lgkmcnt(14)\n"
  "ds_read2_b32 a[50:51], v36 offset0:18 offset1:19\n"
  "v_mfma_f32_16x16x32_bf16 v[28:31], a[8:11], a[84:87], v[28:31]\n"
  "s_waitcnt lgkmcnt(14)\n"
  "ds_read2_b32 a[52:53], v36 offset0:144 offset1:145\n"
  "v_mfma_f32_16x16x32_bf16 v[12:15], a[12:15], a[64:67], v[12:15]\n"
  "s_waitcnt lgkmcnt(14)\n"
  "ds_read2_b32 a[54:55], v36 offset0:146 offset1:147\n"
  "v_mfma_f32_16x16x32_bf16 v[16:19], a[12:15], a[68:71], v[16:19]\n"
  "s_waitcnt lgkmcnt(14)\n"
  "ds_read2_b32 a[56:57], v37 offset0:16 offset1:17\n"
  "v_mfma_f32_16x16x32_bf16 v[20:23], a[12:15], a[72:75], v[20:23]\n"
  "s_waitcnt lgkmcnt(14)\n"
  "ds_read2_b32 a[58:59], v37 offset0:18 offset1:19\n"
  "v_mfma_f32_16x16x32_bf16 v[24:27], a[12:15], a[76:79], v[24:27]\n"
  "s_mov_b32 s47, 2048\n"
  "buffer_load_dwordx2 v[70:71], v86, s[52:55], s47 offen\n"
  "s_waitcnt lgkmcnt(14)\n"
  "ds_read2_b32 a[60:61], v37 offset0:144 offset1:145\n"
  "v_mfma_f32_16x16x32_bf16 v[28:31], a[12:15], a[80:83], v[28:31]\n"
  "s_waitcnt lgkmcnt(14)\n"
  "ds_read2_b32 a[62:63], v37 offset0:146 offset1:147\n"
  "v_mfma_f32_16x16x32_bf16 v[16:19], a[16:19], a[64:67], v[16:19]\n"
  "v_mfma_f32_16x16x32_bf16 v[20:23], a[16:19], a[68:71], v[20:23]\n"
  "v_mfma_f32_16x16x32_bf16 v[24:27], a[16:19], a[72:75], v[24:27]\n"
  "v_mfma_f32_16x16x32_bf16 v[28:31], a[16:19], a[76:79], v[28:31]\n"
  "v_mfma_f32_16x16x32_bf16 v[20:23], a[20:23], a[64:67], v[20:23]\n"
  "v_mfma_f32_16x16x32_bf16 v[24:27], a[20:23], a[68:71], v[24:27]\n"
  "v_mfma_f32_16x16x32_bf16 v[28:31], a[20:23], a[72:75], v[28:31]\n"
  "v_mfma_f32_16x16x32_bf16 v[24:27], a[24:27], a[64:67], v[24:27]\n"
  "v_mfma_f32_16x16x32_bf16 v[28:31], a[24:27], a[68:71], v[28:31]\n"
  "v_mfma_f32_16x16x32_bf16 v[28:31], a[28:31], a[64:67], v[28:31]\n"
  "s_mov_b32 s47, 4096\n"
  "buffer_load_dwordx2 v[72:73], v86, s[52:55], s47 offen\n"
  "v_mfma_f32_16x16x32_bf16 v[0:3], a[32:35], a[96:99], v[0:3]\n"
  "v_mfma_f32_16x16x32_bf16 v[4:7], a[32:35], a[100:103], v[4:7]\n"
  "v_mfma_f32_16x16x32_bf16 v[8:11], a[32:35], a[104:107], v[8:11]\n"
  "s_waitcnt lgkmcnt(14)\n"
  "ds_read2_b32 a[0:1], v34 offset0:32 offset1:33\n"
  "v_mfma_f32_16x16x32_bf16 v[12:15], a[32:35], a[108:111], v[12:15]\n"
  "s_waitcnt lgkmcnt(14)\n"
  "ds_read2_b32 a[2:3], v34 offset0:34 offset1:35\n"
  "v_mfma_f32_16x16x32_bf16 v[16:19], a[32:35], a[112:115], v[16:19]\n"
  "s_waitcnt lgkmcnt(14)\n"
  "ds_read_b128 a[64:67], v38 offset:3712\n"
  "v_mfma_f32_16x16x32_bf16 v[20:23], a[32:35], a[116:119], v[20:23]\n"
  "s_waitcnt lgkmcnt(14)\n"
  "ds_read_b128 a[68:71], v38 offset:3200\n"
  "v_mfma_f32_16x16x32_bf16 v[24:27], a[32:35], a[120:123], v[24:27]\n"
  "s_waitcnt lgkmcnt(14)\n"
  "ds_read_b128 a[72:75], v38 offset:2688\n"
  "v_mfma_f32_16x16x32_bf16 v[28:31], a[32:35], a[124:127], v[28:31]\n"
  "s_waitcnt lgkmcnt(14)\n"
  "ds_read_b128 a[76:79], v38 offset:2176\n"
  "v_mfma_f32_16x16x32_bf16 v[4:7], a[36:39], a[96:99], v[4:7]\n"
  "s_waitcnt lgkmcnt(14)\n"
  "ds_read_b128 a[80:83], v38 offset:1664\n"
  "v_mfma_f32_16x16x32_bf16 v[8:11], a[36:39], a[100:103], v[8:11]\n"
  "s_waitcnt lgkmcnt(14)\n"
  "ds_read_b128 a[84:87], v38 offset:1152\n"
  "v_mfma_f32_16x16x32_bf16 v[12:15], a[36:39], a[104:107], v[12:15]\n"
  "s_mov_b32 s47, 6144\n"
  "buffer_load_dwordx2 v[74:75], v86, s[52:55], s47 offen\n"
  "s_waitcnt lgkmcnt(14)\n"
  "ds_read_b128 a[88:91], v38 offset:640\n"
  "v_mfma_f32_16x16x32_bf16 v[16:19], a[36:39], a[108:111], v[16:19]\n"
  "s_waitcnt lgkmcnt(14)\n"
  "ds_read_b128 a[92:95], v38 offset:128\n"
  "v_mfma_f32_16x16x32_bf16 v[20:23], a[36:39], a[112:115], v[20:23]\n"
  "s_waitcnt lgkmcnt(14)\n"
  "ds_read2_b32 a[4:5], v34 offset0:160 offset1:161\n"
  "v_mfma_f32_16x16x32_bf16 v[24:27], a[36:39], a[116:119], v[24:27]\n"
  "s_waitcnt lgkmcnt(14)\n"
  "ds_read2_b32 a[6:7], v34 offset0:162 offset1:163\n"
  "v_mfma_f32_16x16x32_bf16 v[28:31], a[36:39], a[120:123], v[28:31]\n"
  "s_waitcnt lgkmcnt(14)\n"
  "ds_read2_b32 a[8:9], v35 offset0:32 offset1:33\n"
  "v_mfma_f32_16x16x32_bf16 v[8:11], a[40:43], a[96:99], v[8:11]\n"
  "s_waitcnt lgkmcnt(14)\n"
  "ds_read2_b32 a[10:11], v35 offset0:34 offset1:35\n"
  "v_mfma_f32_16x16x32_bf16 v[12:15], a[40:43], a[100:103], v[12:15]\n"
  "s_waitcnt lgkmcnt(14)\n"
  "ds_read2_b32 a[12:13], v35 offset0:160 offset1:161\n"
  "v_mfma_f32_16x16x32_bf16 v[16:19], a[40:43], a[104:107], v[16:19]\n"
  "s_waitcnt lgkmcnt(14)\n"
  "ds_read2_b32 a[14:15], v35 offset0:162 offset1:163\n"
  "v_mfma_f32_16x16x32_bf16 v[20:23], a[40:43], a[108:111], v[20:23]\n"
  "s_waitcnt lgkmcnt(14)\n"
  "ds_read2_b32 a[16:17], v36 offset0:32 offset1:33\n"
  "v_mfma_f32_16x16x32_bf16 v[24:27], a[40:43], a[112:115], v[24:27]\n"
  "s_waitcnt lgkmcnt(14)\n"
  "ds_read2_b32 a[18:19], v36 offset0:34 offset1:35\n"
  "v_mfma_f32_16x16x32_bf16 v[28:31], a[40:43], a[116:119], v[28:31]\n"
  "s_waitcnt lgkmcnt(14)\n"
  "ds_read2_b32 a[20:21], v36 offset0:160 offset1:161\n"
  "v_mfma_f32_16x16x32_bf16 v[12:15], a[44:47], a[96:99], v[12:15]\n"
  "s_mov_b32 s47, 8192\n"
  "buffer_load_dwordx2 v[76:77], v86, s[52:55], s47 offen\n"
  "s_waitcnt lgkmcnt(14)\n"
  "ds_read2_b32 a[22:23], v36 offset0:162 offset1:163\n"
  "v_mfma_f32_16x16x32_bf16 v[16:19], a[44:47], a[100:103], v[16:19]\n"
  "s_waitcnt lgkmcnt(14)\n"
  "ds_read2_b32 a[24:25], v37 offset0:32 offset1:33\n"
  "v_mfma_f32_16x16x32_bf16 v[20:23], a[44:47], a[104:107], v[20:23]\n"
  "s_waitcnt lgkmcnt(14)\n"
  "ds_read2_b32 a[26:27], v37 offset0:34 offset1:35\n"
  "v_mfma_f32_16x16x32_bf16 v[24:27], a[44:47], a[108:111], v[24:27]\n"
  "s_waitcnt lgkmcnt(14)\n"
  "ds_read2_b32 a[28:29], v37 offset0:160 offset1:161\n"
  "v_mfma_f32_16x16x32_bf16 v[28:31], a[44:47], a[112:115], v[28:31]\n"
  "s_waitcnt lgkmcnt(14)\n"
  "ds_read2_b32 a[30:31], v37 offset0:162 offset1:163\n"
  "v_mfma_f32_16x16x32_bf16 v[16:19], a[48:51], a[96:99], v[16:19]\n"
  "v_mfma_f32_16x16x32_bf16 v[20:23], a[48:51], a[100:103], v[20:23]\n"
  "v_mfma_f32_16x16x32_bf16 v[24:27], a[48:51], a[104:107], v[24:27]\n"
  "v_mfma_f32_16x16x32_bf16 v[28:31], a[48:51], a[108:111], v[28:31]\n"
  "v_mfma_f32_16x16x32_bf16 v[20:23], a[52:55], a[96:99], v[20:23]\n"
  "v_mfma_f32_16x16x32_bf16 v[24:27], a[52:55], a[100:103], v[24:27]\n"
  "v_mfma_f32_16x16x32_bf16 v[28:31], a[52:55], a[104:107], v[28:31]\n"
  "s_mov_b32 s47, 10240\n"
  "buffer_load_dwordx2 v[78:79], v86, s[52:55], s47 offen\n"
  "v_mfma_f32_16x16x32_bf16 v[24:27], a[56:59], a[96:99], v[24:27]\n"
  "v_mfma_f32_16x16x32_bf16 v[28:31], a[56:59], a[100:103], v[28:31]\n"
  "v_mfma_f32_16x16x32_bf16 v[28:31], a[60:63], a[96:99], v[28:31]\n"
  "v_mfma_f32_16x16x32_bf16 v[0:3], a[0:3], a[64:67], v[0:3]\n"
  "v_mfma_f32_16x16x32_bf16 v[4:7], a[0:3], a[68:71], v[4:7]\n"
  "v_mfma_f32_16x16x32_bf16 v[8:11], a[0:3], a[72:75], v[8:11]\n"
  "s_waitcnt lgkmcnt(14)\n"
  "ds_read2_b32 a[32:33], v34 offset0:48 offset1:49\n"
  "v_mfma_f32_16x16x32_bf16 v[12:15], a[0:3], a[76:79], v[12:15]\n"
  "s_waitcnt lgkmcnt(14)\n"
  "ds_read2_b32 a[34:35], v34 offset0:50 offset1:51\n"
  "v_mfma_f32_16x16x32_bf16 v[16:19], a[0:3], a[80:83], v[16:19]\n"
  "s_waitcnt lgkmcnt(14)\n"
  "ds_read_b128 a[96:99], v38 offset:3776\n"
  "v_mfma_f32_16x16x32_bf16 v[20:23], a[0:3], a[84:87], v[20:23]\n"
  "s_waitcnt lgkmcnt(14)\n"
  "ds_read_b128 a[100:103], v38 offset:3264\n"
  "v_mfma_f32_16x16x32_bf16 v[24:27], a[0:3], a[88:91], v[24:27]\n"
  "s_waitcnt lgkmcnt(14)\n"
  "ds_read_b128 a[104:107], v38 offset:2752\n"
  "v_mfma_f32_16x16x32_bf16 v[28:31], a[0:3], a[92:95], v[28:31]\n"
  "s_mov_b32 s47, 12288\n"
  "buffer_load_dwordx2 v[80:81], v86, s[52:55], s47 offen\n"
  "s_waitcnt lgkmcnt(14)\n"
  "ds_read_b128 a[108:111], v38 offset:2240\n"
  "v_mfma_f32_16x16x32_bf16 v[4:7], a[4:7], a[64:67], v[4:7]\n"
  "s_waitcnt lgkmcnt(14)\n"
  "ds_read_b128 a[112:115], v38 offset:1728\n"
  "v_mfma_f32_16x16x32_bf16 v[8:11], a[4:7], a[68:71], v[8:11]\n"
  "s_waitcnt lgkmcnt(14)\n"
  "ds_read_b128 a[116:119], v38 offset:1216\n"
  "v_mfma_f32_16x16x32_bf16 v[12:15], a[4:7], a[72:75], v[12:15]\n"
  "s_waitcnt lgkmcnt(14)\n"
  "ds_read_b128 a[120:123], v38 offset:704\n"
  "v_mfma_f32_16x16x32_bf16 v[16:19], a[4:7], a[76:79], v[16:19]\n"
  "s_waitcnt lgkmcnt(14)\n"
  "ds_read_b128 a[124:127], v38 offset:192\n"
  "v_mfma_f32_16x16x32_bf16 v[20:23], a[4:7], a[80:83], v[20:23]\n"
  "s_waitcnt lgkmcnt(14)\n"
  "ds_read2_b32 a[36:37], v34 offset0:176 offset1:177\n"
  "v_mfma_f32_16x16x32_bf16 v[24:27], a[4:7], a[84:87], v[24:27]\n"
  "s_waitcnt lgkmcnt(14)\n"
  "ds_read2_b32 a[38:39], v34 offset0:178 offset1:179\n"
  "v_mfma_f32_16x16x32_bf16 v[28:31], a[4:7], a[88:91], v[28:31]\n"
  "s_waitcnt lgkmcnt(14)\n"
  "ds_read2_b32 a[40:41], v35 offset0:48 offset1:49\n"
  "v_mfma_f32_16x16x32_bf16 v[8:11], a[8:11], a[64:67], v[8:11]\n"
  "s_waitcnt lgkmcnt(14)\n"
  "ds_read2_b32 a[42:43], v35 offset0:50 offset1:51\n"
  "v_mfma_f32_16x16x32_bf16 v[12:15], a[8:11], a[68:71], v[12:15]\n"
  "s_waitcnt lgkmcnt(14)\n"
  "ds_read2_b32 a[44:45], v35 offset0:176 offset1:177\n"
  "v_mfma_f32_16x16x32_bf16 v[16:19], a[8:11], a[72:75], v[16:19]\n"
  "s_waitcnt lgkmcnt(14)\n"
  "ds_read2_b32 a[46:47], v35 offset0:178 offset1:179\n"
  "v_mfma_f32_16x16x32_bf16 v[20:23], a[8:11], a[76:79], v[20:23]\n"
  "s_mov_b32 s47, 14336\n"
  "buffer_load_dwordx2 v[82:83], v86, s[52:55], s47 offen\n"
  "s_waitcnt lgkmcnt(14)\n"
  "ds_read2_b32 a[48:49], v36 offset0:48 offset1:49\n"
  "v_mfma_f32_16x16x32_bf16 v[24:27], a[8:11], a[80:83], v[24:27]\n"
  "s_waitcnt lgkmcnt(14)\n"
  "ds_read2_b32 a[50:51], v36 offset0:50 offset1:51\n"
  "v_mfma_f32_16x16x32_bf16 v[28:31], a[8:11], a[84:87], v[28:31]\n"
  "s_waitcnt lgkmcnt(14)\n"
  "ds_read2_b32 a[52:53], v36 offset0:176 offset1:177\n"
  "v_mfma_f32_16x16x32_bf16 v[12:15], a[12:15], a[64:67], v[12:15]\n"
  "s_waitcnt lgkmcnt(14)\n"
  "ds_read2_b32 a[54:55], v36 offset0:178 offset1:179\n"
  "v_mfma_f32_16x16x32_bf16 v[16:19], a[12:15], a[68:71], v[16:19]\n"
  "s_waitcnt lgkmcnt(14)\n"
  "ds_read2_b32 a[56:57], v37 offset0:48 offset1:49\n"
  "v_mfma_f32_16x16x32_bf16 v[20:23], a[12:15], a[72:75], v[20:23]\n"
  "s_waitcnt lgkmcnt(14)\n"
  "ds_read2_b32 a[58:59], v37 offset0:50 offset1:51\n"
  "v_mfma_f32_16x16x32_bf16 v[24:27], a[12:15], a[76:79], v[24:27]\n"
  "s_waitcnt lgkmcnt(14)\n"
  "ds_read2_b32 a[60:61], v37 offset0:176 offset1:177\n"
  "v_mfma_f32_16x16x32_bf16 v[28:31], a[12:15], a[80:83], v[28:31]\n"
  "s_waitcnt lgkmcnt(14)\n"
  "ds_read2_b32 a[62:63], v37 offset0:178 offset1:179\n"
  "v_mfma_f32_16x16x32_bf16 v[16:19], a[16:19], a[64:67], v[16:19]\n"
  "v_mfma_f32_16x16x32_bf16 v[20:23], a[16:19], a[68:71], v[20:23]\n"
  "v_mfma_f32_16x16x32_bf16 v[24:27], a[16:19], a[72:75], v[24:27]\n"
  "v_mfma_f32_16x16x32_bf16 v[28:31], a[16:19], a[76:79], v[28:31]\n"
  "buffer_load_dwordx2 v[84:85], v88, s[64:67], 0 offen\n"
  "v_mfma_f32_16x16x32_bf16 v[20:23], a[20:23], a[64:67], v[20:23]\n"
  "v_mfma_f32_16x16x32_bf16 v[24:27], a[20:23], a[68:71], v[24:27]\n"
  "v_mfma_f32_16x16x32_bf16 v[28:31], a[20:23], a[72:75], v[28:31]\n"
  "v_mfma_f32_16x16x32_bf16 v[24:27], a[24:27], a[64:67], v[24:27]\n"
  "v_mfma_f32_16x16x32_bf16 v[28:31], a[24:27], a[68:71], v[28:31]\n"
  "v_mfma_f32_16x16x32_bf16 v[28:31], a[28:31], a[64:67], v[28:31]\n"
  "v_mfma_f32_16x16x32_bf16 v[0:3], a[32:35], a[96:99], v[0:3]\n"
  "v_mfma_f32_16x16x32_bf16 v[4:7], a[32:35], a[100:103], v[4:7]\n"
  "v_mfma_f32_16x16x32_bf16 v[8:11], a[32:35], a[104:107], v[8:11]\n"
  "s_waitcnt lgkmcnt(14)\n"
  "ds_read2_b32 a[0:1], v34 offset0:64 offset1:65\n"
  "v_mfma_f32_16x16x32_bf16 v[12:15], a[32:35], a[108:111], v[12:15]\n"
  "s_waitcnt lgkmcnt(14)\n"
  "ds_read2_b32 a[2:3], v34 offset0:66 offset1:67\n"
  "v_mfma_f32_16x16x32_bf16 v[16:19], a[32:35], a[112:115], v[16:19]\n"
  "s_mov_b32 s47, 0\n"
  "s_add_u32 m0, s46, 0\n"
  "s_nop 0\n"
  "buffer_load_dwordx4 v87, s[56:59], s47 offen lds\n"
  "s_waitcnt lgkmcnt(14)\n"
  "ds_read_b128 a[64:67], v38 offset:3840\n"
  "v_mfma_f32_16x16x32_bf16 v[20:23], a[32:35], a[116:119], v[20:23]\n"
  "s_waitcnt lgkmcnt(14)\n"
  "ds_read_b128 a[68:71], v38 offset:3328\n"
  "v_mfma_f32_16x16x32_bf16 v[24:27], a[32:35], a[120:123], v[24:27]\n"
  "s_waitcnt lgkmcnt(14)\n"
  "ds_read_b128 a[72:75], v38 offset:2816\n"
  "v_mfma_f32_16x16x32_bf16 v[28:31], a[32:35], a[124:127], v[28:31]\n"
  "s_waitcnt lgkmcnt(14)\n"
  "ds_read_b128 a[76:79], v38 offset:2304\n"
  "v_mfma_f32_16x16x32_bf16 v[4:7], a[36:39], a[96:99], v[4:7]\n"
  "s_waitcnt lgkmcnt(14)\n"
  "ds_read_b128 a[80:83], v38 offset:1792\n"
  "v_mfma_f32_16x16x32_bf16 v[8:11], a[36:39], a[100:103], v[8:11]\n"
  "s_waitcnt lgkmcnt(14)\n"
  "ds_read_b128 a[84:87], v38 offset:1280\n"
  "v_mfma_f32_16x16x32_bf16 v[12:15], a[36:39], a[104:107], v[12:15]\n"
  "s_waitcnt lgkmcnt(14)\n"
  "ds_read_b128 a[88:91], v38 offset:768\n"
  "v_mfma_f32_16x16x32_bf16 v[16:19], a[36:39], a[108:111], v[16:19]\n"
  "s_waitcnt lgkmcnt(14)\n"
  "ds_read_b128 a[92:95], v38 offset:256\n"
  "v_mfma_f32_16x16x32_bf16 v[20:23], a[36:39], a[112:115], v[20:23]\n"
  "s_waitcnt lgkmcnt(14)\n"
  "ds_read2_b32 a[4:5], v34 offset0:192 offset1:193\n"
  "v_mfma_f32_16x16x32_bf16 v[24:27], a[36:39], a[116:119], v[24:27]\n"
  "s_waitcnt lgkmcnt(14)\n"
  "ds_read2_b32 a[6:7], v34 offset0:194 offset1:195\n"
  "v_mfma_f32_16x16x32_bf16 v[28:31], a[36:39], a[120:123], v[28:31]\n"
  "s_waitcnt lgkmcnt(14)\n"
  "ds_read2_b32 a[8:9], v35 offset0:64 offset1:65\n"
  "v_mfma_f32_16x16x32_bf16 v[8:11], a[40:43], a[96:99], v[8:11]\n"
  "s_mov_b32 s47, 98304\n"
  "s_add_u32 m0, s46, 1024\n"
  "s_nop 0\n"
  "buffer_load_dwordx4 v87, s[56:59], s47 offen lds\n"
  "s_waitcnt lgkmcnt(14)\n"
  "ds_read2_b32 a[10:11], v35 offset0:66 offset1:67\n"
  "v_mfma_f32_16x16x32_bf16 v[12:15], a[40:43], a[100:103], v[12:15]\n"
  "s_waitcnt lgkmcnt(14)\n"
  "ds_read2_b32 a[12:13], v35 offset0:192 offset1:193\n"
  "v_mfma_f32_16x16x32_bf16 v[16:19], a[40:43], a[104:107], v[16:19]\n"
  "s_waitcnt lgkmcnt(14)\n"
  "ds_read2_b32 a[14:15], v35 offset0:194 offset1:195\n"
  "v_mfma_f32_16x16x32_bf16 v[20:23], a[40:43], a[108:111], v[20:23]\n"
  "s_waitcnt lgkmcnt(14)\n"
  "ds_read2_b32 a[16:17], v36 offset0:64 offset1:65\n"
  "v_mfma_f32_16x16x32_bf16 v[24:27], a[40:43], a[112:115], v[24:27]\n"
  "s_waitcnt lgkmcnt(14)\n"
  "ds_read2_b32 a[18:19], v36 offset0:66 offset1:67\n"
  "v_mfma_f32_16x16x32_bf16 v[28:31], a[40:43], a[116:119], v[28:31]\n"
  "s_waitcnt lgkmcnt(14)\n"
  "ds_read2_b32 a[20:21], v36 offset0:192 offset1:193\n"
  "v_mfma_f32_16x16x32_bf16 v[12:15], a[44:47], a[96:99], v[12:15]\n"
  "s_waitcnt lgkmcnt(14)\n"
  "ds_read2_b32 a[22:23], v36 offset0:194 offset1:195\n"
  "v_mfma_f32_16x16x32_bf16 v[16:19], a[44:47], a[100:103], v[16:19]\n"
  "s_waitcnt lgkmcnt(14)\n"
  "ds_read2_b32 a[24:25], v37 offset0:64 offset1:65\n"
  "v_mfma_f32_16x16x32_bf16 v[20:23], a[44:47], a[104:107], v[20:23]\n"
  "s_waitcnt lgkmcnt(14)\n"
  "ds_read2_b32 a[26:27], v37 offset0:66 offset1:67\n"
  "v_mfma_f32_16x16x32_bf16 v[24:27], a[44:47], a[108:111], v[24:27]\n"
  "s_waitcnt lgkmcnt(14)\n"
  "ds_read2_b32 a[28:29], v37 offset0:192 offset1:193\n"
  "v_mfma_f32_16x16x32_bf16 v[28:31], a[44:47], a[112:115], v[28:31]\n"
  "s_waitcnt lgkmcnt(14)\n"
  "ds_read2_b32 a[30:31], v37 offset0:194 offset1:195\n"
  "v_mfma_f32_16x16x32_bf16 v[16:19], a[48:51], a[96:99], v[16:19]\n"
  "s_mov_b32 s47, 196608\n"
  "s_add_u32 m0, s46, 2048\n"
  "s_nop 0\n"
  "buffer_load_dwordx4 v87, s[56:59], s47 offen lds\n"
  "v_mfma_f32_16x16x32_bf16 v[20:23], a[48:51], a[100:103], v[20:23]\n"
  "v_mfma_f32_16x16x32_bf16 v[24:27], a[48:51], a[104:107], v[24:27]\n"
  "v_mfma_f32_16x16x32_bf16 v[28:31], a[48:51], a[108:111], v[28:31]\n"
  "v_mfma_f32_16x16x32_bf16 v[20:23], a[52:55], a[96:99], v[20:23]\n"
  "v_mfma_f32_16x16x32_bf16 v[24:27], a[52:55], a[100:103], v[24:27]\n"
  "v_mfma_f32_16x16x32_bf16 v[28:31], a[52:55], a[104:107], v[28:31]\n"
  "v_mfma_f32_16x16x32_bf16 v[24:27], a[56:59], a[96:99], v[24:27]\n"
  "v_mfma_f32_16x16x32_bf16 v[28:31], a[56:59], a[100:103], v[28:31]\n"
  "v_mfma_f32_16x16x32_bf16 v[28:31], a[60:63], a[96:99], v[28:31]\n"
  "v_mfma_f32_16x16x32_bf16 v[0:3], a[0:3], a[64:67], v[0:3]\n"
  "v_mfma_f32_16x16x32_bf16 v[4:7], a[0:3], a[68:71], v[4:7]\n"
  "s_mov_b32 s47, 294912\n"
  "s_add_u32 m0, s46, 3072\n"
  "s_nop 0\n"
  "buffer_load_dwordx4 v87, s[56:59], s47 offen lds\n"
  "v_mfma_f32_16x16x32_bf16 v[8:11], a[0:3], a[72:75], v[8:11]\n"
  "s_waitcnt lgkmcnt(14)\n"
  "ds_read2_b32 a[32:33], v34 offset0:80 offset1:81\n"
  "v_mfma_f32_16x16x32_bf16 v[12:15], a[0:3], a[76:79], v[12:15]\n"
  "s_waitcnt lgkmcnt(14)\n"
  "ds_read2_b32 a[34:35], v34 offset0:82 offset1:83\n"
  "v_mfma_f32_16x16x32_bf16 v[16:19], a[0:3], a[80:83], v[16:19]\n"
  "s_waitcnt lgkmcnt(14)\n"
  "ds_read_b128 a[96:99], v38 offset:3904\n"
  "v_mfma_f32_16x16x32_bf16 v[20:23], a[0:3], a[84:87], v[20:23]\n"
  "s_waitcnt lgkmcnt(14)\n"
  "ds_read_b128 a[100:103], v38 offset:3392\n"
  "v_mfma_f32_16x16x32_bf16 v[24:27], a[0:3], a[88:91], v[24:27]\n"
  "s_waitcnt lgkmcnt(14)\n"
  "ds_read_b128 a[104:107], v38 offset:2880\n"
  "v_mfma_f32_16x16x32_bf16 v[28:31], a[0:3], a[92:95], v[28:31]\n"
  "s_waitcnt lgkmcnt(14)\n"
  "ds_read_b128 a[108:111], v38 offset:2368\n"
  "v_mfma_f32_16x16x32_bf16 v[4:7], a[4:7], a[64:67], v[4:7]\n"
  "s_waitcnt lgkmcnt(14)\n"
  "ds_read_b128 a[112:115], v38 offset:1856\n"
  "v_mfma_f32_16x16x32_bf16 v[8:11], a[4:7], a[68:71], v[8:11]\n"
  "s_waitcnt lgkmcnt(14)\n"
  "ds_read_b128 a[116:119], v38 offset:1344\n"
  "v_mfma_f32_16x16x32_bf16 v[12:15], a[4:7], a[72:75], v[12:15]\n"
  "s_waitcnt lgkmcnt(14)\n"
  "ds_read_b128 a[120:123], v38 offset:832\n"
  "v_mfma_f32_16x16x32_bf16 v[16:19], a[4:7], a[76:79], v[16:19]\n"
  "s_waitcnt lgkmcnt(14)\n"
  "ds_read_b128 a[124:127], v38 offset:320\n"
  "v_mfma_f32_16x16x32_bf16 v[20:23], a[4:7], a[80:83], v[20:23]\n"
  "s_mov_b32 s47, 393216\n"
  "s_add_u32 m0, s46, 4096\n"
  "s_nop 0\n"
  "buffer_load_dwordx4 v87, s[56:59], s47 offen lds\n"
  "s_waitcnt lgkmcnt(14)\n"
  "ds_read2_b32 a[36:37], v34 offset0:208 offset1:209\n"
  "v_mfma_f32_16x16x32_bf16 v[24:27], a[4:7], a[84:87], v[24:27]\n"
  "s_waitcnt lgkmcnt(14)\n"
  "ds_read2_b32 a[38:39], v34 offset0:210 offset1:211\n"
  "v_mfma_f32_16x16x32_bf16 v[28:31], a[4:7], a[88:91], v[28:31]\n"
  "s_waitcnt lgkmcnt(14)\n"
  "ds_read2_b32 a[40:41], v35 offset0:80 offset1:81\n"
  "v_mfma_f32_16x16x32_bf16 v[8:11], a[8:11], a[64:67], v[8:11]\n"
  "s_waitcnt lgkmcnt(14)\n"
  "ds_read2_b32 a[42:43], v35 offset0:82 offset1:83\n"
  "v_mfma_f32_16x16x32_bf16 v[12:15], a[8:11], a[68:71], v[12:15]\n"
  "s_waitcnt lgkmcnt(14)\n"
  "ds_read2_b32 a[44:45], v35 offset0:208 offset1:209\n"
  "v_mfma_f32_16x16x32_bf16 v[16:19], a[8:11], a[72:75], v[16:19]\n"
  "s_waitcnt lgkmcnt(14)\n"
  "ds_read2_b32 a[46:47], v35 offset0:210 offset1:211\n"
  "v_mfma_f32_16x16x32_bf16 v[20:23], a[8:11], a[76:79], v[20:23]\n"
  "s_waitcnt lgkmcnt(14)\n"
  "ds_read2_b32 a[48:49], v36 offset0:80 offset1:81\n"
  "v_mfma_f32_16x16x32_bf16 v[24:27], a[8:11], a[80:83], v[24:27]\n"
  "s_waitcnt lgkmcnt(14)\n"
  "ds_read2_b32 a[50:51], v36 offset0:82 offset1:83\n"
  "v_mfma_f32_16x16x32_bf16 v[28:31], a[8:11], a[84:87], v[28:31]\n"
  "s_waitcnt lgkmcnt(14)\n"
  "ds_read2_b32 a[52:53], v36 offset0:208 offset1:209\n"
  "v_mfma_f32_16x16x32_bf16 v[12:15], a[12:15], a[64:67], v[12:15]\n"
  "s_waitcnt lgkmcnt(14)\n"
  "ds_read2_b32 a[54:55], v36 offset0:210 offset1:211\n"
  "v_mfma_f32_16x16x32_bf16 v[16:19], a[12:15], a[68:71], v[16:19]\n"
  "s_waitcnt lgkmcnt(14)\n"
  "ds_read2_b32 a[56:57], v37 offset0:80 offset1:81\n"
  "v_mfma_f32_16x16x32_bf16 v[20:23], a[12:15], a[72:75], v[20:23]\n"
  "s_mov_b32 s47, 491520\n"
  "s_add_u32 m0, s46, 5120\n"
  "s_nop 0\n"
  "buffer_load_dwordx4 v87, s[56:59], s47 offen lds\n"
  "s_waitcnt lgkmcnt(14)\n"
  "ds_read2_b32 a[58:59], v37 offset0:82 offset1:83\n"
  "v_mfma_f32_16x16x32_bf16 v[24:27], a[12:15], a[76:79], v[24:27]\n"
  "s_waitcnt lgkmcnt(14)\n"
  "ds_read2_b32 a[60:61], v37 offset0:208 offset1:209\n"
  "v_mfma_f32_16x16x32_bf16 v[28:31], a[12:15], a[80:83], v[28:31]\n"
  "s_waitcnt lgkmcnt(14)\n"
  "ds_read2_b32 a[62:63], v37 offset0:210 offset1:211\n"
  "v_mfma_f32_16x16x32_bf16 v[16:19], a[16:19], a[64:67], v[16:19]\n"
  "v_mfma_f32_16x16x32_bf16 v[20:23], a[16:19], a[68:71], v[20:23]\n"
  "v_mfma_f32_16x16x32_bf16 v[24:27], a[16:19], a[72:75], v[24:27]\n"
  "v_mfma_f32_16x16x32_bf16 v[28:31], a[16:19], a[76:79], v[28:31]\n"
  "v_mfma_f32_16x16x32_bf16 v[20:23], a[20:23], a[64:67], v[20:23]\n"
  "v_mfma_f32_16x16x32_bf16 v[24:27], a[20:23], a[68:71], v[24:27]\n"
  "v_mfma_f32_16x16x32_bf16 v[28:31], a[20:23], a[72:75], v[28:31]\n"
  "v_mfma_f32_16x16x32_bf16 v[24:27], a[24:27], a[64:67], v[24:27]\n"
  "v_mfma_f32_16x16x32_bf16 v[28:31], a[24:27], a[68:71], v[28:31]\n"
  "s_mov_b32 s47, 589824\n"
  "s_add_u32 m0, s46, 6144\n"
  "s_nop 0\n"
  "buffer_load_dwordx4 v87, s[56:59], s47 offen lds\n"
  "v_mfma_f32_16x16x32_bf16 v[28:31], a[28:31], a[64:67], v[28:31]\n"
  "v_mfma_f32_16x16x32_bf16 v[0:3], a[32:35], a[96:99], v[0:3]\n"
  "v_mfma_f32_16x16x32_bf16 v[4:7], a[32:35], a[100:103], v[4:7]\n"
  "v_mfma_f32_16x16x32_bf16 v[8:11], a[32:35], a[104:107], v[8:11]\n"
  "s_waitcnt lgkmcnt(14)\n"
  "ds_read2_b32 a[0:1], v34 offset0:96 offset1:97\n"
  "v_mfma_f32_16x16x32_bf16 v[12:15], a[32:35], a[108:111], v[12:15]\n"
  "s_waitcnt lgkmcnt(14)\n"
  "ds_read2_b32 a[2:3], v34 offset0:98 offset1:99\n"
  "v_mfma_f32_16x16x32_bf16 v[16:19], a[32:35], a[112:115], v[16:19]\n"
  "s_waitcnt lgkmcnt(14)\n"
  "ds_read_b128 a[64:67], v38 offset:3968\n"
  "v_mfma_f32_16x16x32_bf16 v[20:23], a[32:35], a[116:119], v[20:23]\n"
  "s_waitcnt lgkmcnt(14)\n"
  "ds_read_b128 a[68:71], v38 offset:3456\n"
  "v_mfma_f32_16x16x32_bf16 v[24:27], a[32:35], a[120:123], v[24:27]\n"
  "s_waitcnt lgkmcnt(14)\n"
  "ds_read_b128 a[72:75], v38 offset:2944\n"
  "v_mfma_f32_16x16x32_bf16 v[28:31], a[32:35], a[124:127], v[28:31]\n"
  "s_waitcnt lgkmcnt(14)\n"
  "ds_read_b128 a[76:79], v38 offset:2432\n"
  "v_mfma_f32_16x16x32_bf16 v[4:7], a[36:39], a[96:99], v[4:7]\n"
  "s_waitcnt lgkmcnt(14)\n"
  "ds_read_b128 a[80:83], v38 offset:1920\n"
  "v_mfma_f32_16x16x32_bf16 v[8:11], a[36:39], a[100:103], v[8:11]\n"
  "s_mov_b32 s47, 688128\n"
  "s_add_u32 m0, s46, 7168\n"
  "s_nop 0\n"
  "buffer_load_dwordx4 v87, s[56:59], s47 offen lds\n"
  "s_waitcnt lgkmcnt(14)\n"
  "ds_read_b128 a[84:87], v38 offset:1408\n"
  "v_mfma_f32_16x16x32_bf16 v[12:15], a[36:39], a[104:107], v[12:15]\n"
  "s_waitcnt lgkmcnt(14)\n"
  "ds_read_b128 a[88:91], v38 offset:896\n"
  "v_mfma_f32_16x16x32_bf16 v[16:19], a[36:39], a[108:111], v[16:19]\n"
  "s_waitcnt lgkmcnt(14)\n"
  "ds_read_b128 a[92:95], v38 offset:384\n"
  "v_mfma_f32_16x16x32_bf16 v[20:23], a[36:39], a[112:115], v[20:23]\n"
  "s_waitcnt lgkmcnt(14)\n"
  "ds_read2_b32 a[4:5], v34 offset0:224 offset1:225\n"
  "v_mfma_f32_16x16x32_bf16 v[24:27], a[36:39], a[116:119], v[24:27]\n"
  "s_waitcnt lgkmcnt(14)\n"
  "ds_read2_b32 a[6:7], v34 offset0:226 offset1:227\n"
  "v_mfma_f32_16x16x32_bf16 v[28:31], a[36:39], a[120:123], v[28:31]\n"
  "s_waitcnt lgkmcnt(14)\n"
  "ds_read2_b32 a[8:9], v35 offset0:96 offset1:97\n"
  "v_mfma_f32_16x16x32_bf16 v[8:11], a[40:43], a[96:99], v[8:11]\n"
  "s_waitcnt lgkmcnt(14)\n"
  "ds_read2_b32 a[10:11], v35 offset0:98 offset1:99\n"
  "v_mfma_f32_16x16x32_bf16 v[12:15], a[40:43], a[100:103], v[12:15]\n"
  "s_waitcnt lgkmcnt(14)\n"
  "ds_read2_b32 a[12:13], v35 offset0:224 offset1:225\n"
  "v_mfma_f32_16x16x32_bf16 v[16:19], a[40:43], a[104:107], v[16:19]\n"
  "s_waitcnt lgkmcnt(14)\n"
  "ds_read2_b32 a[14:15], v35 offset0:226 offset1:227\n"
  "v_mfma_f32_16x16x32_bf16 v[20:23], a[40:43], a[108:111], v[20:23]\n"
  "s_waitcnt lgkmcnt(14)\n"
  "ds_read2_b32 a[16:17], v36 offset0:96 offset1:97\n"
  "v_mfma_f32_16x16x32_bf16 v[24:27], a[40:43], a[112:115], v[24:27]\n"
  "s_waitcnt lgkmcnt(14)\n"
  "ds_read2_b32 a[18:19], v36 offset0:98 offset1:99\n"
  "v_mfma_f32_16x16x32_bf16 v[28:31], a[40:43], a[116:119], v[28:31]\n"
  "s_mov_b32 s47, 786432\n"
  "s_add_u32 m0, s46, 8192\n"
  "s_nop 0\n"
  "buffer_load_dwordx4 v87, s[56:59], s47 offen lds\n"
  "s_waitcnt lgkmcnt(14)\n"
  "ds_read2_b32 a[20:21], v36 offset0:224 offset1:225\n"
  "v_mfma_f32_16x16x32_bf16 v[12:15], a[44:47], a[96:99], v[12:15]\n"
  "s_waitcnt lgkmcnt(14)\n"
  "ds_read2_b32 a[22:23], v36 offset0:226 offset1:227\n"
  "v_mfma_f32_16x16x32_bf16 v[16:19], a[44:47], a[100:103], v[16:19]\n"
  "s_waitcnt lgkmcnt(14)\n"
  "ds_read2_b32 a[24:25], v37 offset0:96 offset1:97\n"
  "v_mfma_f32_16x16x32_bf16 v[20:23], a[44:47], a[104:107], v[20:23]\n"
  "s_waitcnt lgkmcnt(14)\n"
  "ds_read2_b32 a[26:27], v37 offset0:98 offset1:99\n"
  "v_mfma_f32_16x16x32_bf16 v[24:27], a[44:47], a[108:111], v[24:27]\n"
  "s_waitcnt lgkmcnt(14)\n"
  "ds_read2_b32 a[28:29], v37 offset0:224 offset1:225\n"
  "v_mfma_f32_16x16x32_bf16 v[28:31], a[44:47], a[112:115], v[28:31]\n"
  "s_waitcnt lgkmcnt(14)\n"
  "ds_read2_b32 a[30:31], v37 offset0:226 offset1:227\n"
  "v_mfma_f32_16x16x32_bf16 v[16:19], a[48:51], a[96:99], v[16:19]\n"
  "v_mfma_f32_16x16x32_bf16 v[20:23], a[48:51], a[100:103], v[20:23]\n"
  "v_mfma_f32_16x16x32_bf16 v[24:27], a[48:51], a[104:107], v[24:27]\n"
  "v_mfma_f32_16x16x32_bf16 v[28:31], a[48:51], a[108:111], v[28:31]\n"
  "v_mfma_f32_16x16x32_bf16 v[20:23], a[52:55], a[96:99], v[20:23]\n"
  "v_mfma_f32_16x16x32_bf16 v[24:27], a[52:55], a[100:103], v[24:27]\n"
  "s_mov_b32 s47, 884736\n"
  "s_add_u32 m0, s46, 9216\n"
  "s_nop 0\n"
  "buffer_load_dwordx4 v87, s[56:59], s47 offen lds\n"
  "v_mfma_f32_16x16x32_bf16 v[28:31], a[52:55], a[104:107], v[28:31]\n"
  "v_mfma_f32_16x16x32_bf16 v[24:27], a[56:59], a[96:99], v[24:27]\n"
  "v_mfma_f32_16x16x32_bf16 v[28:31], a[56:59], a[100:103], v[28:31]\n"
  "v_mfma_f32_16x16x32_bf16 v[28:31], a[60:63], a[96:99], v[28:31]\n"
  "v_mfma_f32_16x16x32_bf16 v[0:3], a[0:3], a[64:67], v[0:3]\n"
  "v_mfma_f32_16x16x32_bf16 v[4:7], a[0:3], a[68:71], v[4:7]\n"
  "v_mfma_f32_16x16x32_bf16 v[8:11], a[0:3], a[72:75], v[8:11]\n"
  "s_waitcnt lgkmcnt(14)\n"
  "ds_read2_b32 a[32:33], v34 offset0:112 offset1:113\n"
  "v_mfma_f32_16x16x32_bf16 v[12:15], a[0:3], a[76:79], v[12:15]\n"
  "s_waitcnt lgkmcnt(14)\n"
  "ds_read2_b32 a[34:35], v34 offset0:114 offset1:115\n"
  "v_mfma_f32_16x16x32_bf16 v[16:19], a[0:3], a[80:83], v[16:19]\n"
  "s_waitcnt lgkmcnt(14)\n"
  "ds_read_b128 a[96:99], v38 offset:4032\n"
  "v_mfma_f32_16x16x32_bf16 v[20:23], a[0:3], a[84:87], v[20:23]\n"
  "s_waitcnt lgkmcnt(14)\n"
  "ds_read_b128 a[100:103], v38 offset:3520\n"
  "v_mfma_f32_16x16x32_bf16 v[24:27], a[0:3], a[88:91], v[24:27]\n"
  "s_mov_b32 s47, 983040\n"
  "s_add_u32 m0, s46, 10240\n"
  "s_nop 0\n"
  "buffer_load_dwordx4 v87, s[56:59], s47 offen lds\n"
  "s_waitcnt lgkmcnt(14)\n"
  "ds_read_b128 a[104:107], v38 offset:3008\n"
  "v_mfma_f32_16x16x32_bf16 v[28:31], a[0:3], a[92:95], v[28:31]\n"
  "s_waitcnt lgkmcnt(14)\n"
  "ds_read_b128 a[108:111], v38 offset:2496\n"
  "v_mfma_f32_16x16x32_bf16 v[4:7], a[4:7], a[64:67], v[4:7]\n"
  "s_waitcnt lgkmcnt(14)\n"
  "ds_read_b128 a[112:115], v38 offset:1984\n"
  "v_mfma_f32_16x16x32_bf16 v[8:11], a[4:7], a[68:71], v[8:11]\n"
  "s_waitcnt lgkmcnt(14)\n"
  "ds_read_b128 a[116:119], v38 offset:1472\n"
  "v_mfma_f32_16x16x32_bf16 v[12:15], a[4:7], a[72:75], v[12:15]\n"
  "s_waitcnt lgkmcnt(14)\n"
  "ds_read_b128 a[120:123], v38 offset:960\n"
  "v_mfma_f32_16x16x32_bf16 v[16:19], a[4:7], a[76:79], v[16:19]\n"
  "s_waitcnt lgkmcnt(14)\n"
  "ds_read_b128 a[124:127], v38 offset:448\n"
  "v_mfma_f32_16x16x32_bf16 v[20:23], a[4:7], a[80:83], v[20:23]\n"
  "s_waitcnt lgkmcnt(14)\n"
  "ds_read2_b32 a[36:37], v34 offset0:240 offset1:241\n"
  "v_mfma_f32_16x16x32_bf16 v[24:27], a[4:7], a[84:87], v[24:27]\n"
  "s_waitcnt lgkmcnt(14)\n"
  "ds_read2_b32 a[38:39], v34 offset0:242 offset1:243\n"
  "v_mfma_f32_16x16x32_bf16 v[28:31], a[4:7], a[88:91], v[28:31]\n"
  "s_waitcnt lgkmcnt(14)\n"
  "ds_read2_b32 a[40:41], v35 offset0:112 offset1:113\n"
  "v_mfma_f32_16x16x32_bf16 v[8:11], a[8:11], a[64:67], v[8:11]\n"
  "s_waitcnt lgkmcnt(14)\n"
  "ds_read2_b32 a[42:43], v35 offset0:114 offset1:115\n"
  "v_mfma_f32_16x16x32_bf16 v[12:15], a[8:11], a[68:71], v[12:15]\n"
  "s_waitcnt lgkmcnt(14)\n"
  "ds_read2_b32 a[44:45], v35 offset0:240 offset1:241\n"
  "v_mfma_f32_16x16x32_bf16 v[16:19], a[8:11], a[72:75], v[16:19]\n"
  "s_mov_b32 s47, 1081344\n"
  "s_add_u32 m0, s46, 11264\n"
  "s_nop 0\n"
  "buffer_load_dwordx4 v87, s[56:59], s47 offen lds\n"
  "s_waitcnt lgkmcnt(14)\n"
  "ds_read2_b32 a[46:47], v35 offset0:242 offset1:243\n"
  "v_mfma_f32_16x16x32_bf16 v[20:23], a[8:11], a[76:79], v[20:23]\n"
  "s_waitcnt lgkmcnt(14)\n"
  "ds_read2_b32 a[48:49], v36 offset0:112 offset1:113\n"
  "v_mfma_f32_16x16x32_bf16 v[24:27], a[8:11], a[80:83], v[24:27]\n"
  "s_waitcnt lgkmcnt(14)\n"
  "ds_read2_b32 a[50:51], v36 offset0:114 offset1:115\n"
  "v_mfma_f32_16x16x32_bf16 v[28:31], a[8:11], a[84:87], v[28:31]\n"
  "s_waitcnt lgkmcnt(14)\n"
  "ds_read2_b32 a[52:53], v36 offset0:240 offset1:241\n"
  "v_mfma_f32_16x16x32_bf16 v[12:15], a[12:15], a[64:67], v[12:15]\n"
  "s_waitcnt lgkmcnt(14)\n"
  "ds_read2_b32 a[54:55], v36 offset0:242 offset1:243\n"
  "v_mfma_f32_16x16x32_bf16 v[16:19], a[12:15], a[68:71], v[16:19]\n"
  "s_waitcnt lgkmcnt(14)\n"
  "ds_read2_b32 a[56:57], v37 offset0:112 offset1:113\n"
  "v_mfma_f32_16x16x32_bf16 v[20:23], a[12:15], a[72:75], v[20:23]\n"
  "s_waitcnt lgkmcnt(14)\n"
  "ds_read2_b32 a[58:59], v37 offset0:114 offset1:115\n"
  "v_mfma_f32_16x16x32_bf16 v[24:27], a[12:15], a[76:79], v[24:27]\n"
  "s_waitcnt lgkmcnt(14)\n"
  "ds_read2_b32 a[60:61], v37 offset0:240 offset1:241\n"
  "v_mfma_f32_16x16x32_bf16 v[28:31], a[12:15], a[80:83], v[28:31]\n"
  "s_waitcnt lgkmcnt(14)\n"
  "ds_read2_b32 a[62:63], v37 offset0:242 offset1:243\n"
  "v_mfma_f32_16x16x32_bf16 v[16:19], a[16:19], a[64:67], v[16:19]\n"
  "v_mfma_f32_16x16x32_bf16 v[20:23], a[16:19], a[68:71], v[20:23]\n"
  "v_mfma_f32_16x16x32_bf16 v[24:27], a[16:19], a[72:75], v[24:27]\n"
  "s_mov_b32 s47, 1179648\n"
  "s_add_u32 m0, s46, 12288\n"
  "s_nop 0\n"
  "buffer_load_dwordx4 v87, s[56:59], s47 offen lds\n"
  "v_mfma_f32_16x16x32_bf16 v[28:31], a[16:19], a[76:79], v[28:31]\n"
  "v_mfma_f32_16x16x32_bf16 v[20:23], a[20:23], a[64:67], v[20:23]\n"
  "v_mfma_f32_16x16x32_bf16 v[24:27], a[20:23], a[68:71], v[24:27]\n"
  "v_mfma_f32_16x16x32_bf16 v[28:31], a[20:23], a[72:75], v[28:31]\n"
  "v_mfma_f32_16x16x32_bf16 v[24:27], a[24:27], a[64:67], v[24:27]\n"
  "v_mfma_f32_16x16x32_bf16 v[28:31], a[24:27], a[68:71], v[28:31]\n"
  "v_mfma_f32_16x16x32_bf16 v[28:31], a[28:31], a[64:67], v[28:31]\n"
  "v_mfma_f32_16x16x32_bf16 v[0:3], a[32:35], a[96:99], v[0:3]\n"
  "v_mfma_f32_16x16x32_bf16 v[4:7], a[32:35], a[100:103], v[4:7]\n"
  "v_mfma_f32_16x16x32_bf16 v[8:11], a[32:35], a[104:107], v[8:11]\n"
  "v_mfma_f32_16x16x32_bf16 v[12:15], a[32:35], a[108:111], v[12:15]\n"
  "s_mov_b32 s47, 1277952\n"
  "s_add_u32 m0, s46, 13312\n"
  "s_nop 0\n"
  "buffer_load_dwordx4 v87, s[56:59], s47 offen lds\n"
  "v_mfma_f32_16x16x32_bf16 v[16:19], a[32:35], a[112:115], v[16:19]\n"
  "v_mfma_f32_16x16x32_bf16 v[20:23], a[32:35], a[116:119], v[20:23]\n"
  "v_mfma_f32_16x16x32_bf16 v[24:27], a[32:35], a[120:123], v[24:27]\n"
  "s_waitcnt lgkmcnt(14)\n"
  "v_mfma_f32_16x16x32_bf16 v[28:31], a[32:35], a[124:127], v[28:31]\n"
  "s_waitcnt lgkmcnt(12)\n"
  "v_mfma_f32_16x16x32_bf16 v[4:7], a[36:39], a[96:99], v[4:7]\n"
  "v_mfma_f32_16x16x32_bf16 v[8:11], a[36:39], a[100:103], v[8:11]\n"
  "v_mfma_f32_16x16x32_bf16 v[12:15], a[36:39], a[104:107], v[12:15]\n"
  "v_mfma_f32_16x16x32_bf16 v[16:19], a[36:39], a[108:111], v[16:19]\n"
  "v_mfma_f32_16x16x32_bf16 v[20:23], a[36:39], a[112:115], v[20:23]\n"
  "v_mfma_f32_16x16x32_bf16 v[24:27], a[36:39], a[116:119], v[24:27]\n"
  "v_mfma_f32_16x16x32_bf16 v[28:31], a[36:39], a[120:123], v[28:31]\n"
  "s_mov_b32 s47, 1376256\n"
  "s_add_u32 m0, s46, 14336\n"
  "s_nop 0\n"
  "buffer_load_dwordx4 v87, s[56:59], s47 offen lds\n"
  "s_waitcnt lgkmcnt(10)\n"
  "v_mfma_f32_16x16x32_bf16 v[8:11], a[40:43], a[96:99], v[8:11]\n"
  "v_mfma_f32_16x16x32_bf16 v[12:15], a[40:43], a[100:103], v[12:15]\n"
  "v_mfma_f32_16x16x32_bf16 v[16:19], a[40:43], a[104:107], v[16:19]\n"
  "v_mfma_f32_16x16x32_bf16 v[20:23], a[40:43], a[108:111], v[20:23]\n"
  "v_mfma_f32_16x16x32_bf16 v[24:27], a[40:43], a[112:115], v[24:27]\n"
  "v_mfma_f32_16x16x32_bf16 v[28:31], a[40:43], a[116:119], v[28:31]\n"
  "s_waitcnt lgkmcnt(8)\n"
  "v_mfma_f32_16x16x32_bf16 v[12:15], a[44:47], a[96:99], v[12:15]\n"
  "v_mfma_f32_16x16x32_bf16 v[16:19], a[44:47], a[100:103], v[16:19]\n"
  "v_mfma_f32_16x16x32_bf16 v[20:23], a[44:47], a[104:107], v[20:23]\n"
  "v_mfma_f32_16x16x32_bf16 v[24:27], a[44:47], a[108:111], v[24:27]\n"
  "v_mfma_f32_16x16x32_bf16 v[28:31], a[44:47], a[112:115], v[28:31]\n"
  "s_mov_b32 s47, 1474560\n"
  "s_add_u32 m0, s46, 15360\n"
  "s_nop 0\n"
  "buffer_load_dwordx4 v87, s[56:59], s47 offen lds\n"
  "s_waitcnt lgkmcnt(6)\n"
  "v_mfma_f32_16x16x32_bf16 v[16:19], a[48:51], a[96:99], v[16:19]\n"
  "v_mfma_f32_16x16x32_bf16 v[20:23], a[48:51], a[100:103], v[20:23]\n"
  "v_mfma_f32_16x16x32_bf16 v[24:27], a[48:51], a[104:107], v[24:27]\n"
  "v_mfma_f32_16x16x32_bf16 v[28:31], a[48:51], a[108:111], v[28:31]\n"
  "s_waitcnt lgkmcnt(4)\n"
  "v_mfma_f32_16x16x32_bf16 v[20:23], a[52:55], a[96:99], v[20:23]\n"
  "v_mfma_f32_16x16x32_bf16 v[24:27], a[52:55], a[100:103], v[24:27]\n"
  "v_mfma_f32_16x16x32_bf16 v[28:31], a[52:55], a[104:107], v[28:31]\n"
  "s_waitcnt lgkmcnt(2)\n"
  "v_mfma_f32_16x16x32_bf16 v[24:27], a[56:59], a[96:99], v[24:27]\n"
  "v_mfma_f32_16x16x32_bf16 v[28:31], a[56:59], a[100:103], v[28:31]\n"
  "s_waitcnt lgkmcnt(0)\n"
  "v_mfma_f32_16x16x32_bf16 v[28:31], a[60:63], a[96:99], v[28:31]\n"
  "ds_read_b64 a[0:1], v39 offset:3584\n"
  "ds_read_b64 a[2:3], v39 offset:3072\n"
  "ds_read_b64 a[4:5], v39 offset:2560\n"
  "ds_read_b64 a[6:7], v39 offset:2048\n"
  "ds_read_b64 a[8:9], v39 offset:1536\n"
  "ds_read_b64 a[10:11], v39 offset:1024\n"
  "ds_read_b64 a[12:13], v39 offset:512\n"
  "ds_read_b64 a[14:15], v39 offset:0\n"
  "s_waitcnt lgkmcnt(0)\n"
  "s_nop 4\n"
  "v_accvgpr_read_b32 v52, a0\n"
  "v_accvgpr_read_b32 v53, a1\n"
  "v_and_b32 v48, 0xffff0000, v53\n"
  "v_lshlrev_b32 v49, 16, v53\n"
  "v_and_b32 v50, 0xffff0000, v52\n"
  "v_lshlrev_b32 v51, 16, v52\n"
  "v_fmac_f32 v0, s22, v48\n"
  "v_fmac_f32 v1, s22, v49\n"
  "v_fmac_f32 v2, s22, v50\n"
  "v_fmac_f32 v3, s22, v51\n"
  "v_cvt_pk_bf16_f32 v0, v0, v1\n"
  "v_cvt_pk_bf16_f32 v1, v2, v3\n"
  "ds_write_b64 v40, v[0:1] offset:0\n"
  "v_accvgpr_read_b32 v60, a2\n"
  "v_accvgpr_read_b32 v61, a3\n"
  "v_and_b32 v56, 0xffff0000, v61\n"
  "v_lshlrev_b32 v57, 16, v61\n"
  "v_and_b32 v58, 0xffff0000, v60\n"
  "v_lshlrev_b32 v59, 16, v60\n"
  "v_fmac_f32 v4, s22, v56\n"
  "v_fmac_f32 v5, s22, v57\n"
  "v_fmac_f32 v6, s22, v58\n"
  "v_fmac_f32 v7, s22, v59\n"
  "v_cvt_pk_bf16_f32 v4, v4, v5\n"
  "v_cvt_pk_bf16_f32 v5, v6, v7\n"
  "ds_write_b64 v40, v[4:5] offset:640\n"
  "v_accvgpr_read_b32 v52, a4\n"
  "v_accvgpr_read_b32 v53, a5\n"
  "v_and_b32 v48, 0xffff0000, v53\n"
  "v_lshlrev_b32 v49, 16, v53\n"
  "v_and_b32 v50, 0xffff0000, v52\n"
  "v_lshlrev_b32 v51, 16, v52\n"
  "v_fmac_f32 v8, s22, v48\n"
  "v_fmac_f32 v9, s22, v49\n"
  "v_fmac_f32 v10, s22, v50\n"
  "v_fmac_f32 v11, s22, v51\n"
  "v_cvt_pk_bf16_f32 v8, v8, v9\n"
  "v_cvt_pk_bf16_f32 v9, v10, v11\n"
  "ds_write_b64 v40, v[8:9] offset:1280\n"
  "v_accvgpr_read_b32 v60, a6\n"
  "v_accvgpr_read_b32 v61, a7\n"
  "v_and_b32 v56, 0xffff0000, v61\n"
  "v_lshlrev_b32 v57, 16, v61\n"
  "v_and_b32 v58, 0xffff0000, v60\n"
  "v_lshlrev_b32 v59, 16, v60\n"
  "v_fmac_f32 v12, s22, v56\n"
  "v_fmac_f32 v13, s22, v57\n"
  "v_fmac_f32 v14, s22, v58\n"
  "v_fmac_f32 v15, s22, v59\n"
  "v_cvt_pk_bf16_f32 v12, v12, v13\n"
  "v_cvt_pk_bf16_f32 v13, v14, v15\n"
  "ds_write_b64 v40, v[12:13] offset:1920\n"
  "v_accvgpr_read_b32 v52, a8\n"
  "v_accvgpr_read_b32 v53, a9\n"
  "v_and_b32 v48, 0xffff0000, v53\n"
  "v_lshlrev_b32 v49, 16, v53\n"
  "v_and_b32 v50, 0xffff0000, v52\n"
  "v_lshlrev_b32 v51, 16, v52\n"
  "v_fmac_f32 v16, s22, v48\n"
  "v_fmac_f32 v17, s22, v49\n"
  "v_fmac_f32 v18, s22, v50\n"
  "v_fmac_f32 v19, s22, v51\n"
  "v_cvt_pk_bf16_f32 v16, v16, v17\n"
  "v_cvt_pk_bf16_f32 v17, v18, v19\n"
  "ds_write_b64 v40, v[16:17] offset:2560\n"
  "v_accvgpr_read_b32 v60, a10\n"
  "v_accvgpr_read_b32 v61, a11\n"
  "v_and_b32 v56, 0xffff0000, v61\n"
  "v_lshlrev_b32 v57, 16, v61\n"
  "v_and_b32 v58, 0xffff0000, v60\n"
  "v_lshlrev_b32 v59, 16, v60\n"
  "v_fmac_f32 v20, s22, v56\n"
  "v_fmac_f32 v21, s22, v57\n"
  "v_fmac_f32 v22, s22, v58\n"
  "v_fmac_f32 v23, s22, v59\n"
  "v_cvt_pk_bf16_f32 v20, v20, v21\n"
  "v_cvt_pk_bf16_f32 v21, v22, v23\n"
  "ds_write_b64 v40, v[20:21] offset:3200\n"
  "v_accvgpr_read_b32 v52, a12\n"
  "v_accvgpr_read_b32 v53, a13\n"
  "v_and_b32 v48, 0xffff0000, v53\n"
  "v_lshlrev_b32 v49, 16, v53\n"
  "v_and_b32 v50, 0xffff0000, v52\n"
  "v_lshlrev_b32 v51, 16, v52\n"
  "v_fmac_f32 v24, s22, v48\n"
  "v_fmac_f32 v25, s22, v49\n"
  "v_fmac_f32 v26, s22, v50\n"
  "v_fmac_f32 v27, s22, v51\n"
  "v_cvt_pk_bf16_f32 v24, v24, v25\n"
  "v_cvt_pk_bf16_f32 v25, v26, v27\n"
  "ds_write_b64 v40, v[24:25] offset:3840\n"
  "v_accvgpr_read_b32 v60, a14\n"
  "v_accvgpr_read_b32 v61, a15\n"
  "v_and_b32 v56, 0xffff0000, v61\n"
  "v_lshlrev_b32 v57, 16, v61\n"
  "v_and_b32 v58, 0xffff0000, v60\n"
  "v_lshlrev_b32 v59, 16, v60\n"
  "v_fmac_f32 v28, s22, v56\n"
  "v_fmac_f32 v29, s22, v57\n"
  "v_fmac_f32 v30, s22, v58\n"
  "v_fmac_f32 v31, s22, v59\n"
  "v_cvt_pk_bf16_f32 v28, v28, v29\n"
  "v_cvt_pk_bf16_f32 v29, v30, v31\n"
  "ds_write_b64 v40, v[28:29] offset:4480\n"
  "s_waitcnt lgkmcnt(0)\n"
  "s_barrier\n"
  "ds_read_b32 v0, v41 offset:0\n"
  "ds_read_b32 v1, v41 offset:12928\n"
  "ds_read_b32 v2, v41 offset:25856\n"
  "ds_read_b32 v3, v41 offset:38784\n"
  "ds_read_b32 v4, v41 offset:1280\n"
  "ds_read_b32 v5, v41 offset:14208\n"
  "ds_read_b32 v6, v41 offset:27136\n"
  "ds_read_b32 v7, v41 offset:40064\n"
  "ds_read_b32 v8, v41 offset:2560\n"
  "ds_read_b32 v9, v41 offset:15488\n"
  "ds_read_b32 v10, v41 offset:28416\n"
  "ds_read_b32 v11, v41 offset:41344\n"
  "ds_read_b32 v12, v41 offset:3840\n"
  "ds_read_b32 v13, v41 offset:16768\n"
  "ds_read_b32 v14, v41 offset:29696\n"
  "ds_read_b32 v15, v41 offset:42624\n"
  "s_mov_b32 s23, 0x05040100\n"
  "s_mov_b32 s60, 0x07060302\n"
  "s_waitcnt lgkmcnt(12)\n"
  "v_perm_b32 v16, v1, v0, s23\n"
  "v_perm_b32 v17, v3, v2, s23\n"
  "v_perm_b32 v18, v1, v0, s60\n"
  "v_perm_b32 v19, v3, v2, s60\n"
  "buffer_store_dwordx4 v[16:19], v42, s[16:19], s28 offen sc1\n"
  "s_add_u32 s28, s28, 1056768\n"
  "s_waitcnt lgkmcnt(8)\n"
  "v_perm_b32 v20, v5, v4, s23\n"
  "v_perm_b32 v21, v7, v6, s23\n"
  "v_perm_b32 v22, v5, v4, s60\n"
  "v_perm_b32 v23, v7, v6, s60\n"
  "buffer_store_dwordx4 v[20:23], v42, s[16:19], s28 offen sc1\n"
  "s_add_u32 s28, s28, 1056768\n"
  "s_waitcnt lgkmcnt(4)\n"
  "v_perm_b32 v24, v9, v8, s23\n"
  "v_perm_b32 v25, v11, v10, s23\n"
  "v_perm_b32 v26, v9, v8, s60\n"
  "v_perm_b32 v27, v11, v10, s60\n"
  "buffer_store_dwordx4 v[24:27], v42, s[16:19], s28 offen sc1\n"
  "s_add_u32 s28, s28, 1056768\n"
  "s_waitcnt lgkmcnt(0)\n"
  "v_perm_b32 v28, v13, v12, s23\n"
  "v_perm_b32 v29, v15, v14, s23\n"
  "v_perm_b32 v30, v13, v12, s60\n"
  "v_perm_b32 v31, v15, v14, s60\n"
  "buffer_store_dwordx4 v[28:31], v42, s[16:19], s28 offen sc1\n"
  "s_nop 1\n"
  "s_waitcnt vmcnt(20)\n"
  "k1_asm_end:\n"
    : "={v[68:69]}"(qv[0]),"={v[70:71]}"(qv[1]),"={v[72:73]}"(qv[2]),"={v[74:75]}"(qv[3]),"={v[76:77]}"(qv[4]),"={v[78:79]}"(qv[5]),"={v[80:81]}"(qv[6]),"={v[82:83]}"(qv[7]),"={v[84:85]}"(cv2)
    : "{v32}"(lane*16u),"{v33}"(lane*16u+2u),"{v34}"(aad),"{v35}"(aad+1024u),"{v36}"(aad+2048u),"{v37}"(aad+3072u),
       "{v38}"(bad),"{v39}"(kad),"{v40}"(yw),"{v41}"(yr),"{v42}"(st),"{v43}"(z0),"{v44}"(msk),"{v45}"(zu),
       "{s[8:11]}"(srdH),"{s[12:15]}"(srdU),"{s[16:19]}"(srdY),"{s[24:27]}"(srdP),"{s[48:51]}"(srdQ),
       "{s20}"(d*4096u),"{s21}"(base),"{s[62:63]}"(dptr),"{s29}"((d&1)?0u:16u),"{v46}"(pf),
       "{v86}"(tg*16384u+tid*8u),"{v87}"((mhalf*512u+w*128u+(lane>>3))*12288u+mcb*128u+(lane&7u)*16u),"{v88}"(mhalf*1024u+w*256u+(lane&31u)*8u),
       "{s[52:55]}"(srdX),"{s[56:59]}"(srdM),"{s[64:67]}"(srdC),"{s46}"(ldsb+L_MODW+w*16384u),"{s61}"(ldsb+L_JUNK+w*1024u)
       ,"{s[68:71]}"(code_range("k1_asm_end","k3_loop_beg")),"{s72}"(((idx*4+w)&15u)*1024u)
    : "v0","v1","v2","v3","v4","v5","v6","v7","v8","v9","v10","v11","v12","v13","v14","v15","v16","v17","v18","v19","v20","v21","v22","v23","v24","v25","v26","v27","v28","v29","v30","v31","v47","v48","v49","v50","v51","v52","v53","v54","v55","v56","v57","v58","v59","v60","v61","v62","v63","v64","v65","v66","v67","a0","a1","a2","a3","a4","a5","a6","a7","a8","a9","a10","a11","a12","a13","a14","a15","a16","a17","a18","a19","a20","a21","a22","a23","a24","a25","a26","a27","a28","a29","a30","a31","a32","a33","a34","a35","a36","a37","a38","a39","a40","a41","a42","a43","a44","a45","a46","a47","a48","a49","a50","a51","a52","a53","a54","a55","a56","a57","a58","a59","a60","a61","a62","a63","a64","a65","a66","a67","a68","a69","a70","a71","a72","a73","a74","a75","a76","a77","a78","a79","a80","a81","a82","a83","a84","a85","a86","a87","a88","a89","a90","a91","a92","a93","a94","a95","a96","a97","a98","a99","a100","a101","a102","a103","a104","a105","a106","a107","a108","a109","a110","a111","a112","a113","a114","a115","a116","a117","a118","a119","a120","a121","a122","a123","a124","a125","a126","a127","s22","s23","s28","s47","s60","m0","scc","vcc","memory");
  }
#define K1_STN 0
  {
    const float c0=blo(cv2[0]),c1=bhi(cv2[0]),c2=blo(cv2[1]),c3=bhi(cv2[1]);
    const unsigned s01=pkbf(c0*__builtin_amdgcn_rcpf(1.f+__expf(-c0)),c1*__builtin_amdgcn_rcpf(1.f+__expf(-c1))), s23=pkbf(c2*__builtin_amdgcn_rcpf(1.f+__expf(-c2)),c3*__builtin_amdgcn_rcpf(1.f+__expf(-c3)));
    char* svp=lds3+L_SV+w*256;
    *(uint2*)(svp+(lane&31)*8)=make_uint2(s01,s23);
    __builtin_amdgcn_wave_barrier();
    asm volatile("s_waitcnt vmcnt(%0)"::"n"(2*K1_PFROWS+4+K1_STN+K1_QLAST):"memory");
    const unsigned g=lane>>4;
    const unsigned ta=ldsb+L_MODW+w*16384u+(4u*g+((lane&15)>>2))*128u+(lane&3)*8u;
    f32x4_t acc[4]={{0.f,0.f,0.f,0.f},{0.f,0.f,0.f,0.f},{0.f,0.f,0.f,0.f},{0.f,0.f,0.f,0.f}};
    u32x2_t fa[4][4][2]; u32x4 af[4];
    #pragma unroll
    for(int u=0;u<4;u++){
      const uint2 sa_=*(const uint2*)(svp+u*64+g*8), sb_=*(const uint2*)(svp+u*64+32+g*8);
      af[u]=(u32x4){sa_.x,sa_.y,sb_.x,sb_.y};
      #pragma unroll
      for(int n=0;n<4;n++){ fa[u][n][0]=lds_tr(ta+u*4096u+n*32u); fa[u][n][1]=lds_tr(ta+u*4096u+2048u+n*32u); }
    }
    #pragma unroll
    for(int u=0;u<4;u++){
      #pragma unroll
      for(int n=0;n<4;n++){
        const u32x4 f={fa[u][n][0][0],fa[u][n][0][1],fa[u][n][1][0],fa[u][n][1][1]};
        acc[n]=__builtin_amdgcn_mfma_f32_16x16x32_bf16(__builtin_bit_cast(bf16x8_t,af[u]),__builtin_bit_cast(bf16x8_t,f),acc[n],0,0,0);
      }
    }
    float* part=(float*)(lds3+L_PART);
    if(lane<16){
      #pragma unroll
      for(int n=0;n<4;n++) part[w*64+n*16+lane]=acc[n][0];
    }
  }
  asm volatile("s_waitcnt lgkmcnt(0)\n s_barrier":::"memory");
  if(tid<64 && has){
    const float* part=(const float*)(lds3+L_PART);
    const float s=(part[tid]+part[64+tid])+(part[128+tid]+part[192+tid]);
    asm volatile("global_store_dword %0, %1, off sc1"::"v"(modp+mhalf*6144+mcb*64+tid),"v"(s):"memory");
  }
  STAMP_MID(1)
  unsigned* gfl=flags+4096;
  unsigned* fa_=gfl+((tid&(K1_NREP-1))*256+cg)*K1_FSTR; unsigned one_=1u;
  asm volatile("":"+v"(fa_),"+v"(one_));
  asm volatile("s_waitcnt vmcnt(0) lgkmcnt(0)\n s_barrier":::"memory");
  if(tid<K1_NREP) asm volatile("global_store_dword %0, %1, off sc1"::"v"(fa_),"v"(one_):"memory");
  const unsigned pfo=(K1_PFROW0+(idx*4+w)*K1_PF2)*7168u+xcd*896u+(lane<56?lane:55u)*16u;
  auto pf2=[&](){
    u32x4 srdP=mksrd(wg,1024u*7168u), srdQ=mksrd(wu,1024u*7168u);
    #pragma unroll
    for(int j=0;j<K1_PF2;j++){ pf_lds<0>(srdP,pfo,j*7168u,ldsb+L_CONV+w*1024u); pf_lds<0>(srdQ,pfo,j*7168u,ldsb+L_CONV+w*1024u); }
    pf_lds<0>(code_range("k3_loop_beg","k3_loop_end"),((idx*4+w)&63u)*1024u+lane*16u,0u,ldsb+L_CONV+w*1024u);
  };
  bool pfd=false;
  if(K1_PFW==2){ pf2(); pfd=true; }
  #pragma unroll
  for(int t=0;t<8;t++) asm volatile("":"+v"(qv[t]));
  float xq[8][4];
  #pragma unroll
  for(int t=0;t<8;t++){ xq[t][0]=blo(qv[t][0]); xq[t][1]=bhi(qv[t][0]); xq[t][2]=blo(qv[t][1]); xq[t][3]=bhi(qv[t][1]); }
  {
    const float2 s1=wave_stats(xq,lane);
    if((lane&7)==0) ldsS[32+w*8+(lane>>3)]=s1;
  }
  const bf16_t* wsel=(w&1)?wu:wg;
  u32x4 srdW=mksrd(wsel,1024u*7168u);
  unsigned doff[7];
  #pragma unroll
  for(int p=0;p<7;p++){
    unsigned c=64*p+lane, row=c/14, cc=c-row*14;
    unsigned colb = fb*224+cc*16+odd*((cc<12)?32u:0xffffff40u);
    doff[p]=((w>>1)*32+row)*7168u+colb-(unsigned)((p<4?p:p-4)*1024);
    asm volatile("":"+v"(doff[p]));
  }
  asm volatile("s_waitcnt lgkmcnt(0)\n s_barrier":::"memory");
  STAMP_MID(2)
  u32x4 srdYp=mksrd((const char*)yg+(size_t)tg*16512,16384u), srdMp=mksrd(modp,12288u*4u);
  unsigned yop=w*4096u+(lane>>2)*64u+(((lane&3)-(lane>>4))&3)*16u, mo0=tid*16u, mo1=tid*16u+24576u, ytw=ldsb+K3_LDS+w*4608u;
  asm volatile("":"+v"(yop),"+v"(mo0),"+v"(mo1),"+s"(ytw),"+s"(srdYp),"+s"(srdMp));
  {
    const unsigned* fp=gfl+((K1_REPX)%K1_NREP*256+tid)*K1_FSTR;
    unsigned n=0, v;
    do{
      asm volatile("global_load_dword %0, %1, off sc1\n s_waitcnt vmcnt(0)":"=v"(v):"v"(fp):"memory");
      if(__builtin_amdgcn_ballot_w64(v!=1u)==0ull) break;
      if(K1_PFW==1 && !pfd){ pf2(); pfd=true; }
      __builtin_amdgcn_s_sleep(K1_PSLEEP);
    }while(++n<0x40000u);
  }
  STAMP_MID(3)
  {
  u32x4 yv[4], mv[5], mw[5];
  asm volatile("s_mov_b32 m0, %0\n s_nop 0\n"
    "buffer_load_dwordx4 %1, %2, 0 offen offset:0 sc1 lds\n buffer_load_dwordx4 %1, %2, 0 offen offset:1024 sc1 lds\n"
    "buffer_load_dwordx4 %1, %2, 0 offen offset:2048 sc1 lds\n buffer_load_dwordx4 %1, %2, 0 offen offset:3072 sc1 lds"
    ::"s"(ytw),"v"(yop),"s"(srdYp):"memory");
  #pragma unroll
  for(int j=0;j<5;j++)
    asm volatile("buffer_load_dwordx4 %0, %2, %4, %5 offen sc1\n buffer_load_dwordx4 %1, %3, %4, %5 offen sc1"
      :"=&v"(mv[j]),"=&v"(mw[j]):"v"(mo0),"v"(mo1),"s"(srdMp),"s"(j*4096u):"memory");
  #pragma unroll
  for(int p=0;p<7;p++)
    asm volatile("s_mov_b32 m0, %0\n s_nop 0\n buffer_load_dwordx4 %1, %2, %3 offen offset:%4 lds"
      ::"s"(dreg+(p>=4?4096u:0u)),"v"(doff[p]),"s"(srdW),"s"(0u),"n"((p<4?p:p-4)*1024):"memory");
  if(!pfd) pf2();
  f2v hn2[8][2];
  {
    const float2 st1=comb_stats(ldsS+32,lane);
    #pragma unroll
    for(int t=0;t<8;t++){
      const float m=__uint_as_float(__builtin_amdgcn_readlane(__float_as_uint(st1.x),t));
      const float rs=__uint_as_float(__builtin_amdgcn_readlane(__float_as_uint(st1.y),t));
      #pragma unroll
      for(int h=0;h<2;h++){ hn2[t][h]=(up2(qv[t][h])-(f2v){m,m})*(f2v){rs,rs}; asm volatile("":"+v"(hn2[t][h])); }
    }
  }
  if(pfd) asm volatile("s_waitcnt vmcnt(7)":::"memory"); else asm volatile("s_waitcnt vmcnt(%0)"::"n"(7+2*K1_PF2+((CPF&4)?1:0)):"memory");
  {
    const char* yl=lds3+K3_LDS+w*4608+lane*64;
    #pragma unroll
    for(int j=0;j<4;j++) yv[j]=*(const u32x4*)(yl+((j+(lane>>2))&3)*16);
    __builtin_amdgcn_wave_barrier();
  }
  #pragma unroll
  for(int j=0;j<5;j++){ LD_PIN(mv[j]); LD_PIN(mw[j]); }
  f2v sa2[2],ca2[2],ga2[2],sb2[2],cb2[2];
  #pragma unroll
  for(int h=0;h<2;h++){
    const f2v one={1.f,1.f};
    sa2[h]=asf2(mv[0][2*h],mv[0][2*h+1])+asf2(mw[0][2*h],mw[0][2*h+1]); ca2[h]=(one+asf2(mv[1][2*h],mv[1][2*h+1]))+asf2(mw[1][2*h],mw[1][2*h+1]);
    ga2[h]=asf2(mv[2][2*h],mv[2][2*h+1])+asf2(mw[2][2*h],mw[2][2*h+1]); sb2[h]=asf2(mv[3][2*h],mv[3][2*h+1])+asf2(mw[3][2*h],mw[3][2*h+1]);
    cb2[h]=(one+asf2(mv[4][2*h],mv[4][2*h+1]))+asf2(mw[4][2*h],mw[4][2*h+1]);
  }
  f2v x12[8][2]; unsigned xp[8][2];
  #pragma unroll
  for(int t=0;t<8;t++){
    #pragma unroll
    for(int h=0;h<2;h++){
      const f2v hh=__builtin_elementwise_fma(hn2[t][h],ca2[h],sa2[h]);
      const f2v r=__builtin_elementwise_fma(ga2[h],hh*up2(yv[t>>1][(t&1)*2+h]),up2(qv[t][h]));
      xp[t][h]=pkbf(r.x,r.y); x12[t][h]=up2(xp[t][h]);
    }
  }
  STAMP_MID(1)
  #pragma unroll
  for(int t=1;t<K23_EARLY;t++){
    #pragma unroll
    for(int p=0;p<7;p++)
      asm volatile("s_mov_b32 m0, %0\n s_nop 0\n buffer_load_dwordx4 %1, %2, %3 offen offset:%4 lds"
        ::"s"(dreg+t*28672u+(p>=4?4096u:0u)),"v"(doff[p]),"s"(srdW),"s"(t*64u*7168u),"n"((p<4?p:p-4)*1024):"memory");
  }
  char* tw=lds3+K3_LDS+w*4608;
  const unsigned wa=((lane>>3)*4+(lane&3))*144+((lane>>2)&1)*8, ra=(lane>>3)*144+(lane&7)*16;
  {
    const float2 s2=wave_stats2(x12,lane);
    if((lane&7)==0) ldsS[w*8+(lane>>3)]=s2;
  }
  const unsigned Tp=tg>>3, ii=(tg>>1)&3, r0=(tg&1)*8;
  const size_t gb=(size_t)Tp*131072+ii*1024+r0*16;
  const unsigned ll=lane>>3;
  asm volatile("s_waitcnt lgkmcnt(0)\n s_barrier":::"memory");
  STAMP_MID(2)
  const float2 st2=comb_stats(ldsS,lane);
  #pragma unroll
  for(int t=0;t<8;t++){
    const float m=__uint_as_float(__builtin_amdgcn_readlane(__float_as_uint(st2.x),t));
    const float rs=__uint_as_float(__builtin_amdgcn_readlane(__float_as_uint(st2.y),t));
    const f2v r0_=__builtin_elementwise_fma((x12[t][0]-(f2v){m,m})*(f2v){rs,rs},cb2[0],sb2[0]), r1_=__builtin_elementwise_fma((x12[t][1]-(f2v){m,m})*(f2v){rs,rs},cb2[1],sb2[1]);
    *(uint2*)(tw+wa+t*16)=make_uint2(pkbf(r0_.x,r0_.y),pkbf(r1_.x,r1_.y));
  }
  __builtin_amdgcn_wave_barrier();
  #pragma unroll
  for(int j=0;j<4;j++){
    const unsigned l=ll+8*j, s=w*8+(l>>2), g=l&3;
    u32x4 v=*(const u32x4*)(tw+ra+j*8*144);
    st16<1>((char*)h2t+gb+s*4096+g*256+(lane&7)*16,v);
  }
  STAMP_MID(3)
  unsigned* fp=flags+(Tp*4+(tid&3))*32+(tg&7); unsigned one2_=1u;
  asm volatile("":"+v"(fp),"+v"(one2_));
  asm volatile("s_waitcnt vmcnt(0) lgkmcnt(0)\n s_barrier":::"memory");
  if(tid<4) asm volatile("global_store_dword %0, %1, off sc1"::"v"(fp),"v"(one2_):"memory");
  __builtin_amdgcn_wave_barrier();
  #pragma unroll
  for(int t=0;t<8;t++) *(uint2*)(tw+wa+t*16)=make_uint2(xp[t][0],xp[t][1]);
  __builtin_amdgcn_wave_barrier();
  #pragma unroll
  for(int j=0;j<4;j++){
    const unsigned l=ll+8*j, s=w*8+(l>>2), g=l&3;
    u32x4 v=*(const u32x4*)(tw+ra+j*8*144);
    st16<K2_SFX>((char*)x1t+gb+s*4096+g*256+(lane&7)*16,v);
  }
  }
  const unsigned l16=lane*16;
  const unsigned g=lane>>4, qq=(lane&15)>>2, pp=lane&3;
  const unsigned ldl=ldsb+(4*g+qq)*224+8*pp;
  const unsigned vhalf=l16+odd*8;
  u32x4 srdT=mksrd((const char*)h2t+(size_t)T*131072,131072u);
  u32x4 srdO=mksrd((const char*)Ht+(size_t)T*(112u*4096u),112u*4096u);
  u32x4 srdP=mksrd(wd,3584u*2048u);
  const unsigned c7=(fb>>1)*7;
  const unsigned ooff=(c7+(odd?4:0))*4096u, hoff=(c7+3)*4096u;
  const unsigned* fpoll=flags+(T*4+(fb&3))*32;
  const unsigned tb4=(xcd>>1)*4+(idx&3), nb4=(xcd&1)*8+(idx>>2);
  const unsigned rot4=K4_ROTEXPR;
  unsigned doff4;
  { const unsigned r8=lane>>3, c8=lane&7; doff4=(w*8+r8)*2048u+nb4*128u+32u*((c8>>1)^((r8>>1)&3))+16u*(c8&1); }
  u32x4 srdC2=code_range("k3_loop_end","k4_code_end");
  unsigned ooff_=ooff, pfo_=(blk*4+w)*7168u;
  unsigned* const hfl=flags+8192;
  u32x4 hd0,hd1,hd2,hd3; u32x2_t he0,he1,he2,he3; unsigned pv;
  const unsigned T4=tb4*2+(w>>1), ih=w&1;
  asm volatile(
  "s_nop 4\n"
  "k3_loop_beg:\n"
  "ds_read_b64_tr_b16 a[0:1], v232 offset:0\n"
  "ds_read_b64_tr_b16 a[2:3], v232 offset:3584\n"
  "ds_read_b64_tr_b16 a[4:5], v232 offset:7168\n"
  "ds_read_b64_tr_b16 a[6:7], v232 offset:10752\n"
  "ds_read_b64_tr_b16 a[8:9], v232 offset:32\n"
  "ds_read_b64_tr_b16 a[10:11], v232 offset:3616\n"
  "s_mov_b32 s20, 0\n"
  "1:\n"
  "global_load_dword v236, v237, s[56:57] sc1\n"
  "s_waitcnt vmcnt(0)\n"
  "v_cmp_ne_u32 vcc, 1, v236\n"
  "s_nop 4\n"
  "s_cbranch_vccz 2f\n"
  "s_add_u32 s20, s20, 1\n"
  "s_cmp_lt_u32 s20, 0x40000\n"
  "s_cbranch_scc0 2f\n"
  "s_sleep 1\n"
  "s_branch 1b\n"
  "2:\n"
  "s_mov_b32 s21, 0\n"
  "buffer_load_dwordx4 a[28:31], v231, s[12:15], s21 offen offset:0 sc1\n"
  "buffer_load_dwordx4 a[32:35], v231, s[12:15], s21 offen offset:1024 sc1\n"
  "buffer_load_dwordx4 a[36:39], v231, s[12:15], s21 offen offset:2048 sc1\n"
  "buffer_load_dwordx4 a[40:43], v231, s[12:15], s21 offen offset:3072 sc1\n"
  "s_mov_b32 s21, 4096\n"
  "buffer_load_dwordx4 a[44:47], v231, s[12:15], s21 offen offset:0 sc1\n"
  "buffer_load_dwordx4 a[48:51], v231, s[12:15], s21 offen offset:1024 sc1\n"
  "buffer_load_dwordx4 a[52:55], v231, s[12:15], s21 offen offset:2048 sc1\n"
  "buffer_load_dwordx4 a[56:59], v231, s[12:15], s21 offen offset:3072 sc1\n"
  "s_mov_b32 s20, 458752\n"
  "s_add_u32 m0, s23, 28672\n"
  "s_nop 0\n"
  "buffer_load_dwordx4 v224, s[8:11], s20 offen offset:0 lds\n"
  "buffer_load_dwordx4 v225, s[8:11], s20 offen offset:1024 lds\n"
  "buffer_load_dwordx4 v226, s[8:11], s20 offen offset:2048 lds\n"
  "buffer_load_dwordx4 v227, s[8:11], s20 offen offset:3072 lds\n"
  "s_add_u32 m0, s23, 32768\n"
  "s_nop 0\n"
  "buffer_load_dwordx4 v228, s[8:11], s20 offen offset:0 lds\n"
  "buffer_load_dwordx4 v229, s[8:11], s20 offen offset:1024 lds\n"
  "buffer_load_dwordx4 v230, s[8:11], s20 offen offset:2048 lds\n"
  "s_mov_b32 s21, 8192\n"
  "buffer_load_dwordx4 a[60:63], v231, s[12:15], s21 offen offset:0 sc1\n"
  "buffer_load_dwordx4 a[64:67], v231, s[12:15], s21 offen offset:1024 sc1\n"
  "buffer_load_dwordx4 a[68:71], v231, s[12:15], s21 offen offset:2048 sc1\n"
  "buffer_load_dwordx4 a[72:75], v231, s[12:15], s21 offen offset:3072 sc1\n"
  "s_mov_b32 s21, 12288\n"
  "buffer_load_dwordx4 a[76:79], v231, s[12:15], s21 offen offset:0 sc1\n"
  "buffer_load_dwordx4 a[80:83], v231, s[12:15], s21 offen offset:1024 sc1\n"
  "buffer_load_dwordx4 a[84:87], v231, s[12:15], s21 offen offset:2048 sc1\n"
  "s_waitcnt vmcnt(18) lgkmcnt(4)\n"
  "v_mfma_f32_16x16x32_bf16 v[0:3], a[0:3], a[28:31], 0\n"
  "ds_read_b64_tr_b16 a[12:13], v232 offset:7200\n"
  "v_mfma_f32_16x16x32_bf16 v[4:7], a[0:3], a[32:35], 0\n"
  "ds_read_b64_tr_b16 a[14:15], v232 offset:10784\n"
  "v_mfma_f32_16x16x32_bf16 v[8:11], a[0:3], a[36:39], 0\n"
  "v_mfma_f32_16x16x32_bf16 v[12:15], a[0:3], a[40:43], 0\n"
  "s_waitcnt lgkmcnt(4)\n"
  "v_mfma_f32_16x16x32_bf16 v[16:19], a[4:7], a[28:31], 0\n"
  "ds_read_b64_tr_b16 a[16:17], v232 offset:64\n"
  "v_mfma_f32_16x16x32_bf16 v[20:23], a[4:7], a[32:35], 0\n"
  "ds_read_b64_tr_b16 a[18:19], v232 offset:3648\n"
  "v_mfma_f32_16x16x32_bf16 v[24:27], a[4:7], a[36:39], 0\n"
  "buffer_load_dwordx4 a[88:91], v231, s[12:15], s21 offen offset:3072 sc1\n"
  "v_mfma_f32_16x16x32_bf16 v[28:31], a[4:7], a[40:43], 0\n"
  "s_waitcnt lgkmcnt(4)\n"
  "v_mfma_f32_16x16x32_bf16 v[32:35], a[8:11], a[28:31], 0\n"
  "ds_read_b64_tr_b16 a[20:21], v232 offset:7232\n"
  "v_mfma_f32_16x16x32_bf16 v[36:39], a[8:11], a[32:35], 0\n"
  "ds_read_b64_tr_b16 a[22:23], v232 offset:10816\n"
  "v_mfma_f32_16x16x32_bf16 v[40:43], a[8:11], a[36:39], 0\n"
  "s_mov_b32 s20, 917504\n"
  "s_add_u32 m0, s23, 57344\n"
  "s_nop 0\n"
  "buffer_load_dwordx4 v224, s[8:11], s20 offen offset:0 lds\n"
  "v_mfma_f32_16x16x32_bf16 v[44:47], a[8:11], a[40:43], 0\n"
  "s_waitcnt lgkmcnt(4)\n"
  "v_mfma_f32_16x16x32_bf16 v[48:51], a[12:15], a[28:31], 0\n"
  "ds_read_b64_tr_b16 a[24:25], v232 offset:96\n"
  "v_mfma_f32_16x16x32_bf16 v[52:55], a[12:15], a[32:35], 0\n"
  "ds_read_b64_tr_b16 a[26:27], v232 offset:3680\n"
  "v_mfma_f32_16x16x32_bf16 v[56:59], a[12:15], a[36:39], 0\n"
  "v_mfma_f32_16x16x32_bf16 v[60:63], a[12:15], a[40:43], 0\n"
  "s_waitcnt lgkmcnt(4)\n"
  "v_mfma_f32_16x16x32_bf16 v[64:67], a[16:19], a[28:31], 0\n"
  "ds_read_b64_tr_b16 a[0:1], v232 offset:7264\n"
  "v_mfma_f32_16x16x32_bf16 v[68:71], a[16:19], a[32:35], 0\n"
  "ds_read_b64_tr_b16 a[2:3], v232 offset:10848\n"
  "v_mfma_f32_16x16x32_bf16 v[72:75], a[16:19], a[36:39], 0\n"
  "buffer_load_dwordx4 v225, s[8:11], s20 offen offset:1024 lds\n"
  "v_mfma_f32_16x16x32_bf16 v[76:79], a[16:19], a[40:43], 0\n"
  "s_waitcnt lgkmcnt(4)\n"
  "v_mfma_f32_16x16x32_bf16 v[80:83], a[20:23], a[28:31], 0\n"
  "ds_read_b64_tr_b16 a[4:5], v232 offset:128\n"
  "v_mfma_f32_16x16x32_bf16 v[84:87], a[20:23], a[32:35], 0\n"
  "ds_read_b64_tr_b16 a[6:7], v232 offset:3712\n"
  "v_mfma_f32_16x16x32_bf16 v[88:91], a[20:23], a[36:39], 0\n"
  "buffer_load_dwordx4 v226, s[8:11], s20 offen offset:2048 lds\n"
  "v_mfma_f32_16x16x32_bf16 v[92:95], a[20:23], a[40:43], 0\n"
  "s_waitcnt lgkmcnt(4)\n"
  "v_mfma_f32_16x16x32_bf16 v[96:99], a[24:27], a[28:31], 0\n"
  "ds_read_b64_tr_b16 a[8:9], v232 offset:7296\n"
  "v_mfma_f32_16x16x32_bf16 v[100:103], a[24:27], a[32:35], 0\n"
  "ds_read_b64_tr_b16 a[10:11], v232 offset:10880\n"
  "v_mfma_f32_16x16x32_bf16 v[104:107], a[24:27], a[36:39], 0\n"
  "v_mfma_f32_16x16x32_bf16 v[108:111], a[24:27], a[40:43], 0\n"
  "s_waitcnt lgkmcnt(4)\n"
  "v_mfma_f32_16x16x32_bf16 v[112:115], a[0:3], a[28:31], 0\n"
  "ds_read_b64_tr_b16 a[12:13], v232 offset:160\n"
  "v_mfma_f32_16x16x32_bf16 v[116:119], a[0:3], a[32:35], 0\n"
  "ds_read_b64_tr_b16 a[14:15], v232 offset:3744\n"
  "v_mfma_f32_16x16x32_bf16 v[120:123], a[0:3], a[36:39], 0\n"
  "buffer_load_dwordx4 v227, s[8:11], s20 offen offset:3072 lds\n"
  "v_mfma_f32_16x16x32_bf16 v[124:127], a[0:3], a[40:43], 0\n"
  "s_waitcnt lgkmcnt(4)\n"
  "v_mfma_f32_16x16x32_bf16 v[128:131], a[4:7], a[28:31], 0\n"
  "ds_read_b64_tr_b16 a[16:17], v232 offset:7328\n"
  "v_mfma_f32_16x16x32_bf16 v[132:135], a[4:7], a[32:35], 0\n"
  "ds_read_b64_tr_b16 a[18:19], v232 offset:10912\n"
  "v_mfma_f32_16x16x32_bf16 v[136:139], a[4:7], a[36:39], 0\n"
  "s_add_u32 m0, s23, 61440\n"
  "s_nop 0\n"
  "buffer_load_dwordx4 v228, s[8:11], s20 offen offset:0 lds\n"
  "v_mfma_f32_16x16x32_bf16 v[140:143], a[4:7], a[40:43], 0\n"
  "s_waitcnt lgkmcnt(4)\n"
  "v_mfma_f32_16x16x32_bf16 v[144:147], a[8:11], a[28:31], 0\n"
  "ds_read_b64_tr_b16 a[20:21], v232 offset:192\n"
  "v_mfma_f32_16x16x32_bf16 v[148:151], a[8:11], a[32:35], 0\n"
  "ds_read_b64_tr_b16 a[22:23], v232 offset:3776\n"
  "v_mfma_f32_16x16x32_bf16 v[152:155], a[8:11], a[36:39], 0\n"
  "buffer_load_dwordx4 v229, s[8:11], s20 offen offset:1024 lds\n"
  "v_mfma_f32_16x16x32_bf16 v[156:159], a[8:11], a[40:43], 0\n"
  "s_waitcnt lgkmcnt(4)\n"
  "v_mfma_f32_16x16x32_bf16 v[160:163], a[12:15], a[28:31], 0\n"
  "ds_read_b64_tr_b16 a[24:25], v232 offset:7360\n"
  "v_mfma_f32_16x16x32_bf16 v[164:167], a[12:15], a[32:35], 0\n"
  "ds_read_b64_tr_b16 a[26:27], v232 offset:10944\n"
  "v_mfma_f32_16x16x32_bf16 v[168:171], a[12:15], a[36:39], 0\n"
  "v_mfma_f32_16x16x32_bf16 v[172:175], a[12:15], a[40:43], 0\n"
  "s_waitcnt lgkmcnt(4)\n"
  "v_mfma_f32_16x16x32_bf16 v[176:179], a[16:19], a[28:31], 0\n"
  "ds_read_b64_tr_b16 a[0:1], v232 offset:14336\n"
  "v_mfma_f32_16x16x32_bf16 v[180:183], a[16:19], a[32:35], 0\n"
  "ds_read_b64_tr_b16 a[2:3], v232 offset:17920\n"
  "v_mfma_f32_16x16x32_bf16 v[184:187], a[16:19], a[36:39], 0\n"
  "buffer_load_dwordx4 v230, s[8:11], s20 offen offset:2048 lds\n"
  "v_mfma_f32_16x16x32_bf16 v[188:191], a[16:19], a[40:43], 0\n"
  "s_waitcnt lgkmcnt(4)\n"
  "v_mfma_f32_16x16x32_bf16 v[192:195], a[20:23], a[28:31], 0\n"
  "ds_read_b64_tr_b16 a[4:5], v232 offset:21504\n"
  "v_mfma_f32_16x16x32_bf16 v[196:199], a[20:23], a[32:35], 0\n"
  "ds_read_b64_tr_b16 a[6:7], v232 offset:25088\n"
  "v_mfma_f32_16x16x32_bf16 v[200:203], a[20:23], a[36:39], 0\n"
  "s_mov_b32 s21, 16384\n"
  "buffer_load_dwordx4 a[92:95], v231, s[12:15], s21 offen offset:0 sc1\n"
  "v_mfma_f32_16x16x32_bf16 v[204:207], a[20:23], a[40:43], 0\n"
  "s_waitcnt lgkmcnt(4)\n"
  "v_mfma_f32_16x16x32_bf16 v[208:211], a[24:27], a[28:31], 0\n"
  "ds_read_b64_tr_b16 a[8:9], v232 offset:14368\n"
  "v_mfma_f32_16x16x32_bf16 v[212:215], a[24:27], a[32:35], 0\n"
  "ds_read_b64_tr_b16 a[10:11], v232 offset:17952\n"
  "v_mfma_f32_16x16x32_bf16 v[216:219], a[24:27], a[36:39], 0\n"
  "v_mfma_f32_16x16x32_bf16 v[220:223], a[24:27], a[40:43], 0\n"
  "s_waitcnt vmcnt(23) lgkmcnt(4)\n"
  "v_mfma_f32_16x16x32_bf16 v[0:3], a[0:3], a[44:47], v[0:3]\n"
  "ds_read_b64_tr_b16 a[12:13], v232 offset:21536\n"
  "v_mfma_f32_16x16x32_bf16 v[4:7], a[0:3], a[48:51], v[4:7]\n"
  "ds_read_b64_tr_b16 a[14:15], v232 offset:25120\n"
  "v_mfma_f32_16x16x32_bf16 v[8:11], a[0:3], a[52:55], v[8:11]\n"
  "buffer_load_dwordx4 a[96:99], v231, s[12:15], s21 offen offset:1024 sc1\n"
  "v_mfma_f32_16x16x32_bf16 v[12:15], a[0:3], a[56:59], v[12:15]\n"
  "s_waitcnt lgkmcnt(4)\n"
  "v_mfma_f32_16x16x32_bf16 v[16:19], a[4:7], a[44:47], v[16:19]\n"
  "ds_read_b64_tr_b16 a[16:17], v232 offset:14400\n"
  "v_mfma_f32_16x16x32_bf16 v[20:23], a[4:7], a[48:51], v[20:23]\n"
  "ds_read_b64_tr_b16 a[18:19], v232 offset:17984\n"
  "v_mfma_f32_16x16x32_bf16 v[24:27], a[4:7], a[52:55], v[24:27]\n"
  "buffer_load_dwordx4 a[100:103], v231, s[12:15], s21 offen offset:2048 sc1\n"
  "v_mfma_f32_16x16x32_bf16 v[28:31], a[4:7], a[56:59], v[28:31]\n"
  "s_waitcnt lgkmcnt(4)\n"
  "v_mfma_f32_16x16x32_bf16 v[32:35], a[8:11], a[44:47], v[32:35]\n"
  "ds_read_b64_tr_b16 a[20:21], v232 offset:21568\n"
  "v_mfma_f32_16x16x32_bf16 v[36:39], a[8:11], a[48:51], v[36:39]\n"
  "ds_read_b64_tr_b16 a[22:23], v232 offset:25152\n"
  "v_mfma_f32_16x16x32_bf16 v[40:43], a[8:11], a[52:55], v[40:43]\n"
  "buffer_load_dwordx4 a[104:107], v231, s[12:15], s21 offen offset:3072 sc1\n"
  "v_mfma_f32_16x16x32_bf16 v[44:47], a[8:11], a[56:59], v[44:47]\n"
  "s_waitcnt lgkmcnt(4)\n"
  "v_mfma_f32_16x16x32_bf16 v[48:51], a[12:15], a[44:47], v[48:51]\n"
  "ds_read_b64_tr_b16 a[24:25], v232 offset:14432\n"
  "v_mfma_f32_16x16x32_bf16 v[52:55], a[12:15], a[48:51], v[52:55]\n"
  "ds_read_b64_tr_b16 a[26:27], v232 offset:18016\n"
  "v_mfma_f32_16x16x32_bf16 v[56:59], a[12:15], a[52:55], v[56:59]\n"
  "v_mfma_f32_16x16x32_bf16 v[60:63], a[12:15], a[56:59], v[60:63]\n"
  "s_waitcnt lgkmcnt(4)\n"
  "v_mfma_f32_16x16x32_bf16 v[64:67], a[16:19], a[44:47], v[64:67]\n"
  "ds_read_b64_tr_b16 a[0:1], v232 offset:21600\n"
  "v_mfma_f32_16x16x32_bf16 v[68:71], a[16:19], a[48:51], v[68:71]\n"
  "ds_read_b64_tr_b16 a[2:3], v232 offset:25184\n"
  "v_mfma_f32_16x16x32_bf16 v[72:75], a[16:19], a[52:55], v[72:75]\n"
  "s_mov_b32 s21, 20480\n"
  "buffer_load_dwordx4 a[108:111], v231, s[12:15], s21 offen offset:0 sc1\n"
  "v_mfma_f32_16x16x32_bf16 v[76:79], a[16:19], a[56:59], v[76:79]\n"
  "s_waitcnt lgkmcnt(4)\n"
  "v_mfma_f32_16x16x32_bf16 v[80:83], a[20:23], a[44:47], v[80:83]\n"
  "ds_read_b64_tr_b16 a[4:5], v232 offset:14464\n"
  "v_mfma_f32_16x16x32_bf16 v[84:87], a[20:23], a[48:51], v[84:87]\n"
  "ds_read_b64_tr_b16 a[6:7], v232 offset:18048\n"
  "v_mfma_f32_16x16x32_bf16 v[88:91], a[20:23], a[52:55], v[88:91]\n"
  "buffer_load_dwordx4 a[112:115], v231, s[12:15], s21 offen offset:1024 sc1\n"
  "v_mfma_f32_16x16x32_bf16 v[92:95], a[20:23], a[56:59], v[92:95]\n"
  "s_waitcnt lgkmcnt(4)\n"
  "v_mfma_f32_16x16x32_bf16 v[96:99], a[24:27], a[44:47], v[96:99]\n"
  "ds_read_b64_tr_b16 a[8:9], v232 offset:21632\n"
  "v_mfma_f32_16x16x32_bf16 v[100:103], a[24:27], a[48:51], v[100:103]\n"
  "ds_read_b64_tr_b16 a[10:11], v232 offset:25216\n"
  "v_mfma_f32_16x16x32_bf16 v[104:107], a[24:27], a[52:55], v[104:107]\n"
  "v_mfma_f32_16x16x32_bf16 v[108:111], a[24:27], a[56:59], v[108:111]\n"
  "s_waitcnt lgkmcnt(4)\n"
  "v_mfma_f32_16x16x32_bf16 v[112:115], a[0:3], a[44:47], v[112:115]\n"
  "ds_read_b64_tr_b16 a[12:13], v232 offset:14496\n"
  "v_mfma_f32_16x16x32_bf16 v[116:119], a[0:3], a[48:51], v[116:119]\n"
  "ds_read_b64_tr_b16 a[14:15], v232 offset:18080\n"
  "v_mfma_f32_16x16x32_bf16 v[120:123], a[0:3], a[52:55], v[120:123]\n"
  "buffer_load_dwordx4 a[116:119], v231, s[12:15], s21 offen offset:2048 sc1\n"
  "v_mfma_f32_16x16x32_bf16 v[124:127], a[0:3], a[56:59], v[124:127]\n"
  "s_waitcnt lgkmcnt(4)\n"
  "v_mfma_f32_16x16x32_bf16 v[128:131], a[4:7], a[44:47], v[128:131]\n"
  "ds_read_b64_tr_b16 a[16:17], v232 offset:21664\n"
  "v_mfma_f32_16x16x32_bf16 v[132:135], a[4:7], a[48:51], v[132:135]\n"
  "ds_read_b64_tr_b16 a[18:19], v232 offset:25248\n"
  "v_mfma_f32_16x16x32_bf16 v[136:139], a[4:7], a[52:55], v[136:139]\n"
  "buffer_load_dwordx4 a[120:123], v231, s[12:15], s21 offen offset:3072 sc1\n"
  "v_mfma_f32_16x16x32_bf16 v[140:143], a[4:7], a[56:59], v[140:143]\n"
  "s_waitcnt lgkmcnt(4)\n"
  "v_mfma_f32_16x16x32_bf16 v[144:147], a[8:11], a[44:47], v[144:147]\n"
  "ds_read_b64_tr_b16 a[20:21], v232 offset:14528\n"
  "v_mfma_f32_16x16x32_bf16 v[148:151], a[8:11], a[48:51], v[148:151]\n"
  "ds_read_b64_tr_b16 a[22:23], v232 offset:18112\n"
  "v_mfma_f32_16x16x32_bf16 v[152:155], a[8:11], a[52:55], v[152:155]\n"
  "s_mov_b32 s20, 1376256\n"
  "s_add_u32 m0, s23, 86016\n"
  "s_nop 0\n"
  "buffer_load_dwordx4 v224, s[8:11], s20 offen offset:0 lds\n"
  "v_mfma_f32_16x16x32_bf16 v[156:159], a[8:11], a[56:59], v[156:159]\n"
  "s_waitcnt lgkmcnt(4)\n"
  "v_mfma_f32_16x16x32_bf16 v[160:163], a[12:15], a[44:47], v[160:163]\n"
  "ds_read_b64_tr_b16 a[24:25], v232 offset:21696\n"
  "v_mfma_f32_16x16x32_bf16 v[164:167], a[12:15], a[48:51], v[164:167]\n"
  "ds_read_b64_tr_b16 a[26:27], v232 offset:25280\n"
  "v_mfma_f32_16x16x32_bf16 v[168:171], a[12:15], a[52:55], v[168:171]\n"
  "v_mfma_f32_16x16x32_bf16 v[172:175], a[12:15], a[56:59], v[172:175]\n"
  "s_waitcnt vmcnt(24)\n"
  "s_barrier\n"
  "s_waitcnt lgkmcnt(4)\n"
  "v_mfma_f32_16x16x32_bf16 v[176:179], a[16:19], a[44:47], v[176:179]\n"
  "ds_read_b64_tr_b16 a[0:1], v232 offset:28672\n"
  "v_mfma_f32_16x16x32_bf16 v[180:183], a[16:19], a[48:51], v[180:183]\n"
  "ds_read_b64_tr_b16 a[2:3], v232 offset:32256\n"
  "v_mfma_f32_16x16x32_bf16 v[184:187], a[16:19], a[52:55], v[184:187]\n"
  "buffer_load_dwordx4 v225, s[8:11], s20 offen offset:1024 lds\n"
  "v_mfma_f32_16x16x32_bf16 v[188:191], a[16:19], a[56:59], v[188:191]\n"
  "s_waitcnt lgkmcnt(4)\n"
  "v_mfma_f32_16x16x32_bf16 v[192:195], a[20:23], a[44:47], v[192:195]\n"
  "ds_read_b64_tr_b16 a[4:5], v232 offset:35840\n"
  "v_mfma_f32_16x16x32_bf16 v[196:199], a[20:23], a[48:51], v[196:199]\n"
  "ds_read_b64_tr_b16 a[6:7], v232 offset:39424\n"
  "v_mfma_f32_16x16x32_bf16 v[200:203], a[20:23], a[52:55], v[200:203]\n"
  "buffer_load_dwordx4 v226, s[8:11], s20 offen offset:2048 lds\n"
  "v_mfma_f32_16x16x32_bf16 v[204:207], a[20:23], a[56:59], v[204:207]\n"
  "s_waitcnt lgkmcnt(4)\n"
  "v_mfma_f32_16x16x32_bf16 v[208:211], a[24:27], a[44:47], v[208:211]\n"
  "ds_read_b64_tr_b16 a[8:9], v232 offset:28704\n"
  "v_mfma_f32_16x16x32_bf16 v[212:215], a[24:27], a[48:51], v[212:215]\n"
  "ds_read_b64_tr_b16 a[10:11], v232 offset:32288\n"
  "v_mfma_f32_16x16x32_bf16 v[216:219], a[24:27], a[52:55], v[216:219]\n"
  "v_mfma_f32_16x16x32_bf16 v[220:223], a[24:27], a[56:59], v[220:223]\n"
  "s_waitcnt vmcnt(22) lgkmcnt(4)\n"
  "v_mfma_f32_16x16x32_bf16 v[0:3], a[0:3], a[60:63], v[0:3]\n"
  "ds_read_b64_tr_b16 a[12:13], v232 offset:35872\n"
  "v_mfma_f32_16x16x32_bf16 v[4:7], a[0:3], a[64:67], v[4:7]\n"
  "ds_read_b64_tr_b16 a[14:15], v232 offset:39456\n"
  "v_mfma_f32_16x16x32_bf16 v[8:11], a[0:3], a[68:71], v[8:11]\n"
  "buffer_load_dwordx4 v227, s[8:11], s20 offen offset:3072 lds\n"
  "v_mfma_f32_16x16x32_bf16 v[12:15], a[0:3], a[72:75], v[12:15]\n"
  "s_waitcnt lgkmcnt(4)\n"
  "v_mfma_f32_16x16x32_bf16 v[16:19], a[4:7], a[60:63], v[16:19]\n"
  "ds_read_b64_tr_b16 a[16:17], v232 offset:28736\n"
  "v_mfma_f32_16x16x32_bf16 v[20:23], a[4:7], a[64:67], v[20:23]\n"
  "ds_read_b64_tr_b16 a[18:19], v232 offset:32320\n"
  "v_mfma_f32_16x16x32_bf16 v[24:27], a[4:7], a[68:71], v[24:27]\n"
  "s_add_u32 m0, s23, 90112\n"
  "s_nop 0\n"
  "buffer_load_dwordx4 v228, s[8:11], s20 offen offset:0 lds\n"
  "v_mfma_f32_16x16x32_bf16 v[28:31], a[4:7], a[72:75], v[28:31]\n"
  "s_waitcnt lgkmcnt(4)\n"
  "v_mfma_f32_16x16x32_bf16 v[32:35], a[8:11], a[60:63], v[32:35]\n"
  "ds_read_b64_tr_b16 a[20:21], v232 offset:35904\n"
  "v_mfma_f32_16x16x32_bf16 v[36:39], a[8:11], a[64:67], v[36:39]\n"
  "ds_read_b64_tr_b16 a[22:23], v232 offset:39488\n"
  "v_mfma_f32_16x16x32_bf16 v[40:43], a[8:11], a[68:71], v[40:43]\n"
  "buffer_load_dwordx4 v229, s[8:11], s20 offen offset:1024 lds\n"
  "v_mfma_f32_16x16x32_bf16 v[44:47], a[8:11], a[72:75], v[44:47]\n"
  "s_waitcnt lgkmcnt(4)\n"
  "v_mfma_f32_16x16x32_bf16 v[48:51], a[12:15], a[60:63], v[48:51]\n"
  "ds_read_b64_tr_b16 a[24:25], v232 offset:28768\n"
  "v_mfma_f32_16x16x32_bf16 v[52:55], a[12:15], a[64:67], v[52:55]\n"
  "ds_read_b64_tr_b16 a[26:27], v232 offset:32352\n"
  "v_mfma_f32_16x16x32_bf16 v[56:59], a[12:15], a[68:71], v[56:59]\n"
  "v_mfma_f32_16x16x32_bf16 v[60:63], a[12:15], a[72:75], v[60:63]\n"
  "s_waitcnt lgkmcnt(4)\n"
  "v_mfma_f32_16x16x32_bf16 v[64:67], a[16:19], a[60:63], v[64:67]\n"
  "ds_read_b64_tr_b16 a[0:1], v232 offset:35936\n"
  "v_mfma_f32_16x16x32_bf16 v[68:71], a[16:19], a[64:67], v[68:71]\n"
  "ds_read_b64_tr_b16 a[2:3], v232 offset:39520\n"
  "v_mfma_f32_16x16x32_bf16 v[72:75], a[16:19], a[68:71], v[72:75]\n"
  "buffer_load_dwordx4 v230, s[8:11], s20 offen offset:2048 lds\n"
  "v_mfma_f32_16x16x32_bf16 v[76:79], a[16:19], a[72:75], v[76:79]\n"
  "s_waitcnt lgkmcnt(4)\n"
  "v_mfma_f32_16x16x32_bf16 v[80:83], a[20:23], a[60:63], v[80:83]\n"
  "ds_read_b64_tr_b16 a[4:5], v232 offset:28800\n"
  "v_mfma_f32_16x16x32_bf16 v[84:87], a[20:23], a[64:67], v[84:87]\n"
  "ds_read_b64_tr_b16 a[6:7], v232 offset:32384\n"
  "v_mfma_f32_16x16x32_bf16 v[88:91], a[20:23], a[68:71], v[88:91]\n"
  "s_mov_b32 s21, 24576\n"
  "buffer_load_dwordx4 a[124:127], v231, s[12:15], s21 offen offset:0 sc1\n"
  "v_mfma_f32_16x16x32_bf16 v[92:95], a[20:23], a[72:75], v[92:95]\n"
  "s_waitcnt lgkmcnt(4)\n"
  "v_mfma_f32_16x16x32_bf16 v[96:99], a[24:27], a[60:63], v[96:99]\n"
  "ds_read_b64_tr_b16 a[8:9], v232 offset:35968\n"
  "v_mfma_f32_16x16x32_bf16 v[100:103], a[24:27], a[64:67], v[100:103]\n"
  "ds_read_b64_tr_b16 a[10:11], v232 offset:39552\n"
  "v_mfma_f32_16x16x32_bf16 v[104:107], a[24:27], a[68:71], v[104:107]\n"
  "v_mfma_f32_16x16x32_bf16 v[108:111], a[24:27], a[72:75], v[108:111]\n"
  "s_waitcnt lgkmcnt(4)\n"
  "v_mfma_f32_16x16x32_bf16 v[112:115], a[0:3], a[60:63], v[112:115]\n"
  "ds_read_b64_tr_b16 a[12:13], v232 offset:28832\n"
  "v_mfma_f32_16x16x32_bf16 v[116:119], a[0:3], a[64:67], v[116:119]\n"
  "ds_read_b64_tr_b16 a[14:15], v232 offset:32416\n"
  "v_mfma_f32_16x16x32_bf16 v[120:123], a[0:3], a[68:71], v[120:123]\n"
  "buffer_load_dwordx4 a[128:131], v231, s[12:15], s21 offen offset:1024 sc1\n"
  "v_mfma_f32_16x16x32_bf16 v[124:127], a[0:3], a[72:75], v[124:127]\n"
  "s_waitcnt lgkmcnt(4)\n"
  "v_mfma_f32_16x16x32_bf16 v[128:131], a[4:7], a[60:63], v[128:131]\n"
  "ds_read_b64_tr_b16 a[16:17], v232 offset:36000\n"
  "v_mfma_f32_16x16x32_bf16 v[132:135], a[4:7], a[64:67], v[132:135]\n"
  "ds_read_b64_tr_b16 a[18:19], v232 offset:39584\n"
  "v_mfma_f32_16x16x32_bf16 v[136:139], a[4:7], a[68:71], v[136:139]\n"
  "buffer_load_dwordx4 a[132:135], v231, s[12:15], s21 offen offset:2048 sc1\n"
  "v_mfma_f32_16x16x32_bf16 v[140:143], a[4:7], a[72:75], v[140:143]\n"
  "s_waitcnt lgkmcnt(4)\n"
  "v_mfma_f32_16x16x32_bf16 v[144:147], a[8:11], a[60:63], v[144:147]\n"
  "ds_read_b64_tr_b16 a[20:21], v232 offset:28864\n"
  "v_mfma_f32_16x16x32_bf16 v[148:151], a[8:11], a[64:67], v[148:151]\n"
  "ds_read_b64_tr_b16 a[22:23], v232 offset:32448\n"
  "v_mfma_f32_16x16x32_bf16 v[152:155], a[8:11], a[68:71], v[152:155]\n"
  "buffer_load_dwordx4 a[136:139], v231, s[12:15], s21 offen offset:3072 sc1\n"
  "v_mfma_f32_16x16x32_bf16 v[156:159], a[8:11], a[72:75], v[156:159]\n"
  "s_waitcnt lgkmcnt(4)\n"
  "v_mfma_f32_16x16x32_bf16 v[160:163], a[12:15], a[60:63], v[160:163]\n"
  "ds_read_b64_tr_b16 a[24:25], v232 offset:36032\n"
  "v_mfma_f32_16x16x32_bf16 v[164:167], a[12:15], a[64:67], v[164:167]\n"
  "ds_read_b64_tr_b16 a[26:27], v232 offset:39616\n"
  "v_mfma_f32_16x16x32_bf16 v[168:171], a[12:15], a[68:71], v[168:171]\n"
  "v_mfma_f32_16x16x32_bf16 v[172:175], a[12:15], a[72:75], v[172:175]\n"
  "s_waitcnt lgkmcnt(4)\n"
  "v_mfma_f32_16x16x32_bf16 v[176:179], a[16:19], a[60:63], v[176:179]\n"
  "ds_read_b64_tr_b16 a[0:1], v232 offset:43008\n"
  "v_mfma_f32_16x16x32_bf16 v[180:183], a[16:19], a[64:67], v[180:183]\n"
  "ds_read_b64_tr_b16 a[2:3], v232 offset:46592\n"
  "v_mfma_f32_16x16x32_bf16 v[184:187], a[16:19], a[68:71], v[184:187]\n"
  "s_mov_b32 s21, 28672\n"
  "buffer_load_dwordx4 a[140:143], v231, s[12:15], s21 offen offset:0 sc1\n"
  "v_mfma_f32_16x16x32_bf16 v[188:191], a[16:19], a[72:75], v[188:191]\n"
  "s_waitcnt lgkmcnt(4)\n"
  "v_mfma_f32_16x16x32_bf16 v[192:195], a[20:23], a[60:63], v[192:195]\n"
  "ds_read_b64_tr_b16 a[4:5], v232 offset:50176\n"
  "v_mfma_f32_16x16x32_bf16 v[196:199], a[20:23], a[64:67], v[196:199]\n"
  "ds_read_b64_tr_b16 a[6:7], v232 offset:53760\n"
  "v_mfma_f32_16x16x32_bf16 v[200:203], a[20:23], a[68:71], v[200:203]\n"
  "buffer_load_dwordx4 a[144:147], v231, s[12:15], s21 offen offset:1024 sc1\n"
  "v_mfma_f32_16x16x32_bf16 v[204:207], a[20:23], a[72:75], v[204:207]\n"
  "s_waitcnt lgkmcnt(4)\n"
  "v_mfma_f32_16x16x32_bf16 v[208:211], a[24:27], a[60:63], v[208:211]\n"
  "ds_read_b64_tr_b16 a[8:9], v232 offset:43040\n"
  "v_mfma_f32_16x16x32_bf16 v[212:215], a[24:27], a[64:67], v[212:215]\n"
  "ds_read_b64_tr_b16 a[10:11], v232 offset:46624\n"
  "v_mfma_f32_16x16x32_bf16 v[216:219], a[24:27], a[68:71], v[216:219]\n"
  "v_mfma_f32_16x16x32_bf16 v[220:223], a[24:27], a[72:75], v[220:223]\n"
  "s_waitcnt vmcnt(28) lgkmcnt(4)\n"
  "v_mfma_f32_16x16x32_bf16 v[0:3], a[0:3], a[76:79], v[0:3]\n"
  "ds_read_b64_tr_b16 a[12:13], v232 offset:50208\n"
  "v_mfma_f32_16x16x32_bf16 v[4:7], a[0:3], a[80:83], v[4:7]\n"
  "ds_read_b64_tr_b16 a[14:15], v232 offset:53792\n"
  "v_mfma_f32_16x16x32_bf16 v[8:11], a[0:3], a[84:87], v[8:11]\n"
  "buffer_load_dwordx4 a[148:151], v231, s[12:15], s21 offen offset:2048 sc1\n"
  "v_mfma_f32_16x16x32_bf16 v[12:15], a[0:3], a[88:91], v[12:15]\n"
  "s_waitcnt lgkmcnt(4)\n"
  "v_mfma_f32_16x16x32_bf16 v[16:19], a[4:7], a[76:79], v[16:19]\n"
  "ds_read_b64_tr_b16 a[16:17], v232 offset:43072\n"
  "v_mfma_f32_16x16x32_bf16 v[20:23], a[4:7], a[80:83], v[20:23]\n"
  "ds_read_b64_tr_b16 a[18:19], v232 offset:46656\n"
  "v_mfma_f32_16x16x32_bf16 v[24:27], a[4:7], a[84:87], v[24:27]\n"
  "buffer_load_dwordx4 a[152:155], v231, s[12:15], s21 offen offset:3072 sc1\n"
  "v_mfma_f32_16x16x32_bf16 v[28:31], a[4:7], a[88:91], v[28:31]\n"
  "s_waitcnt lgkmcnt(4)\n"
  "v_mfma_f32_16x16x32_bf16 v[32:35], a[8:11], a[76:79], v[32:35]\n"
  "ds_read_b64_tr_b16 a[20:21], v232 offset:50240\n"
  "v_mfma_f32_16x16x32_bf16 v[36:39], a[8:11], a[80:83], v[36:39]\n"
  "ds_read_b64_tr_b16 a[22:23], v232 offset:53824\n"
  "v_mfma_f32_16x16x32_bf16 v[40:43], a[8:11], a[84:87], v[40:43]\n"
  "s_mov_b32 s20, 1835008\n"
  "s_add_u32 m0, s23, 114688\n"
  "s_nop 0\n"
  "buffer_load_dwordx4 v224, s[8:11], s20 offen offset:0 lds\n"
  "v_mfma_f32_16x16x32_bf16 v[44:47], a[8:11], a[88:91], v[44:47]\n"
  "s_waitcnt lgkmcnt(4)\n"
  "v_mfma_f32_16x16x32_bf16 v[48:51], a[12:15], a[76:79], v[48:51]\n"
  "ds_read_b64_tr_b16 a[24:25], v232 offset:43104\n"
  "v_mfma_f32_16x16x32_bf16 v[52:55], a[12:15], a[80:83], v[52:55]\n"
  "ds_read_b64_tr_b16 a[26:27], v232 offset:46688\n"
  "v_mfma_f32_16x16x32_bf16 v[56:59], a[12:15], a[84:87], v[56:59]\n"
  "v_mfma_f32_16x16x32_bf16 v[60:63], a[12:15], a[88:91], v[60:63]\n"
  "s_waitcnt lgkmcnt(4)\n"
  "v_mfma_f32_16x16x32_bf16 v[64:67], a[16:19], a[76:79], v[64:67]\n"
  "ds_read_b64_tr_b16 a[0:1], v232 offset:50272\n"
  "v_mfma_f32_16x16x32_bf16 v[68:71], a[16:19], a[80:83], v[68:71]\n"
  "ds_read_b64_tr_b16 a[2:3], v232 offset:53856\n"
  "v_mfma_f32_16x16x32_bf16 v[72:75], a[16:19], a[84:87], v[72:75]\n"
  "buffer_load_dwordx4 v225, s[8:11], s20 offen offset:1024 lds\n"
  "v_mfma_f32_16x16x32_bf16 v[76:79], a[16:19], a[88:91], v[76:79]\n"
  "s_waitcnt lgkmcnt(4)\n"
  "v_mfma_f32_16x16x32_bf16 v[80:83], a[20:23], a[76:79], v[80:83]\n"
  "ds_read_b64_tr_b16 a[4:5], v232 offset:43136\n"
  "v_mfma_f32_16x16x32_bf16 v[84:87], a[20:23], a[80:83], v[84:87]\n"
  "ds_read_b64_tr_b16 a[6:7], v232 offset:46720\n"
  "v_mfma_f32_16x16x32_bf16 v[88:91], a[20:23], a[84:87], v[88:91]\n"
  "buffer_load_dwordx4 v226, s[8:11], s20 offen offset:2048 lds\n"
  "v_mfma_f32_16x16x32_bf16 v[92:95], a[20:23], a[88:91], v[92:95]\n"
  "s_waitcnt lgkmcnt(4)\n"
  "v_mfma_f32_16x16x32_bf16 v[96:99], a[24:27], a[76:79], v[96:99]\n"
  "ds_read_b64_tr_b16 a[8:9], v232 offset:50304\n"
  "v_mfma_f32_16x16x32_bf16 v[100:103], a[24:27], a[80:83], v[100:103]\n"
  "ds_read_b64_tr_b16 a[10:11], v232 offset:53888\n"
  "v_mfma_f32_16x16x32_bf16 v[104:107], a[24:27], a[84:87], v[104:107]\n"
  "v_mfma_f32_16x16x32_bf16 v[108:111], a[24:27], a[88:91], v[108:111]\n"
  "s_waitcnt lgkmcnt(4)\n"
  "v_mfma_f32_16x16x32_bf16 v[112:115], a[0:3], a[76:79], v[112:115]\n"
  "ds_read_b64_tr_b16 a[12:13], v232 offset:43168\n"
  "v_mfma_f32_16x16x32_bf16 v[116:119], a[0:3], a[80:83], v[116:119]\n"
  "ds_read_b64_tr_b16 a[14:15], v232 offset:46752\n"
  "v_mfma_f32_16x16x32_bf16 v[120:123], a[0:3], a[84:87], v[120:123]\n"
  "buffer_load_dwordx4 v227, s[8:11], s20 offen offset:3072 lds\n"
  "v_mfma_f32_16x16x32_bf16 v[124:127], a[0:3], a[88:91], v[124:127]\n"
  "s_waitcnt lgkmcnt(4)\n"
  "v_mfma_f32_16x16x32_bf16 v[128:131], a[4:7], a[76:79], v[128:131]\n"
  "ds_read_b64_tr_b16 a[16:17], v232 offset:50336\n"
  "v_mfma_f32_16x16x32_bf16 v[132:135], a[4:7], a[80:83], v[132:135]\n"
  "ds_read_b64_tr_b16 a[18:19], v232 offset:53920\n"
  "v_mfma_f32_16x16x32_bf16 v[136:139], a[4:7], a[84:87], v[136:139]\n"
  "s_add_u32 m0, s23, 118784\n"
  "s_nop 0\n"
  "buffer_load_dwordx4 v228, s[8:11], s20 offen offset:0 lds\n"
  "v_mfma_f32_16x16x32_bf16 v[140:143], a[4:7], a[88:91], v[140:143]\n"
  "s_waitcnt lgkmcnt(4)\n"
  "v_mfma_f32_16x16x32_bf16 v[144:147], a[8:11], a[76:79], v[144:147]\n"
  "ds_read_b64_tr_b16 a[20:21], v232 offset:43200\n"
  "v_mfma_f32_16x16x32_bf16 v[148:151], a[8:11], a[80:83], v[148:151]\n"
  "ds_read_b64_tr_b16 a[22:23], v232 offset:46784\n"
  "v_mfma_f32_16x16x32_bf16 v[152:155], a[8:11], a[84:87], v[152:155]\n"
  "buffer_load_dwordx4 v229, s[8:11], s20 offen offset:1024 lds\n"
  "v_mfma_f32_16x16x32_bf16 v[156:159], a[8:11], a[88:91], v[156:159]\n"
  "s_waitcnt lgkmcnt(4)\n"
  "v_mfma_f32_16x16x32_bf16 v[160:163], a[12:15], a[76:79], v[160:163]\n"
  "ds_read_b64_tr_b16 a[24:25], v232 offset:50368\n"
  "v_mfma_f32_16x16x32_bf16 v[164:167], a[12:15], a[80:83], v[164:167]\n"
  "ds_read_b64_tr_b16 a[26:27], v232 offset:53952\n"
  "v_mfma_f32_16x16x32_bf16 v[168:171], a[12:15], a[84:87], v[168:171]\n"
  "v_mfma_f32_16x16x32_bf16 v[172:175], a[12:15], a[88:91], v[172:175]\n"
  "s_waitcnt vmcnt(29)\n"
  "s_barrier\n"
  "s_waitcnt lgkmcnt(4)\n"
  "v_mfma_f32_16x16x32_bf16 v[176:179], a[16:19], a[76:79], v[176:179]\n"
  "ds_read_b64_tr_b16 a[0:1], v233 offset:0\n"
  "v_mfma_f32_16x16x32_bf16 v[180:183], a[16:19], a[80:83], v[180:183]\n"
  "ds_read_b64_tr_b16 a[2:3], v233 offset:3584\n"
  "v_mfma_f32_16x16x32_bf16 v[184:187], a[16:19], a[84:87], v[184:187]\n"
  "buffer_load_dwordx4 v230, s[8:11], s20 offen offset:2048 lds\n"
  "v_mfma_f32_16x16x32_bf16 v[188:191], a[16:19], a[88:91], v[188:191]\n"
  "s_waitcnt lgkmcnt(4)\n"
  "v_mfma_f32_16x16x32_bf16 v[192:195], a[20:23], a[76:79], v[192:195]\n"
  "ds_read_b64_tr_b16 a[4:5], v233 offset:7168\n"
  "v_mfma_f32_16x16x32_bf16 v[196:199], a[20:23], a[80:83], v[196:199]\n"
  "ds_read_b64_tr_b16 a[6:7], v233 offset:10752\n"
  "v_mfma_f32_16x16x32_bf16 v[200:203], a[20:23], a[84:87], v[200:203]\n"
  "s_mov_b32 s21, 32768\n"
  "buffer_load_dwordx4 a[28:31], v231, s[12:15], s21 offen offset:0 sc1\n"
  "v_mfma_f32_16x16x32_bf16 v[204:207], a[20:23], a[88:91], v[204:207]\n"
  "s_waitcnt lgkmcnt(4)\n"
  "v_mfma_f32_16x16x32_bf16 v[208:211], a[24:27], a[76:79], v[208:211]\n"
  "ds_read_b64_tr_b16 a[8:9], v233 offset:32\n"
  "v_mfma_f32_16x16x32_bf16 v[212:215], a[24:27], a[80:83], v[212:215]\n"
  "ds_read_b64_tr_b16 a[10:11], v233 offset:3616\n"
  "v_mfma_f32_16x16x32_bf16 v[216:219], a[24:27], a[84:87], v[216:219]\n"
  "v_mfma_f32_16x16x32_bf16 v[220:223], a[24:27], a[88:91], v[220:223]\n"
  "s_waitcnt vmcnt(27) lgkmcnt(4)\n"
  "v_mfma_f32_16x16x32_bf16 v[0:3], a[0:3], a[92:95], v[0:3]\n"
  "ds_read_b64_tr_b16 a[12:13], v233 offset:7200\n"
  "v_mfma_f32_16x16x32_bf16 v[4:7], a[0:3], a[96:99], v[4:7]\n"
  "ds_read_b64_tr_b16 a[14:15], v233 offset:10784\n"
  "v_mfma_f32_16x16x32_bf16 v[8:11], a[0:3], a[100:103], v[8:11]\n"
  "buffer_load_dwordx4 a[32:35], v231, s[12:15], s21 offen offset:1024 sc1\n"
  "v_mfma_f32_16x16x32_bf16 v[12:15], a[0:3], a[104:107], v[12:15]\n"
  "s_waitcnt lgkmcnt(4)\n"
  "v_mfma_f32_16x16x32_bf16 v[16:19], a[4:7], a[92:95], v[16:19]\n"
  "ds_read_b64_tr_b16 a[16:17], v233 offset:64\n"
  "v_mfma_f32_16x16x32_bf16 v[20:23], a[4:7], a[96:99], v[20:23]\n"
  "ds_read_b64_tr_b16 a[18:19], v233 offset:3648\n"
  "v_mfma_f32_16x16x32_bf16 v[24:27], a[4:7], a[100:103], v[24:27]\n"
  "buffer_load_dwordx4 a[36:39], v231, s[12:15], s21 offen offset:2048 sc1\n"
  "v_mfma_f32_16x16x32_bf16 v[28:31], a[4:7], a[104:107], v[28:31]\n"
  "s_waitcnt lgkmcnt(4)\n"
  "v_mfma_f32_16x16x32_bf16 v[32:35], a[8:11], a[92:95], v[32:35]\n"
  "ds_read_b64_tr_b16 a[20:21], v233 offset:7232\n"
  "v_mfma_f32_16x16x32_bf16 v[36:39], a[8:11], a[96:99], v[36:39]\n"
  "ds_read_b64_tr_b16 a[22:23], v233 offset:10816\n"
  "v_mfma_f32_16x16x32_bf16 v[40:43], a[8:11], a[100:103], v[40:43]\n"
  "buffer_load_dwordx4 a[40:43], v231, s[12:15], s21 offen offset:3072 sc1\n"
  "v_mfma_f32_16x16x32_bf16 v[44:47], a[8:11], a[104:107], v[44:47]\n"
  "s_waitcnt lgkmcnt(4)\n"
  "v_mfma_f32_16x16x32_bf16 v[48:51], a[12:15], a[92:95], v[48:51]\n"
  "ds_read_b64_tr_b16 a[24:25], v233 offset:96\n"
  "v_mfma_f32_16x16x32_bf16 v[52:55], a[12:15], a[96:99], v[52:55]\n"
  "ds_read_b64_tr_b16 a[26:27], v233 offset:3680\n"
  "v_mfma_f32_16x16x32_bf16 v[56:59], a[12:15], a[100:103], v[56:59]\n"
  "v_mfma_f32_16x16x32_bf16 v[60:63], a[12:15], a[104:107], v[60:63]\n"
  "s_waitcnt lgkmcnt(4)\n"
  "v_mfma_f32_16x16x32_bf16 v[64:67], a[16:19], a[92:95], v[64:67]\n"
  "ds_read_b64_tr_b16 a[0:1], v233 offset:7264\n"
  "v_mfma_f32_16x16x32_bf16 v[68:71], a[16:19], a[96:99], v[68:71]\n"
  "ds_read_b64_tr_b16 a[2:3], v233 offset:10848\n"
  "v_mfma_f32_16x16x32_bf16 v[72:75], a[16:19], a[100:103], v[72:75]\n"
  "s_mov_b32 s21, 36864\n"
  "buffer_load_dwordx4 a[44:47], v231, s[12:15], s21 offen offset:0 sc1\n"
  "v_mfma_f32_16x16x32_bf16 v[76:79], a[16:19], a[104:107], v[76:79]\n"
  "s_waitcnt lgkmcnt(4)\n"
  "v_mfma_f32_16x16x32_bf16 v[80:83], a[20:23], a[92:95], v[80:83]\n"
  "ds_read_b64_tr_b16 a[4:5], v233 offset:128\n"
  "v_mfma_f32_16x16x32_bf16 v[84:87], a[20:23], a[96:99], v[84:87]\n"
  "ds_read_b64_tr_b16 a[6:7], v233 offset:3712\n"
  "v_mfma_f32_16x16x32_bf16 v[88:91], a[20:23], a[100:103], v[88:91]\n"
  "buffer_load_dwordx4 a[48:51], v231, s[12:15], s21 offen offset:1024 sc1\n"
  "v_mfma_f32_16x16x32_bf16 v[92:95], a[20:23], a[104:107], v[92:95]\n"
  "s_waitcnt lgkmcnt(4)\n"
  "v_mfma_f32_16x16x32_bf16 v[96:99], a[24:27], a[92:95], v[96:99]\n"
  "ds_read_b64_tr_b16 a[8:9], v233 offset:7296\n"
  "v_mfma_f32_16x16x32_bf16 v[100:103], a[24:27], a[96:99], v[100:103]\n"
  "ds_read_b64_tr_b16 a[10:11], v233 offset:10880\n"
  "v_mfma_f32_16x16x32_bf16 v[104:107], a[24:27], a[100:103], v[104:107]\n"
  "v_mfma_f32_16x16x32_bf16 v[108:111], a[24:27], a[104:107], v[108:111]\n"
  "s_waitcnt lgkmcnt(4)\n"
  "v_mfma_f32_16x16x32_bf16 v[112:115], a[0:3], a[92:95], v[112:115]\n"
  "ds_read_b64_tr_b16 a[12:13], v233 offset:160\n"
  "v_mfma_f32_16x16x32_bf16 v[116:119], a[0:3], a[96:99], v[116:119]\n"
  "ds_read_b64_tr_b16 a[14:15], v233 offset:3744\n"
  "v_mfma_f32_16x16x32_bf16 v[120:123], a[0:3], a[100:103], v[120:123]\n"
  "buffer_load_dwordx4 a[52:55], v231, s[12:15], s21 offen offset:2048 sc1\n"
  "v_mfma_f32_16x16x32_bf16 v[124:127], a[0:3], a[104:107], v[124:127]\n"
  "s_waitcnt lgkmcnt(4)\n"
  "v_mfma_f32_16x16x32_bf16 v[128:131], a[4:7], a[92:95], v[128:131]\n"
  "ds_read_b64_tr_b16 a[16:17], v233 offset:7328\n"
  "v_mfma_f32_16x16x32_bf16 v[132:135], a[4:7], a[96:99], v[132:135]\n"
  "ds_read_b64_tr_b16 a[18:19], v233 offset:10912\n"
  "v_mfma_f32_16x16x32_bf16 v[136:139], a[4:7], a[100:103], v[136:139]\n"
  "buffer_load_dwordx4 a[56:59], v231, s[12:15], s21 offen offset:3072 sc1\n"
  "v_mfma_f32_16x16x32_bf16 v[140:143], a[4:7], a[104:107], v[140:143]\n"
  "s_waitcnt lgkmcnt(4)\n"
  "v_mfma_f32_16x16x32_bf16 v[144:147], a[8:11], a[92:95], v[144:147]\n"
  "ds_read_b64_tr_b16 a[20:21], v233 offset:192\n"
  "v_mfma_f32_16x16x32_bf16 v[148:151], a[8:11], a[96:99], v[148:151]\n"
  "ds_read_b64_tr_b16 a[22:23], v233 offset:3776\n"
  "v_mfma_f32_16x16x32_bf16 v[152:155], a[8:11], a[100:103], v[152:155]\n"
  "s_mov_b32 s20, 2293760\n"
  "s_add_u32 m0, s23, 0\n"
  "s_nop 0\n"
  "buffer_load_dwordx4 v224, s[8:11], s20 offen offset:0 lds\n"
  "v_mfma_f32_16x16x32_bf16 v[156:159], a[8:11], a[104:107], v[156:159]\n"
  "s_waitcnt lgkmcnt(4)\n"
  "v_mfma_f32_16x16x32_bf16 v[160:163], a[12:15], a[92:95], v[160:163]\n"
  "ds_read_b64_tr_b16 a[24:25], v233 offset:7360\n"
  "v_mfma_f32_16x16x32_bf16 v[164:167], a[12:15], a[96:99], v[164:167]\n"
  "ds_read_b64_tr_b16 a[26:27], v233 offset:10944\n"
  "v_mfma_f32_16x16x32_bf16 v[168:171], a[12:15], a[100:103], v[168:171]\n"
  "v_mfma_f32_16x16x32_bf16 v[172:175], a[12:15], a[104:107], v[172:175]\n"
  "s_waitcnt lgkmcnt(4)\n"
  "v_mfma_f32_16x16x32_bf16 v[176:179], a[16:19], a[92:95], v[176:179]\n"
  "ds_read_b64_tr_b16 a[0:1], v233 offset:14336\n"
  "v_mfma_f32_16x16x32_bf16 v[180:183], a[16:19], a[96:99], v[180:183]\n"
  "ds_read_b64_tr_b16 a[2:3], v233 offset:17920\n"
  "v_mfma_f32_16x16x32_bf16 v[184:187], a[16:19], a[100:103], v[184:187]\n"
  "buffer_load_dwordx4 v225, s[8:11], s20 offen offset:1024 lds\n"
  "v_mfma_f32_16x16x32_bf16 v[188:191], a[16:19], a[104:107], v[188:191]\n"
  "s_waitcnt lgkmcnt(4)\n"
  "v_mfma_f32_16x16x32_bf16 v[192:195], a[20:23], a[92:95], v[192:195]\n"
  "ds_read_b64_tr_b16 a[4:5], v233 offset:21504\n"
  "v_mfma_f32_16x16x32_bf16 v[196:199], a[20:23], a[96:99], v[196:199]\n"
  "ds_read_b64_tr_b16 a[6:7], v233 offset:25088\n"
  "v_mfma_f32_16x16x32_bf16 v[200:203], a[20:23], a[100:103], v[200:203]\n"
  "buffer_load_dwordx4 v226, s[8:11], s20 offen offset:2048 lds\n"
  "v_mfma_f32_16x16x32_bf16 v[204:207], a[20:23], a[104:107], v[204:207]\n"
  "s_waitcnt lgkmcnt(4)\n"
  "v_mfma_f32_16x16x32_bf16 v[208:211], a[24:27], a[92:95], v[208:211]\n"
  "ds_read_b64_tr_b16 a[8:9], v233 offset:14368\n"
  "v_mfma_f32_16x16x32_bf16 v[212:215], a[24:27], a[96:99], v[212:215]\n"
  "ds_read_b64_tr_b16 a[10:11], v233 offset:17952\n"
  "v_mfma_f32_16x16x32_bf16 v[216:219], a[24:27], a[100:103], v[216:219]\n"
  "v_mfma_f32_16x16x32_bf16 v[220:223], a[24:27], a[104:107], v[220:223]\n"
  "s_waitcnt vmcnt(33) lgkmcnt(4)\n"
  "v_mfma_f32_16x16x32_bf16 v[0:3], a[0:3], a[108:111], v[0:3]\n"
  "ds_read_b64_tr_b16 a[12:13], v233 offset:21536\n"
  "v_mfma_f32_16x16x32_bf16 v[4:7], a[0:3], a[112:115], v[4:7]\n"
  "ds_read_b64_tr_b16 a[14:15], v233 offset:25120\n"
  "v_mfma_f32_16x16x32_bf16 v[8:11], a[0:3], a[116:119], v[8:11]\n"
  "buffer_load_dwordx4 v227, s[8:11], s20 offen offset:3072 lds\n"
  "v_mfma_f32_16x16x32_bf16 v[12:15], a[0:3], a[120:123], v[12:15]\n"
  "s_waitcnt lgkmcnt(4)\n"
  "v_mfma_f32_16x16x32_bf16 v[16:19], a[4:7], a[108:111], v[16:19]\n"
  "ds_read_b64_tr_b16 a[16:17], v233 offset:14400\n"
  "v_mfma_f32_16x16x32_bf16 v[20:23], a[4:7], a[112:115], v[20:23]\n"
  "ds_read_b64_tr_b16 a[18:19], v233 offset:17984\n"
  "v_mfma_f32_16x16x32_bf16 v[24:27], a[4:7], a[116:119], v[24:27]\n"
  "s_add_u32 m0, s23, 4096\n"
  "s_nop 0\n"
  "buffer_load_dwordx4 v228, s[8:11], s20 offen offset:0 lds\n"
  "v_mfma_f32_16x16x32_bf16 v[28:31], a[4:7], a[120:123], v[28:31]\n"
  "s_waitcnt lgkmcnt(4)\n"
  "v_mfma_f32_16x16x32_bf16 v[32:35], a[8:11], a[108:111], v[32:35]\n"
  "ds_read_b64_tr_b16 a[20:21], v233 offset:21568\n"
  "v_mfma_f32_16x16x32_bf16 v[36:39], a[8:11], a[112:115], v[36:39]\n"
  "ds_read_b64_tr_b16 a[22:23], v233 offset:25152\n"
  "v_mfma_f32_16x16x32_bf16 v[40:43], a[8:11], a[116:119], v[40:43]\n"
  "buffer_load_dwordx4 v229, s[8:11], s20 offen offset:1024 lds\n"
  "v_mfma_f32_16x16x32_bf16 v[44:47], a[8:11], a[120:123], v[44:47]\n"
  "s_waitcnt lgkmcnt(4)\n"
  "v_mfma_f32_16x16x32_bf16 v[48:51], a[12:15], a[108:111], v[48:51]\n"
  "ds_read_b64_tr_b16 a[24:25], v233 offset:14432\n"
  "v_mfma_f32_16x16x32_bf16 v[52:55], a[12:15], a[112:115], v[52:55]\n"
  "ds_read_b64_tr_b16 a[26:27], v233 offset:18016\n"
  "v_mfma_f32_16x16x32_bf16 v[56:59], a[12:15], a[116:119], v[56:59]\n"
  "v_mfma_f32_16x16x32_bf16 v[60:63], a[12:15], a[120:123], v[60:63]\n"
  "s_waitcnt lgkmcnt(4)\n"
  "v_mfma_f32_16x16x32_bf16 v[64:67], a[16:19], a[108:111], v[64:67]\n"
  "ds_read_b64_tr_b16 a[0:1], v233 offset:21600\n"
  "v_mfma_f32_16x16x32_bf16 v[68:71], a[16:19], a[112:115], v[68:71]\n"
  "ds_read_b64_tr_b16 a[2:3], v233 offset:25184\n"
  "v_mfma_f32_16x16x32_bf16 v[72:75], a[16:19], a[116:119], v[72:75]\n"
  "buffer_load_dwordx4 v230, s[8:11], s20 offen offset:2048 lds\n"
  "v_mfma_f32_16x16x32_bf16 v[76:79], a[16:19], a[120:123], v[76:79]\n"
  "s_waitcnt lgkmcnt(4)\n"
  "v_mfma_f32_16x16x32_bf16 v[80:83], a[20:23], a[108:111], v[80:83]\n"
  "ds_read_b64_tr_b16 a[4:5], v233 offset:14464\n"
  "v_mfma_f32_16x16x32_bf16 v[84:87], a[20:23], a[112:115], v[84:87]\n"
  "ds_read_b64_tr_b16 a[6:7], v233 offset:18048\n"
  "v_mfma_f32_16x16x32_bf16 v[88:91], a[20:23], a[116:119], v[88:91]\n"
  "s_mov_b32 s21, 40960\n"
  "buffer_load_dwordx4 a[60:63], v231, s[12:15], s21 offen offset:0 sc1\n"
  "v_mfma_f32_16x16x32_bf16 v[92:95], a[20:23], a[120:123], v[92:95]\n"
  "s_waitcnt lgkmcnt(4)\n"
  "v_mfma_f32_16x16x32_bf16 v[96:99], a[24:27], a[108:111], v[96:99]\n"
  "ds_read_b64_tr_b16 a[8:9], v233 offset:21632\n"
  "v_mfma_f32_16x16x32_bf16 v[100:103], a[24:27], a[112:115], v[100:103]\n"
  "ds_read_b64_tr_b16 a[10:11], v233 offset:25216\n"
  "v_mfma_f32_16x16x32_bf16 v[104:107], a[24:27], a[116:119], v[104:107]\n"
  "v_mfma_f32_16x16x32_bf16 v[108:111], a[24:27], a[120:123], v[108:111]\n"
  "s_waitcnt lgkmcnt(4)\n"
  "v_mfma_f32_16x16x32_bf16 v[112:115], a[0:3], a[108:111], v[112:115]\n"
  "ds_read_b64_tr_b16 a[12:13], v233 offset:14496\n"
  "v_mfma_f32_16x16x32_bf16 v[116:119], a[0:3], a[112:115], v[116:119]\n"
  "ds_read_b64_tr_b16 a[14:15], v233 offset:18080\n"
  "v_mfma_f32_16x16x32_bf16 v[120:123], a[0:3], a[116:119], v[120:123]\n"
  "buffer_load_dwordx4 a[64:67], v231, s[12:15], s21 offen offset:1024 sc1\n"
  "v_mfma_f32_16x16x32_bf16 v[124:127], a[0:3], a[120:123], v[124:127]\n"
  "s_waitcnt lgkmcnt(4)\n"
  "v_mfma_f32_16x16x32_bf16 v[128:131], a[4:7], a[108:111], v[128:131]\n"
  "ds_read_b64_tr_b16 a[16:17], v233 offset:21664\n"
  "v_mfma_f32_16x16x32_bf16 v[132:135], a[4:7], a[112:115], v[132:135]\n"
  "ds_read_b64_tr_b16 a[18:19], v233 offset:25248\n"
  "v_mfma_f32_16x16x32_bf16 v[136:139], a[4:7], a[116:119], v[136:139]\n"
  "buffer_load_dwordx4 a[68:71], v231, s[12:15], s21 offen offset:2048 sc1\n"
  "v_mfma_f32_16x16x32_bf16 v[140:143], a[4:7], a[120:123], v[140:143]\n"
  "s_waitcnt lgkmcnt(4)\n"
  "v_mfma_f32_16x16x32_bf16 v[144:147], a[8:11], a[108:111], v[144:147]\n"
  "ds_read_b64_tr_b16 a[20:21], v233 offset:14528\n"
  "v_mfma_f32_16x16x32_bf16 v[148:151], a[8:11], a[112:115], v[148:151]\n"
  "ds_read_b64_tr_b16 a[22:23], v233 offset:18112\n"
  "v_mfma_f32_16x16x32_bf16 v[152:155], a[8:11], a[116:119], v[152:155]\n"
  "buffer_load_dwordx4 a[72:75], v231, s[12:15], s21 offen offset:3072 sc1\n"
  "v_mfma_f32_16x16x32_bf16 v[156:159], a[8:11], a[120:123], v[156:159]\n"
  "s_waitcnt lgkmcnt(4)\n"
  "v_mfma_f32_16x16x32_bf16 v[160:163], a[12:15], a[108:111], v[160:163]\n"
  "ds_read_b64_tr_b16 a[24:25], v233 offset:21696\n"
  "v_mfma_f32_16x16x32_bf16 v[164:167], a[12:15], a[112:115], v[164:167]\n"
  "ds_read_b64_tr_b16 a[26:27], v233 offset:25280\n"
  "v_mfma_f32_16x16x32_bf16 v[168:171], a[12:15], a[116:119], v[168:171]\n"
  "v_mfma_f32_16x16x32_bf16 v[172:175], a[12:15], a[120:123], v[172:175]\n"
  "s_waitcnt vmcnt(34)\n"
  "s_barrier\n"
  "s_waitcnt lgkmcnt(4)\n"
  "v_mfma_f32_16x16x32_bf16 v[176:179], a[16:19], a[108:111], v[176:179]\n"
  "ds_read_b64_tr_b16 a[0:1], v233 offset:28672\n"
  "v_mfma_f32_16x16x32_bf16 v[180:183], a[16:19], a[112:115], v[180:183]\n"
  "ds_read_b64_tr_b16 a[2:3], v233 offset:32256\n"
  "v_mfma_f32_16x16x32_bf16 v[184:187], a[16:19], a[116:119], v[184:187]\n"
  "s_mov_b32 s21, 45056\n"
  "buffer_load_dwordx4 a[76:79], v231, s[12:15], s21 offen offset:0 sc1\n"
  "v_mfma_f32_16x16x32_bf16 v[188:191], a[16:19], a[120:123], v[188:191]\n"
  "s_waitcnt lgkmcnt(4)\n"
  "v_mfma_f32_16x16x32_bf16 v[192:195], a[20:23], a[108:111], v[192:195]\n"
  "ds_read_b64_tr_b16 a[4:5], v233 offset:35840\n"
  "v_mfma_f32_16x16x32_bf16 v[196:199], a[20:23], a[112:115], v[196:199]\n"
  "ds_read_b64_tr_b16 a[6:7], v233 offset:39424\n"
  "v_mfma_f32_16x16x32_bf16 v[200:203], a[20:23], a[116:119], v[200:203]\n"
  "buffer_load_dwordx4 a[80:83], v231, s[12:15], s21 offen offset:1024 sc1\n"
  "v_mfma_f32_16x16x32_bf16 v[204:207], a[20:23], a[120:123], v[204:207]\n"
  "s_waitcnt lgkmcnt(4)\n"
  "v_mfma_f32_16x16x32_bf16 v[208:211], a[24:27], a[108:111], v[208:211]\n"
  "ds_read_b64_tr_b16 a[8:9], v233 offset:28704\n"
  "v_mfma_f32_16x16x32_bf16 v[212:215], a[24:27], a[112:115], v[212:215]\n"
  "ds_read_b64_tr_b16 a[10:11], v233 offset:32288\n"
  "v_mfma_f32_16x16x32_bf16 v[216:219], a[24:27], a[116:119], v[216:219]\n"
  "v_mfma_f32_16x16x32_bf16 v[220:223], a[24:27], a[120:123], v[220:223]\n"
  "s_waitcnt vmcnt(32) lgkmcnt(4)\n"
  "v_mfma_f32_16x16x32_bf16 v[0:3], a[0:3], a[124:127], v[0:3]\n"
  "ds_read_b64_tr_b16 a[12:13], v233 offset:35872\n"
  "v_mfma_f32_16x16x32_bf16 v[4:7], a[0:3], a[128:131], v[4:7]\n"
  "ds_read_b64_tr_b16 a[14:15], v233 offset:39456\n"
  "v_mfma_f32_16x16x32_bf16 v[8:11], a[0:3], a[132:135], v[8:11]\n"
  "buffer_load_dwordx4 a[84:87], v231, s[12:15], s21 offen offset:2048 sc1\n"
  "v_mfma_f32_16x16x32_bf16 v[12:15], a[0:3], a[136:139], v[12:15]\n"
  "s_waitcnt lgkmcnt(4)\n"
  "v_mfma_f32_16x16x32_bf16 v[16:19], a[4:7], a[124:127], v[16:19]\n"
  "ds_read_b64_tr_b16 a[16:17], v233 offset:28736\n"
  "v_mfma_f32_16x16x32_bf16 v[20:23], a[4:7], a[128:131], v[20:23]\n"
  "ds_read_b64_tr_b16 a[18:19], v233 offset:32320\n"
  "v_mfma_f32_16x16x32_bf16 v[24:27], a[4:7], a[132:135], v[24:27]\n"
  "buffer_load_dwordx4 a[88:91], v231, s[12:15], s21 offen offset:3072 sc1\n"
  "v_mfma_f32_16x16x32_bf16 v[28:31], a[4:7], a[136:139], v[28:31]\n"
  "s_waitcnt lgkmcnt(4)\n"
  "v_mfma_f32_16x16x32_bf16 v[32:35], a[8:11], a[124:127], v[32:35]\n"
  "ds_read_b64_tr_b16 a[20:21], v233 offset:35904\n"
  "v_mfma_f32_16x16x32_bf16 v[36:39], a[8:11], a[128:131], v[36:39]\n"
  "ds_read_b64_tr_b16 a[22:23], v233 offset:39488\n"
  "v_mfma_f32_16x16x32_bf16 v[40:43], a[8:11], a[132:135], v[40:43]\n"
  "s_mov_b32 s20, 2752512\n"
  "s_add_u32 m0, s23, 28672\n"
  "s_nop 0\n"
  "buffer_load_dwordx4 v224, s[8:11], s20 offen offset:0 lds\n"
  "v_mfma_f32_16x16x32_bf16 v[44:47], a[8:11], a[136:139], v[44:47]\n"
  "s_waitcnt lgkmcnt(4)\n"
  "v_mfma_f32_16x16x32_bf16 v[48:51], a[12:15], a[124:127], v[48:51]\n"
  "ds_read_b64_tr_b16 a[24:25], v233 offset:28768\n"
  "v_mfma_f32_16x16x32_bf16 v[52:55], a[12:15], a[128:131], v[52:55]\n"
  "ds_read_b64_tr_b16 a[26:27], v233 offset:32352\n"
  "v_mfma_f32_16x16x32_bf16 v[56:59], a[12:15], a[132:135], v[56:59]\n"
  "v_mfma_f32_16x16x32_bf16 v[60:63], a[12:15], a[136:139], v[60:63]\n"
  "s_waitcnt lgkmcnt(4)\n"
  "v_mfma_f32_16x16x32_bf16 v[64:67], a[16:19], a[124:127], v[64:67]\n"
  "ds_read_b64_tr_b16 a[0:1], v233 offset:35936\n"
  "v_mfma_f32_16x16x32_bf16 v[68:71], a[16:19], a[128:131], v[68:71]\n"
  "ds_read_b64_tr_b16 a[2:3], v233 offset:39520\n"
  "v_mfma_f32_16x16x32_bf16 v[72:75], a[16:19], a[132:135], v[72:75]\n"
  "buffer_load_dwordx4 v225, s[8:11], s20 offen offset:1024 lds\n"
  "v_mfma_f32_16x16x32_bf16 v[76:79], a[16:19], a[136:139], v[76:79]\n"
  "s_waitcnt lgkmcnt(4)\n"
  "v_mfma_f32_16x16x32_bf16 v[80:83], a[20:23], a[124:127], v[80:83]\n"
  "ds_read_b64_tr_b16 a[4:5], v233 offset:28800\n"
  "v_mfma_f32_16x16x32_bf16 v[84:87], a[20:23], a[128:131], v[84:87]\n"
  "ds_read_b64_tr_b16 a[6:7], v233 offset:32384\n"
  "v_mfma_f32_16x16x32_bf16 v[88:91], a[20:23], a[132:135], v[88:91]\n"
  "buffer_load_dwordx4 v226, s[8:11], s20 offen offset:2048 lds\n"
  "v_mfma_f32_16x16x32_bf16 v[92:95], a[20:23], a[136:139], v[92:95]\n"
  "s_waitcnt lgkmcnt(4)\n"
  "v_mfma_f32_16x16x32_bf16 v[96:99], a[24:27], a[124:127], v[96:99]\n"
  "ds_read_b64_tr_b16 a[8:9], v233 offset:35968\n"
  "v_mfma_f32_16x16x32_bf16 v[100:103], a[24:27], a[128:131], v[100:103]\n"
  "ds_read_b64_tr_b16 a[10:11], v233 offset:39552\n"
  "v_mfma_f32_16x16x32_bf16 v[104:107], a[24:27], a[132:135], v[104:107]\n"
  "v_mfma_f32_16x16x32_bf16 v[108:111], a[24:27], a[136:139], v[108:111]\n"
  "s_waitcnt lgkmcnt(4)\n"
  "v_mfma_f32_16x16x32_bf16 v[112:115], a[0:3], a[124:127], v[112:115]\n"
  "ds_read_b64_tr_b16 a[12:13], v233 offset:28832\n"
  "v_mfma_f32_16x16x32_bf16 v[116:119], a[0:3], a[128:131], v[116:119]\n"
  "ds_read_b64_tr_b16 a[14:15], v233 offset:32416\n"
  "v_mfma_f32_16x16x32_bf16 v[120:123], a[0:3], a[132:135], v[120:123]\n"
  "buffer_load_dwordx4 v227, s[8:11], s20 offen offset:3072 lds\n"
  "v_mfma_f32_16x16x32_bf16 v[124:127], a[0:3], a[136:139], v[124:127]\n"
  "s_waitcnt lgkmcnt(4)\n"
  "v_mfma_f32_16x16x32_bf16 v[128:131], a[4:7], a[124:127], v[128:131]\n"
  "ds_read_b64_tr_b16 a[16:17], v233 offset:36000\n"
  "v_mfma_f32_16x16x32_bf16 v[132:135], a[4:7], a[128:131], v[132:135]\n"
  "ds_read_b64_tr_b16 a[18:19], v233 offset:39584\n"
  "v_mfma_f32_16x16x32_bf16 v[136:139], a[4:7], a[132:135], v[136:139]\n"
  "s_add_u32 m0, s23, 32768\n"
  "s_nop 0\n"
  "buffer_load_dwordx4 v228, s[8:11], s20 offen offset:0 lds\n"
  "v_mfma_f32_16x16x32_bf16 v[140:143], a[4:7], a[136:139], v[140:143]\n"
  "s_waitcnt lgkmcnt(4)\n"
  "v_mfma_f32_16x16x32_bf16 v[144:147], a[8:11], a[124:127], v[144:147]\n"
  "ds_read_b64_tr_b16 a[20:21], v233 offset:28864\n"
  "v_mfma_f32_16x16x32_bf16 v[148:151], a[8:11], a[128:131], v[148:151]\n"
  "ds_read_b64_tr_b16 a[22:23], v233 offset:32448\n"
  "v_mfma_f32_16x16x32_bf16 v[152:155], a[8:11], a[132:135], v[152:155]\n"
  "buffer_load_dwordx4 v229, s[8:11], s20 offen offset:1024 lds\n"
  "v_mfma_f32_16x16x32_bf16 v[156:159], a[8:11], a[136:139], v[156:159]\n"
  "s_waitcnt lgkmcnt(4)\n"
  "v_mfma_f32_16x16x32_bf16 v[160:163], a[12:15], a[124:127], v[160:163]\n"
  "ds_read_b64_tr_b16 a[24:25], v233 offset:36032\n"
  "v_mfma_f32_16x16x32_bf16 v[164:167], a[12:15], a[128:131], v[164:167]\n"
  "ds_read_b64_tr_b16 a[26:27], v233 offset:39616\n"
  "v_mfma_f32_16x16x32_bf16 v[168:171], a[12:15], a[132:135], v[168:171]\n"
  "v_mfma_f32_16x16x32_bf16 v[172:175], a[12:15], a[136:139], v[172:175]\n"
  "s_waitcnt lgkmcnt(4)\n"
  "v_mfma_f32_16x16x32_bf16 v[176:179], a[16:19], a[124:127], v[176:179]\n"
  "ds_read_b64_tr_b16 a[0:1], v233 offset:43008\n"
  "v_mfma_f32_16x16x32_bf16 v[180:183], a[16:19], a[128:131], v[180:183]\n"
  "ds_read_b64_tr_b16 a[2:3], v233 offset:46592\n"
  "v_mfma_f32_16x16x32_bf16 v[184:187], a[16:19], a[132:135], v[184:187]\n"
  "buffer_load_dwordx4 v230, s[8:11], s20 offen offset:2048 lds\n"
  "v_mfma_f32_16x16x32_bf16 v[188:191], a[16:19], a[136:139], v[188:191]\n"
  "s_waitcnt lgkmcnt(4)\n"
  "v_mfma_f32_16x16x32_bf16 v[192:195], a[20:23], a[124:127], v[192:195]\n"
  "ds_read_b64_tr_b16 a[4:5], v233 offset:50176\n"
  "v_mfma_f32_16x16x32_bf16 v[196:199], a[20:23], a[128:131], v[196:199]\n"
  "ds_read_b64_tr_b16 a[6:7], v233 offset:53760\n"
  "v_mfma_f32_16x16x32_bf16 v[200:203], a[20:23], a[132:135], v[200:203]\n"
  "s_mov_b32 s21, 49152\n"
  "buffer_load_dwordx4 a[92:95], v231, s[12:15], s21 offen offset:0 sc1\n"
  "v_mfma_f32_16x16x32_bf16 v[204:207], a[20:23], a[136:139], v[204:207]\n"
  "s_waitcnt lgkmcnt(4)\n"
  "v_mfma_f32_16x16x32_bf16 v[208:211], a[24:27], a[124:127], v[208:211]\n"
  "ds_read_b64_tr_b16 a[8:9], v233 offset:43040\n"
  "v_mfma_f32_16x16x32_bf16 v[212:215], a[24:27], a[128:131], v[212:215]\n"
  "ds_read_b64_tr_b16 a[10:11], v233 offset:46624\n"
  "v_mfma_f32_16x16x32_bf16 v[216:219], a[24:27], a[132:135], v[216:219]\n"
  "v_mfma_f32_16x16x32_bf16 v[220:223], a[24:27], a[136:139], v[220:223]\n"
  "s_waitcnt vmcnt(38) lgkmcnt(4)\n"
  "v_mfma_f32_16x16x32_bf16 v[0:3], a[0:3], a[140:143], v[0:3]\n"
  "ds_read_b64_tr_b16 a[12:13], v233 offset:50208\n"
  "v_mfma_f32_16x16x32_bf16 v[4:7], a[0:3], a[144:147], v[4:7]\n"
  "ds_read_b64_tr_b16 a[14:15], v233 offset:53792\n"
  "v_mfma_f32_16x16x32_bf16 v[8:11], a[0:3], a[148:151], v[8:11]\n"
  "buffer_load_dwordx4 a[96:99], v231, s[12:15], s21 offen offset:1024 sc1\n"
  "v_mfma_f32_16x16x32_bf16 v[12:15], a[0:3], a[152:155], v[12:15]\n"
  "s_waitcnt lgkmcnt(4)\n"
  "v_mfma_f32_16x16x32_bf16 v[16:19], a[4:7], a[140:143], v[16:19]\n"
  "ds_read_b64_tr_b16 a[16:17], v233 offset:43072\n"
  "v_mfma_f32_16x16x32_bf16 v[20:23], a[4:7], a[144:147], v[20:23]\n"
  "ds_read_b64_tr_b16 a[18:19], v233 offset:46656\n"
  "v_mfma_f32_16x16x32_bf16 v[24:27], a[4:7], a[148:151], v[24:27]\n"
  "buffer_load_dwordx4 a[100:103], v231, s[12:15], s21 offen offset:2048 sc1\n"
  "v_mfma_f32_16x16x32_bf16 v[28:31], a[4:7], a[152:155], v[28:31]\n"
  "s_waitcnt lgkmcnt(4)\n"
  "v_mfma_f32_16x16x32_bf16 v[32:35], a[8:11], a[140:143], v[32:35]\n"
  "ds_read_b64_tr_b16 a[20:21], v233 offset:50240\n"
  "v_mfma_f32_16x16x32_bf16 v[36:39], a[8:11], a[144:147], v[36:39]\n"
  "ds_read_b64_tr_b16 a[22:23], v233 offset:53824\n"
  "v_mfma_f32_16x16x32_bf16 v[40:43], a[8:11], a[148:151], v[40:43]\n"
  "buffer_load_dwordx4 a[104:107], v231, s[12:15], s21 offen offset:3072 sc1\n"
  "v_mfma_f32_16x16x32_bf16 v[44:47], a[8:11], a[152:155], v[44:47]\n"
  "s_waitcnt lgkmcnt(4)\n"
  "v_mfma_f32_16x16x32_bf16 v[48:51], a[12:15], a[140:143], v[48:51]\n"
  "ds_read_b64_tr_b16 a[24:25], v233 offset:43104\n"
  "v_mfma_f32_16x16x32_bf16 v[52:55], a[12:15], a[144:147], v[52:55]\n"
  "ds_read_b64_tr_b16 a[26:27], v233 offset:46688\n"
  "v_mfma_f32_16x16x32_bf16 v[56:59], a[12:15], a[148:151], v[56:59]\n"
  "v_mfma_f32_16x16x32_bf16 v[60:63], a[12:15], a[152:155], v[60:63]\n"
  "s_waitcnt lgkmcnt(4)\n"
  "v_mfma_f32_16x16x32_bf16 v[64:67], a[16:19], a[140:143], v[64:67]\n"
  "ds_read_b64_tr_b16 a[0:1], v233 offset:50272\n"
  "v_mfma_f32_16x16x32_bf16 v[68:71], a[16:19], a[144:147], v[68:71]\n"
  "ds_read_b64_tr_b16 a[2:3], v233 offset:53856\n"
  "v_mfma_f32_16x16x32_bf16 v[72:75], a[16:19], a[148:151], v[72:75]\n"
  "s_mov_b32 s21, 53248\n"
  "buffer_load_dwordx4 a[108:111], v231, s[12:15], s21 offen offset:0 sc1\n"
  "v_mfma_f32_16x16x32_bf16 v[76:79], a[16:19], a[152:155], v[76:79]\n"
  "s_waitcnt lgkmcnt(4)\n"
  "v_mfma_f32_16x16x32_bf16 v[80:83], a[20:23], a[140:143], v[80:83]\n"
  "ds_read_b64_tr_b16 a[4:5], v233 offset:43136\n"
  "v_mfma_f32_16x16x32_bf16 v[84:87], a[20:23], a[144:147], v[84:87]\n"
  "ds_read_b64_tr_b16 a[6:7], v233 offset:46720\n"
  "v_mfma_f32_16x16x32_bf16 v[88:91], a[20:23], a[148:151], v[88:91]\n"
  "buffer_load_dwordx4 a[112:115], v231, s[12:15], s21 offen offset:1024 sc1\n"
  "v_mfma_f32_16x16x32_bf16 v[92:95], a[20:23], a[152:155], v[92:95]\n"
  "s_waitcnt lgkmcnt(4)\n"
  "v_mfma_f32_16x16x32_bf16 v[96:99], a[24:27], a[140:143], v[96:99]\n"
  "ds_read_b64_tr_b16 a[8:9], v233 offset:50304\n"
  "v_mfma_f32_16x16x32_bf16 v[100:103], a[24:27], a[144:147], v[100:103]\n"
  "ds_read_b64_tr_b16 a[10:11], v233 offset:53888\n"
  "v_mfma_f32_16x16x32_bf16 v[104:107], a[24:27], a[148:151], v[104:107]\n"
  "v_mfma_f32_16x16x32_bf16 v[108:111], a[24:27], a[152:155], v[108:111]\n"
  "s_waitcnt lgkmcnt(4)\n"
  "v_mfma_f32_16x16x32_bf16 v[112:115], a[0:3], a[140:143], v[112:115]\n"
  "ds_read_b64_tr_b16 a[12:13], v233 offset:43168\n"
  "v_mfma_f32_16x16x32_bf16 v[116:119], a[0:3], a[144:147], v[116:119]\n"
  "ds_read_b64_tr_b16 a[14:15], v233 offset:46752\n"
  "v_mfma_f32_16x16x32_bf16 v[120:123], a[0:3], a[148:151], v[120:123]\n"
  "buffer_load_dwordx4 a[116:119], v231, s[12:15], s21 offen offset:2048 sc1\n"
  "v_mfma_f32_16x16x32_bf16 v[124:127], a[0:3], a[152:155], v[124:127]\n"
  "s_waitcnt lgkmcnt(4)\n"
  "v_mfma_f32_16x16x32_bf16 v[128:131], a[4:7], a[140:143], v[128:131]\n"
  "ds_read_b64_tr_b16 a[16:17], v233 offset:50336\n"
  "v_mfma_f32_16x16x32_bf16 v[132:135], a[4:7], a[144:147], v[132:135]\n"
  "ds_read_b64_tr_b16 a[18:19], v233 offset:53920\n"
  "v_mfma_f32_16x16x32_bf16 v[136:139], a[4:7], a[148:151], v[136:139]\n"
  "buffer_load_dwordx4 a[120:123], v231, s[12:15], s21 offen offset:3072 sc1\n"
  "v_mfma_f32_16x16x32_bf16 v[140:143], a[4:7], a[152:155], v[140:143]\n"
  "s_waitcnt lgkmcnt(4)\n"
  "v_mfma_f32_16x16x32_bf16 v[144:147], a[8:11], a[140:143], v[144:147]\n"
  "ds_read_b64_tr_b16 a[20:21], v233 offset:43200\n"
  "v_mfma_f32_16x16x32_bf16 v[148:151], a[8:11], a[144:147], v[148:151]\n"
  "ds_read_b64_tr_b16 a[22:23], v233 offset:46784\n"
  "v_mfma_f32_16x16x32_bf16 v[152:155], a[8:11], a[148:151], v[152:155]\n"
  "v_mfma_f32_16x16x32_bf16 v[156:159], a[8:11], a[152:155], v[156:159]\n"
  "s_waitcnt lgkmcnt(4)\n"
  "v_mfma_f32_16x16x32_bf16 v[160:163], a[12:15], a[140:143], v[160:163]\n"
  "ds_read_b64_tr_b16 a[24:25], v233 offset:50368\n"
  "v_mfma_f32_16x16x32_bf16 v[164:167], a[12:15], a[144:147], v[164:167]\n"
  "ds_read_b64_tr_b16 a[26:27], v233 offset:53952\n"
  "v_mfma_f32_16x16x32_bf16 v[168:171], a[12:15], a[148:151], v[168:171]\n"
  "s_mov_b32 s21, 57344\n"
  "buffer_load_dwordx4 a[124:127], v231, s[12:15], s21 offen offset:0 sc1\n"
  "v_mfma_f32_16x16x32_bf16 v[172:175], a[12:15], a[152:155], v[172:175]\n"
  "s_waitcnt vmcnt(39)\n"
  "s_barrier\n"
  "s_waitcnt lgkmcnt(4)\n"
  "v_mfma_f32_16x16x32_bf16 v[176:179], a[16:19], a[140:143], v[176:179]\n"
  "ds_read_b64_tr_b16 a[0:1], v234 offset:0\n"
  "v_mfma_f32_16x16x32_bf16 v[180:183], a[16:19], a[144:147], v[180:183]\n"
  "ds_read_b64_tr_b16 a[2:3], v234 offset:3584\n"
  "v_mfma_f32_16x16x32_bf16 v[184:187], a[16:19], a[148:151], v[184:187]\n"
  "s_mov_b32 s20, 3211264\n"
  "s_add_u32 m0, s23, 57344\n"
  "s_nop 0\n"
  "buffer_load_dwordx4 v224, s[8:11], s20 offen offset:0 lds\n"
  "v_mfma_f32_16x16x32_bf16 v[188:191], a[16:19], a[152:155], v[188:191]\n"
  "s_waitcnt lgkmcnt(4)\n"
  "v_mfma_f32_16x16x32_bf16 v[192:195], a[20:23], a[140:143], v[192:195]\n"
  "ds_read_b64_tr_b16 a[4:5], v234 offset:7168\n"
  "v_mfma_f32_16x16x32_bf16 v[196:199], a[20:23], a[144:147], v[196:199]\n"
  "ds_read_b64_tr_b16 a[6:7], v234 offset:10752\n"
  "v_mfma_f32_16x16x32_bf16 v[200:203], a[20:23], a[148:151], v[200:203]\n"
  "v_mfma_f32_16x16x32_bf16 v[204:207], a[20:23], a[152:155], v[204:207]\n"
  "s_waitcnt lgkmcnt(4)\n"
  "v_mfma_f32_16x16x32_bf16 v[208:211], a[24:27], a[140:143], v[208:211]\n"
  "ds_read_b64_tr_b16 a[8:9], v234 offset:32\n"
  "v_mfma_f32_16x16x32_bf16 v[212:215], a[24:27], a[144:147], v[212:215]\n"
  "ds_read_b64_tr_b16 a[10:11], v234 offset:3616\n"
  "v_mfma_f32_16x16x32_bf16 v[216:219], a[24:27], a[148:151], v[216:219]\n"
  "buffer_load_dwordx4 v225, s[8:11], s20 offen offset:1024 lds\n"
  "v_mfma_f32_16x16x32_bf16 v[220:223], a[24:27], a[152:155], v[220:223]\n"
  "s_waitcnt vmcnt(37) lgkmcnt(4)\n"
  "v_mfma_f32_16x16x32_bf16 v[0:3], a[0:3], a[28:31], v[0:3]\n"
  "ds_read_b64_tr_b16 a[12:13], v234 offset:7200\n"
  "v_mfma_f32_16x16x32_bf16 v[4:7], a[0:3], a[32:35], v[4:7]\n"
  "ds_read_b64_tr_b16 a[14:15], v234 offset:10784\n"
  "v_mfma_f32_16x16x32_bf16 v[8:11], a[0:3], a[36:39], v[8:11]\n"
  "v_mfma_f32_16x16x32_bf16 v[12:15], a[0:3], a[40:43], v[12:15]\n"
  "s_waitcnt lgkmcnt(4)\n"
  "v_mfma_f32_16x16x32_bf16 v[16:19], a[4:7], a[28:31], v[16:19]\n"
  "ds_read_b64_tr_b16 a[16:17], v234 offset:64\n"
  "v_mfma_f32_16x16x32_bf16 v[20:23], a[4:7], a[32:35], v[20:23]\n"
  "ds_read_b64_tr_b16 a[18:19], v234 offset:3648\n"
  "v_mfma_f32_16x16x32_bf16 v[24:27], a[4:7], a[36:39], v[24:27]\n"
  "buffer_load_dwordx4 v226, s[8:11], s20 offen offset:2048 lds\n"
  "v_mfma_f32_16x16x32_bf16 v[28:31], a[4:7], a[40:43], v[28:31]\n"
  "s_waitcnt lgkmcnt(4)\n"
  "v_mfma_f32_16x16x32_bf16 v[32:35], a[8:11], a[28:31], v[32:35]\n"
  "ds_read_b64_tr_b16 a[20:21], v234 offset:7232\n"
  "v_mfma_f32_16x16x32_bf16 v[36:39], a[8:11], a[32:35], v[36:39]\n"
  "ds_read_b64_tr_b16 a[22:23], v234 offset:10816\n"
  "v_mfma_f32_16x16x32_bf16 v[40:43], a[8:11], a[36:39], v[40:43]\n"
  "buffer_load_dwordx4 v227, s[8:11], s20 offen offset:3072 lds\n"
  "v_mfma_f32_16x16x32_bf16 v[44:47], a[8:11], a[40:43], v[44:47]\n"
  "s_waitcnt lgkmcnt(4)\n"
  "v_mfma_f32_16x16x32_bf16 v[48:51], a[12:15], a[28:31], v[48:51]\n"
  "ds_read_b64_tr_b16 a[24:25], v234 offset:96\n"
  "v_mfma_f32_16x16x32_bf16 v[52:55], a[12:15], a[32:35], v[52:55]\n"
  "ds_read_b64_tr_b16 a[26:27], v234 offset:3680\n"
  "v_mfma_f32_16x16x32_bf16 v[56:59], a[12:15], a[36:39], v[56:59]\n"
  "s_add_u32 m0, s23, 61440\n"
  "s_nop 0\n"
  "buffer_load_dwordx4 v228, s[8:11], s20 offen offset:0 lds\n"
  "v_mfma_f32_16x16x32_bf16 v[60:63], a[12:15], a[40:43], v[60:63]\n"
  "s_waitcnt lgkmcnt(4)\n"
  "v_mfma_f32_16x16x32_bf16 v[64:67], a[16:19], a[28:31], v[64:67]\n"
  "ds_read_b64_tr_b16 a[0:1], v234 offset:7264\n"
  "v_mfma_f32_16x16x32_bf16 v[68:71], a[16:19], a[32:35], v[68:71]\n"
  "ds_read_b64_tr_b16 a[2:3], v234 offset:10848\n"
  "v_mfma_f32_16x16x32_bf16 v[72:75], a[16:19], a[36:39], v[72:75]\n"
  "v_mfma_f32_16x16x32_bf16 v[76:79], a[16:19], a[40:43], v[76:79]\n"
  "s_waitcnt lgkmcnt(4)\n"
  "v_mfma_f32_16x16x32_bf16 v[80:83], a[20:23], a[28:31], v[80:83]\n"
  "ds_read_b64_tr_b16 a[4:5], v234 offset:128\n"
  "v_mfma_f32_16x16x32_bf16 v[84:87], a[20:23], a[32:35], v[84:87]\n"
  "ds_read_b64_tr_b16 a[6:7], v234 offset:3712\n"
  "v_mfma_f32_16x16x32_bf16 v[88:91], a[20:23], a[36:39], v[88:91]\n"
  "buffer_load_dwordx4 v229, s[8:11], s20 offen offset:1024 lds\n"
  "v_mfma_f32_16x16x32_bf16 v[92:95], a[20:23], a[40:43], v[92:95]\n"
  "s_waitcnt lgkmcnt(4)\n"
  "v_mfma_f32_16x16x32_bf16 v[96:99], a[24:27], a[28:31], v[96:99]\n"
  "ds_read_b64_tr_b16 a[8:9], v234 offset:7296\n"
  "v_mfma_f32_16x16x32_bf16 v[100:103], a[24:27], a[32:35], v[100:103]\n"
  "ds_read_b64_tr_b16 a[10:11], v234 offset:10880\n"
  "v_mfma_f32_16x16x32_bf16 v[104:107], a[24:27], a[36:39], v[104:107]\n"
  "buffer_load_dwordx4 v230, s[8:11], s20 offen offset:2048 lds\n"
  "v_mfma_f32_16x16x32_bf16 v[108:111], a[24:27], a[40:43], v[108:111]\n"
  "s_waitcnt lgkmcnt(4)\n"
  "v_mfma_f32_16x16x32_bf16 v[112:115], a[0:3], a[28:31], v[112:115]\n"
  "ds_read_b64_tr_b16 a[12:13], v234 offset:160\n"
  "v_mfma_f32_16x16x32_bf16 v[116:119], a[0:3], a[32:35], v[116:119]\n"
  "ds_read_b64_tr_b16 a[14:15], v234 offset:3744\n"
  "v_mfma_f32_16x16x32_bf16 v[120:123], a[0:3], a[36:39], v[120:123]\n"
  "v_mfma_f32_16x16x32_bf16 v[124:127], a[0:3], a[40:43], v[124:127]\n"
  "s_waitcnt lgkmcnt(4)\n"
  "v_mfma_f32_16x16x32_bf16 v[128:131], a[4:7], a[28:31], v[128:131]\n"
  "ds_read_b64_tr_b16 a[16:17], v234 offset:7328\n"
  "v_mfma_f32_16x16x32_bf16 v[132:135], a[4:7], a[32:35], v[132:135]\n"
  "ds_read_b64_tr_b16 a[18:19], v234 offset:10912\n"
  "v_mfma_f32_16x16x32_bf16 v[136:139], a[4:7], a[36:39], v[136:139]\n"
  "buffer_load_dwordx4 a[128:131], v231, s[12:15], s21 offen offset:1024 sc1\n"
  "v_mfma_f32_16x16x32_bf16 v[140:143], a[4:7], a[40:43], v[140:143]\n"
  "s_waitcnt lgkmcnt(4)\n"
  "v_mfma_f32_16x16x32_bf16 v[144:147], a[8:11], a[28:31], v[144:147]\n"
  "ds_read_b64_tr_b16 a[20:21], v234 offset:192\n"
  "v_mfma_f32_16x16x32_bf16 v[148:151], a[8:11], a[32:35], v[148:151]\n"
  "ds_read_b64_tr_b16 a[22:23], v234 offset:3776\n"
  "v_mfma_f32_16x16x32_bf16 v[152:155], a[8:11], a[36:39], v[152:155]\n"
  "buffer_load_dwordx4 a[132:135], v231, s[12:15], s21 offen offset:2048 sc1\n"
  "v_mfma_f32_16x16x32_bf16 v[156:159], a[8:11], a[40:43], v[156:159]\n"
  "s_waitcnt lgkmcnt(4)\n"
  "v_mfma_f32_16x16x32_bf16 v[160:163], a[12:15], a[28:31], v[160:163]\n"
  "ds_read_b64_tr_b16 a[24:25], v234 offset:7360\n"
  "v_mfma_f32_16x16x32_bf16 v[164:167], a[12:15], a[32:35], v[164:167]\n"
  "ds_read_b64_tr_b16 a[26:27], v234 offset:10944\n"
  "v_mfma_f32_16x16x32_bf16 v[168:171], a[12:15], a[36:39], v[168:171]\n"
  "buffer_load_dwordx4 a[136:139], v231, s[12:15], s21 offen offset:3072 sc1\n"
  "v_mfma_f32_16x16x32_bf16 v[172:175], a[12:15], a[40:43], v[172:175]\n"
  "s_waitcnt lgkmcnt(4)\n"
  "v_mfma_f32_16x16x32_bf16 v[176:179], a[16:19], a[28:31], v[176:179]\n"
  "ds_read_b64_tr_b16 a[0:1], v234 offset:14336\n"
  "v_mfma_f32_16x16x32_bf16 v[180:183], a[16:19], a[32:35], v[180:183]\n"
  "ds_read_b64_tr_b16 a[2:3], v234 offset:17920\n"
  "v_mfma_f32_16x16x32_bf16 v[184:187], a[16:19], a[36:39], v[184:187]\n"
  "v_mfma_f32_16x16x32_bf16 v[188:191], a[16:19], a[40:43], v[188:191]\n"
  "s_waitcnt lgkmcnt(4)\n"
  "v_mfma_f32_16x16x32_bf16 v[192:195], a[20:23], a[28:31], v[192:195]\n"
  "ds_read_b64_tr_b16 a[4:5], v234 offset:21504\n"
  "v_mfma_f32_16x16x32_bf16 v[196:199], a[20:23], a[32:35], v[196:199]\n"
  "ds_read_b64_tr_b16 a[6:7], v234 offset:25088\n"
  "v_mfma_f32_16x16x32_bf16 v[200:203], a[20:23], a[36:39], v[200:203]\n"
  "s_mov_b32 s21, 61440\n"
  "buffer_load_dwordx4 a[140:143], v231, s[12:15], s21 offen offset:0 sc1\n"
  "v_mfma_f32_16x16x32_bf16 v[204:207], a[20:23], a[40:43], v[204:207]\n"
  "s_waitcnt lgkmcnt(4)\n"
  "v_mfma_f32_16x16x32_bf16 v[208:211], a[24:27], a[28:31], v[208:211]\n"
  "ds_read_b64_tr_b16 a[8:9], v234 offset:14368\n"
  "v_mfma_f32_16x16x32_bf16 v[212:215], a[24:27], a[32:35], v[212:215]\n"
  "ds_read_b64_tr_b16 a[10:11], v234 offset:17952\n"
  "v_mfma_f32_16x16x32_bf16 v[216:219], a[24:27], a[36:39], v[216:219]\n"
  "v_mfma_f32_16x16x32_bf16 v[220:223], a[24:27], a[40:43], v[220:223]\n"
  "s_waitcnt vmcnt(42) lgkmcnt(4)\n"
  "v_mfma_f32_16x16x32_bf16 v[0:3], a[0:3], a[44:47], v[0:3]\n"
  "ds_read_b64_tr_b16 a[12:13], v234 offset:21536\n"
  "v_mfma_f32_16x16x32_bf16 v[4:7], a[0:3], a[48:51], v[4:7]\n"
  "ds_read_b64_tr_b16 a[14:15], v234 offset:25120\n"
  "v_mfma_f32_16x16x32_bf16 v[8:11], a[0:3], a[52:55], v[8:11]\n"
  "buffer_load_dwordx4 a[144:147], v231, s[12:15], s21 offen offset:1024 sc1\n"
  "v_mfma_f32_16x16x32_bf16 v[12:15], a[0:3], a[56:59], v[12:15]\n"
  "s_waitcnt lgkmcnt(4)\n"
  "v_mfma_f32_16x16x32_bf16 v[16:19], a[4:7], a[44:47], v[16:19]\n"
  "ds_read_b64_tr_b16 a[16:17], v234 offset:14400\n"
  "v_mfma_f32_16x16x32_bf16 v[20:23], a[4:7], a[48:51], v[20:23]\n"
  "ds_read_b64_tr_b16 a[18:19], v234 offset:17984\n"
  "v_mfma_f32_16x16x32_bf16 v[24:27], a[4:7], a[52:55], v[24:27]\n"
  "buffer_load_dwordx4 a[148:151], v231, s[12:15], s21 offen offset:2048 sc1\n"
  "v_mfma_f32_16x16x32_bf16 v[28:31], a[4:7], a[56:59], v[28:31]\n"
  "s_waitcnt lgkmcnt(4)\n"
  "v_mfma_f32_16x16x32_bf16 v[32:35], a[8:11], a[44:47], v[32:35]\n"
  "ds_read_b64_tr_b16 a[20:21], v234 offset:21568\n"
  "v_mfma_f32_16x16x32_bf16 v[36:39], a[8:11], a[48:51], v[36:39]\n"
  "ds_read_b64_tr_b16 a[22:23], v234 offset:25152\n"
  "v_mfma_f32_16x16x32_bf16 v[40:43], a[8:11], a[52:55], v[40:43]\n"
  "v_mfma_f32_16x16x32_bf16 v[44:47], a[8:11], a[56:59], v[44:47]\n"
  "s_waitcnt lgkmcnt(4)\n"
  "v_mfma_f32_16x16x32_bf16 v[48:51], a[12:15], a[44:47], v[48:51]\n"
  "ds_read_b64_tr_b16 a[24:25], v234 offset:14432\n"
  "v_mfma_f32_16x16x32_bf16 v[52:55], a[12:15], a[48:51], v[52:55]\n"
  "ds_read_b64_tr_b16 a[26:27], v234 offset:18016\n"
  "v_mfma_f32_16x16x32_bf16 v[56:59], a[12:15], a[52:55], v[56:59]\n"
  "buffer_load_dwordx4 a[152:155], v231, s[12:15], s21 offen offset:3072 sc1\n"
  "v_mfma_f32_16x16x32_bf16 v[60:63], a[12:15], a[56:59], v[60:63]\n"
  "s_waitcnt lgkmcnt(4)\n"
  "v_mfma_f32_16x16x32_bf16 v[64:67], a[16:19], a[44:47], v[64:67]\n"
  "ds_read_b64_tr_b16 a[0:1], v234 offset:21600\n"
  "v_mfma_f32_16x16x32_bf16 v[68:71], a[16:19], a[48:51], v[68:71]\n"
  "ds_read_b64_tr_b16 a[2:3], v234 offset:25184\n"
  "v_mfma_f32_16x16x32_bf16 v[72:75], a[16:19], a[52:55], v[72:75]\n"
  "s_mov_b32 s21, 65536\n"
  "buffer_load_dwordx4 a[28:31], v231, s[12:15], s21 offen offset:0 sc1\n"
  "v_mfma_f32_16x16x32_bf16 v[76:79], a[16:19], a[56:59], v[76:79]\n"
  "s_waitcnt lgkmcnt(4)\n"
  "v_mfma_f32_16x16x32_bf16 v[80:83], a[20:23], a[44:47], v[80:83]\n"
  "ds_read_b64_tr_b16 a[4:5], v234 offset:14464\n"
  "v_mfma_f32_16x16x32_bf16 v[84:87], a[20:23], a[48:51], v[84:87]\n"
  "ds_read_b64_tr_b16 a[6:7], v234 offset:18048\n"
  "v_mfma_f32_16x16x32_bf16 v[88:91], a[20:23], a[52:55], v[88:91]\n"
  "v_mfma_f32_16x16x32_bf16 v[92:95], a[20:23], a[56:59], v[92:95]\n"
  "s_waitcnt lgkmcnt(4)\n"
  "v_mfma_f32_16x16x32_bf16 v[96:99], a[24:27], a[44:47], v[96:99]\n"
  "ds_read_b64_tr_b16 a[8:9], v234 offset:21632\n"
  "v_mfma_f32_16x16x32_bf16 v[100:103], a[24:27], a[48:51], v[100:103]\n"
  "ds_read_b64_tr_b16 a[10:11], v234 offset:25216\n"
  "v_mfma_f32_16x16x32_bf16 v[104:107], a[24:27], a[52:55], v[104:107]\n"
  "buffer_load_dwordx4 a[32:35], v231, s[12:15], s21 offen offset:1024 sc1\n"
  "v_mfma_f32_16x16x32_bf16 v[108:111], a[24:27], a[56:59], v[108:111]\n"
  "s_waitcnt lgkmcnt(4)\n"
  "v_mfma_f32_16x16x32_bf16 v[112:115], a[0:3], a[44:47], v[112:115]\n"
  "ds_read_b64_tr_b16 a[12:13], v234 offset:14496\n"
  "v_mfma_f32_16x16x32_bf16 v[116:119], a[0:3], a[48:51], v[116:119]\n"
  "ds_read_b64_tr_b16 a[14:15], v234 offset:18080\n"
  "v_mfma_f32_16x16x32_bf16 v[120:123], a[0:3], a[52:55], v[120:123]\n"
  "v_mfma_f32_16x16x32_bf16 v[124:127], a[0:3], a[56:59], v[124:127]\n"
  "s_waitcnt lgkmcnt(4)\n"
  "v_mfma_f32_16x16x32_bf16 v[128:131], a[4:7], a[44:47], v[128:131]\n"
  "ds_read_b64_tr_b16 a[16:17], v234 offset:21664\n"
  "v_mfma_f32_16x16x32_bf16 v[132:135], a[4:7], a[48:51], v[132:135]\n"
  "ds_read_b64_tr_b16 a[18:19], v234 offset:25248\n"
  "v_mfma_f32_16x16x32_bf16 v[136:139], a[4:7], a[52:55], v[136:139]\n"
  "buffer_load_dwordx4 a[36:39], v231, s[12:15], s21 offen offset:2048 sc1\n"
  "v_mfma_f32_16x16x32_bf16 v[140:143], a[4:7], a[56:59], v[140:143]\n"
  "s_waitcnt lgkmcnt(4)\n"
  "v_mfma_f32_16x16x32_bf16 v[144:147], a[8:11], a[44:47], v[144:147]\n"
  "ds_read_b64_tr_b16 a[20:21], v234 offset:14528\n"
  "v_mfma_f32_16x16x32_bf16 v[148:151], a[8:11], a[48:51], v[148:151]\n"
  "ds_read_b64_tr_b16 a[22:23], v234 offset:18112\n"
  "v_mfma_f32_16x16x32_bf16 v[152:155], a[8:11], a[52:55], v[152:155]\n"
  "buffer_load_dwordx4 a[40:43], v231, s[12:15], s21 offen offset:3072 sc1\n"
  "v_mfma_f32_16x16x32_bf16 v[156:159], a[8:11], a[56:59], v[156:159]\n"
  "s_waitcnt lgkmcnt(4)\n"
  "v_mfma_f32_16x16x32_bf16 v[160:163], a[12:15], a[44:47], v[160:163]\n"
  "ds_read_b64_tr_b16 a[24:25], v234 offset:21696\n"
  "v_mfma_f32_16x16x32_bf16 v[164:167], a[12:15], a[48:51], v[164:167]\n"
  "ds_read_b64_tr_b16 a[26:27], v234 offset:25280\n"
  "v_mfma_f32_16x16x32_bf16 v[168:171], a[12:15], a[52:55], v[168:171]\n"
  "v_mfma_f32_16x16x32_bf16 v[172:175], a[12:15], a[56:59], v[172:175]\n"
  "s_waitcnt vmcnt(42)\n"
  "s_barrier\n"
  "s_waitcnt lgkmcnt(4)\n"
  "v_mfma_f32_16x16x32_bf16 v[176:179], a[16:19], a[44:47], v[176:179]\n"
  "ds_read_b64_tr_b16 a[0:1], v232 offset:0\n"
  "v_mfma_f32_16x16x32_bf16 v[180:183], a[16:19], a[48:51], v[180:183]\n"
  "ds_read_b64_tr_b16 a[2:3], v232 offset:3584\n"
  "v_mfma_f32_16x16x32_bf16 v[184:187], a[16:19], a[52:55], v[184:187]\n"
  "s_mov_b32 s20, 3670016\n"
  "s_add_u32 m0, s23, 86016\n"
  "s_nop 0\n"
  "buffer_load_dwordx4 v224, s[8:11], s20 offen offset:0 lds\n"
  "v_mfma_f32_16x16x32_bf16 v[188:191], a[16:19], a[56:59], v[188:191]\n"
  "s_waitcnt lgkmcnt(4)\n"
  "v_mfma_f32_16x16x32_bf16 v[192:195], a[20:23], a[44:47], v[192:195]\n"
  "ds_read_b64_tr_b16 a[4:5], v232 offset:7168\n"
  "v_mfma_f32_16x16x32_bf16 v[196:199], a[20:23], a[48:51], v[196:199]\n"
  "ds_read_b64_tr_b16 a[6:7], v232 offset:10752\n"
  "v_mfma_f32_16x16x32_bf16 v[200:203], a[20:23], a[52:55], v[200:203]\n"
  "v_mfma_f32_16x16x32_bf16 v[204:207], a[20:23], a[56:59], v[204:207]\n"
  "s_waitcnt lgkmcnt(4)\n"
  "v_mfma_f32_16x16x32_bf16 v[208:211], a[24:27], a[44:47], v[208:211]\n"
  "ds_read_b64_tr_b16 a[8:9], v232 offset:32\n"
  "v_mfma_f32_16x16x32_bf16 v[212:215], a[24:27], a[48:51], v[212:215]\n"
  "ds_read_b64_tr_b16 a[10:11], v232 offset:3616\n"
  "v_mfma_f32_16x16x32_bf16 v[216:219], a[24:27], a[52:55], v[216:219]\n"
  "buffer_load_dwordx4 v225, s[8:11], s20 offen offset:1024 lds\n"
  "v_mfma_f32_16x16x32_bf16 v[220:223], a[24:27], a[56:59], v[220:223]\n"
  "s_waitcnt vmcnt(40) lgkmcnt(4)\n"
  "v_mfma_f32_16x16x32_bf16 v[0:3], a[0:3], a[60:63], v[0:3]\n"
  "ds_read_b64_tr_b16 a[12:13], v232 offset:7200\n"
  "v_mfma_f32_16x16x32_bf16 v[4:7], a[0:3], a[64:67], v[4:7]\n"
  "ds_read_b64_tr_b16 a[14:15], v232 offset:10784\n"
  "v_mfma_f32_16x16x32_bf16 v[8:11], a[0:3], a[68:71], v[8:11]\n"
  "v_mfma_f32_16x16x32_bf16 v[12:15], a[0:3], a[72:75], v[12:15]\n"
  "s_waitcnt lgkmcnt(4)\n"
  "v_mfma_f32_16x16x32_bf16 v[16:19], a[4:7], a[60:63], v[16:19]\n"
  "ds_read_b64_tr_b16 a[16:17], v232 offset:64\n"
  "v_mfma_f32_16x16x32_bf16 v[20:23], a[4:7], a[64:67], v[20:23]\n"
  "ds_read_b64_tr_b16 a[18:19], v232 offset:3648\n"
  "v_mfma_f32_16x16x32_bf16 v[24:27], a[4:7], a[68:71], v[24:27]\n"
  "buffer_load_dwordx4 v226, s[8:11], s20 offen offset:2048 lds\n"
  "v_mfma_f32_16x16x32_bf16 v[28:31], a[4:7], a[72:75], v[28:31]\n"
  "s_waitcnt lgkmcnt(4)\n"
  "v_mfma_f32_16x16x32_bf16 v[32:35], a[8:11], a[60:63], v[32:35]\n"
  "ds_read_b64_tr_b16 a[20:21], v232 offset:7232\n"
  "v_mfma_f32_16x16x32_bf16 v[36:39], a[8:11], a[64:67], v[36:39]\n"
  "ds_read_b64_tr_b16 a[22:23], v232 offset:10816\n"
  "v_mfma_f32_16x16x32_bf16 v[40:43], a[8:11], a[68:71], v[40:43]\n"
  "buffer_load_dwordx4 v227, s[8:11], s20 offen offset:3072 lds\n"
  "v_mfma_f32_16x16x32_bf16 v[44:47], a[8:11], a[72:75], v[44:47]\n"
  "s_waitcnt lgkmcnt(4)\n"
  "v_mfma_f32_16x16x32_bf16 v[48:51], a[12:15], a[60:63], v[48:51]\n"
  "ds_read_b64_tr_b16 a[24:25], v232 offset:96\n"
  "v_mfma_f32_16x16x32_bf16 v[52:55], a[12:15], a[64:67], v[52:55]\n"
  "ds_read_b64_tr_b16 a[26:27], v232 offset:3680\n"
  "v_mfma_f32_16x16x32_bf16 v[56:59], a[12:15], a[68:71], v[56:59]\n"
  "v_mfma_f32_16x16x32_bf16 v[60:63], a[12:15], a[72:75], v[60:63]\n"
  "s_waitcnt lgkmcnt(4)\n"
  "v_mfma_f32_16x16x32_bf16 v[64:67], a[16:19], a[60:63], v[64:67]\n"
  "ds_read_b64_tr_b16 a[0:1], v232 offset:7264\n"
  "v_mfma_f32_16x16x32_bf16 v[68:71], a[16:19], a[64:67], v[68:71]\n"
  "ds_read_b64_tr_b16 a[2:3], v232 offset:10848\n"
  "v_mfma_f32_16x16x32_bf16 v[72:75], a[16:19], a[68:71], v[72:75]\n"
  "s_add_u32 m0, s23, 90112\n"
  "s_nop 0\n"
  "buffer_load_dwordx4 v228, s[8:11], s20 offen offset:0 lds\n"
  "v_mfma_f32_16x16x32_bf16 v[76:79], a[16:19], a[72:75], v[76:79]\n"
  "s_waitcnt lgkmcnt(4)\n"
  "v_mfma_f32_16x16x32_bf16 v[80:83], a[20:23], a[60:63], v[80:83]\n"
  "ds_read_b64_tr_b16 a[4:5], v232 offset:128\n"
  "v_mfma_f32_16x16x32_bf16 v[84:87], a[20:23], a[64:67], v[84:87]\n"
  "ds_read_b64_tr_b16 a[6:7], v232 offset:3712\n"
  "v_mfma_f32_16x16x32_bf16 v[88:91], a[20:23], a[68:71], v[88:91]\n"
  "v_mfma_f32_16x16x32_bf16 v[92:95], a[20:23], a[72:75], v[92:95]\n"
  "s_waitcnt lgkmcnt(4)\n"
  "v_mfma_f32_16x16x32_bf16 v[96:99], a[24:27], a[60:63], v[96:99]\n"
  "ds_read_b64_tr_b16 a[8:9], v232 offset:7296\n"
  "v_mfma_f32_16x16x32_bf16 v[100:103], a[24:27], a[64:67], v[100:103]\n"
  "ds_read_b64_tr_b16 a[10:11], v232 offset:10880\n"
  "v_mfma_f32_16x16x32_bf16 v[104:107], a[24:27], a[68:71], v[104:107]\n"
  "buffer_load_dwordx4 v229, s[8:11], s20 offen offset:1024 lds\n"
  "v_mfma_f32_16x16x32_bf16 v[108:111], a[24:27], a[72:75], v[108:111]\n"
  "s_waitcnt lgkmcnt(4)\n"
  "v_mfma_f32_16x16x32_bf16 v[112:115], a[0:3], a[60:63], v[112:115]\n"
  "ds_read_b64_tr_b16 a[12:13], v232 offset:160\n"
  "v_mfma_f32_16x16x32_bf16 v[116:119], a[0:3], a[64:67], v[116:119]\n"
  "ds_read_b64_tr_b16 a[14:15], v232 offset:3744\n"
  "v_mfma_f32_16x16x32_bf16 v[120:123], a[0:3], a[68:71], v[120:123]\n"
  "v_mfma_f32_16x16x32_bf16 v[124:127], a[0:3], a[72:75], v[124:127]\n"
  "s_waitcnt lgkmcnt(4)\n"
  "v_mfma_f32_16x16x32_bf16 v[128:131], a[4:7], a[60:63], v[128:131]\n"
  "ds_read_b64_tr_b16 a[16:17], v232 offset:7328\n"
  "v_mfma_f32_16x16x32_bf16 v[132:135], a[4:7], a[64:67], v[132:135]\n"
  "ds_read_b64_tr_b16 a[18:19], v232 offset:10912\n"
  "v_mfma_f32_16x16x32_bf16 v[136:139], a[4:7], a[68:71], v[136:139]\n"
  "buffer_load_dwordx4 v230, s[8:11], s20 offen offset:2048 lds\n"
  "v_mfma_f32_16x16x32_bf16 v[140:143], a[4:7], a[72:75], v[140:143]\n"
  "s_waitcnt lgkmcnt(4)\n"
  "v_mfma_f32_16x16x32_bf16 v[144:147], a[8:11], a[60:63], v[144:147]\n"
  "ds_read_b64_tr_b16 a[20:21], v232 offset:192\n"
  "v_mfma_f32_16x16x32_bf16 v[148:151], a[8:11], a[64:67], v[148:151]\n"
  "ds_read_b64_tr_b16 a[22:23], v232 offset:3776\n"
  "v_mfma_f32_16x16x32_bf16 v[152:155], a[8:11], a[68:71], v[152:155]\n"
  "s_mov_b32 s21, 69632\n"
  "buffer_load_dwordx4 a[44:47], v231, s[12:15], s21 offen offset:0 sc1\n"
  "v_mfma_f32_16x16x32_bf16 v[156:159], a[8:11], a[72:75], v[156:159]\n"
  "s_waitcnt lgkmcnt(4)\n"
  "v_mfma_f32_16x16x32_bf16 v[160:163], a[12:15], a[60:63], v[160:163]\n"
  "ds_read_b64_tr_b16 a[24:25], v232 offset:7360\n"
  "v_mfma_f32_16x16x32_bf16 v[164:167], a[12:15], a[64:67], v[164:167]\n"
  "ds_read_b64_tr_b16 a[26:27], v232 offset:10944\n"
  "v_mfma_f32_16x16x32_bf16 v[168:171], a[12:15], a[68:71], v[168:171]\n"
  "v_mfma_f32_16x16x32_bf16 v[172:175], a[12:15], a[72:75], v[172:175]\n"
  "s_waitcnt lgkmcnt(4)\n"
  "v_mfma_f32_16x16x32_bf16 v[176:179], a[16:19], a[60:63], v[176:179]\n"
  "ds_read_b64_tr_b16 a[0:1], v232 offset:14336\n"
  "v_mfma_f32_16x16x32_bf16 v[180:183], a[16:19], a[64:67], v[180:183]\n"
  "ds_read_b64_tr_b16 a[2:3], v232 offset:17920\n"
  "v_mfma_f32_16x16x32_bf16 v[184:187], a[16:19], a[68:71], v[184:187]\n"
  "buffer_load_dwordx4 a[48:51], v231, s[12:15], s21 offen offset:1024 sc1\n"
  "v_mfma_f32_16x16x32_bf16 v[188:191], a[16:19], a[72:75], v[188:191]\n"
  "s_waitcnt lgkmcnt(4)\n"
  "v_mfma_f32_16x16x32_bf16 v[192:195], a[20:23], a[60:63], v[192:195]\n"
  "ds_read_b64_tr_b16 a[4:5], v232 offset:21504\n"
  "v_mfma_f32_16x16x32_bf16 v[196:199], a[20:23], a[64:67], v[196:199]\n"
  "ds_read_b64_tr_b16 a[6:7], v232 offset:25088\n"
  "v_mfma_f32_16x16x32_bf16 v[200:203], a[20:23], a[68:71], v[200:203]\n"
  "v_mfma_f32_16x16x32_bf16 v[204:207], a[20:23], a[72:75], v[204:207]\n"
  "s_waitcnt lgkmcnt(4)\n"
  "v_mfma_f32_16x16x32_bf16 v[208:211], a[24:27], a[60:63], v[208:211]\n"
  "ds_read_b64_tr_b16 a[8:9], v232 offset:14368\n"
  "v_mfma_f32_16x16x32_bf16 v[212:215], a[24:27], a[64:67], v[212:215]\n"
  "ds_read_b64_tr_b16 a[10:11], v232 offset:17952\n"
  "v_mfma_f32_16x16x32_bf16 v[216:219], a[24:27], a[68:71], v[216:219]\n"
  "buffer_load_dwordx4 a[52:55], v231, s[12:15], s21 offen offset:2048 sc1\n"
  "v_mfma_f32_16x16x32_bf16 v[220:223], a[24:27], a[72:75], v[220:223]\n"
  "s_waitcnt vmcnt(44) lgkmcnt(4)\n"
  "v_mfma_f32_16x16x32_bf16 v[0:3], a[0:3], a[76:79], v[0:3]\n"
  "ds_read_b64_tr_b16 a[12:13], v232 offset:21536\n"
  "v_mfma_f32_16x16x32_bf16 v[4:7], a[0:3], a[80:83], v[4:7]\n"
  "ds_read_b64_tr_b16 a[14:15], v232 offset:25120\n"
  "v_mfma_f32_16x16x32_bf16 v[8:11], a[0:3], a[84:87], v[8:11]\n"
  "buffer_load_dwordx4 a[56:59], v231, s[12:15], s21 offen offset:3072 sc1\n"
  "v_mfma_f32_16x16x32_bf16 v[12:15], a[0:3], a[88:91], v[12:15]\n"
  "s_waitcnt lgkmcnt(4)\n"
  "v_mfma_f32_16x16x32_bf16 v[16:19], a[4:7], a[76:79], v[16:19]\n"
  "ds_read_b64_tr_b16 a[16:17], v232 offset:14400\n"
  "v_mfma_f32_16x16x32_bf16 v[20:23], a[4:7], a[80:83], v[20:23]\n"
  "ds_read_b64_tr_b16 a[18:19], v232 offset:17984\n"
  "v_mfma_f32_16x16x32_bf16 v[24:27], a[4:7], a[84:87], v[24:27]\n"
  "v_mfma_f32_16x16x32_bf16 v[28:31], a[4:7], a[88:91], v[28:31]\n"
  "s_waitcnt lgkmcnt(4)\n"
  "v_mfma_f32_16x16x32_bf16 v[32:35], a[8:11], a[76:79], v[32:35]\n"
  "ds_read_b64_tr_b16 a[20:21], v232 offset:21568\n"
  "v_mfma_f32_16x16x32_bf16 v[36:39], a[8:11], a[80:83], v[36:39]\n"
  "ds_read_b64_tr_b16 a[22:23], v232 offset:25152\n"
  "v_mfma_f32_16x16x32_bf16 v[40:43], a[8:11], a[84:87], v[40:43]\n"
  "s_mov_b32 s21, 73728\n"
  "buffer_load_dwordx4 a[60:63], v231, s[12:15], s21 offen offset:0 sc1\n"
  "v_mfma_f32_16x16x32_bf16 v[44:47], a[8:11], a[88:91], v[44:47]\n"
  "s_waitcnt lgkmcnt(4)\n"
  "v_mfma_f32_16x16x32_bf16 v[48:51], a[12:15], a[76:79], v[48:51]\n"
  "ds_read_b64_tr_b16 a[24:25], v232 offset:14432\n"
  "v_mfma_f32_16x16x32_bf16 v[52:55], a[12:15], a[80:83], v[52:55]\n"
  "ds_read_b64_tr_b16 a[26:27], v232 offset:18016\n"
  "v_mfma_f32_16x16x32_bf16 v[56:59], a[12:15], a[84:87], v[56:59]\n"
  "v_mfma_f32_16x16x32_bf16 v[60:63], a[12:15], a[88:91], v[60:63]\n"
  "s_waitcnt lgkmcnt(4)\n"
  "v_mfma_f32_16x16x32_bf16 v[64:67], a[16:19], a[76:79], v[64:67]\n"
  "ds_read_b64_tr_b16 a[0:1], v232 offset:21600\n"
  "v_mfma_f32_16x16x32_bf16 v[68:71], a[16:19], a[80:83], v[68:71]\n"
  "ds_read_b64_tr_b16 a[2:3], v232 offset:25184\n"
  "v_mfma_f32_16x16x32_bf16 v[72:75], a[16:19], a[84:87], v[72:75]\n"
  "buffer_load_dwordx4 a[64:67], v231, s[12:15], s21 offen offset:1024 sc1\n"
  "v_mfma_f32_16x16x32_bf16 v[76:79], a[16:19], a[88:91], v[76:79]\n"
  "s_waitcnt lgkmcnt(4)\n"
  "v_mfma_f32_16x16x32_bf16 v[80:83], a[20:23], a[76:79], v[80:83]\n"
  "ds_read_b64_tr_b16 a[4:5], v232 offset:14464\n"
  "v_mfma_f32_16x16x32_bf16 v[84:87], a[20:23], a[80:83], v[84:87]\n"
  "ds_read_b64_tr_b16 a[6:7], v232 offset:18048\n"
  "v_mfma_f32_16x16x32_bf16 v[88:91], a[20:23], a[84:87], v[88:91]\n"
  "v_mfma_f32_16x16x32_bf16 v[92:95], a[20:23], a[88:91], v[92:95]\n"
  "s_waitcnt lgkmcnt(4)\n"
  "v_mfma_f32_16x16x32_bf16 v[96:99], a[24:27], a[76:79], v[96:99]\n"
  "ds_read_b64_tr_b16 a[8:9], v232 offset:21632\n"
  "v_mfma_f32_16x16x32_bf16 v[100:103], a[24:27], a[80:83], v[100:103]\n"
  "ds_read_b64_tr_b16 a[10:11], v232 offset:25216\n"
  "v_mfma_f32_16x16x32_bf16 v[104:107], a[24:27], a[84:87], v[104:107]\n"
  "buffer_load_dwordx4 a[68:71], v231, s[12:15], s21 offen offset:2048 sc1\n"
  "v_mfma_f32_16x16x32_bf16 v[108:111], a[24:27], a[88:91], v[108:111]\n"
  "s_waitcnt lgkmcnt(4)\n"
  "v_mfma_f32_16x16x32_bf16 v[112:115], a[0:3], a[76:79], v[112:115]\n"
  "ds_read_b64_tr_b16 a[12:13], v232 offset:14496\n"
  "v_mfma_f32_16x16x32_bf16 v[116:119], a[0:3], a[80:83], v[116:119]\n"
  "ds_read_b64_tr_b16 a[14:15], v232 offset:18080\n"
  "v_mfma_f32_16x16x32_bf16 v[120:123], a[0:3], a[84:87], v[120:123]\n"
  "buffer_load_dwordx4 a[72:75], v231, s[12:15], s21 offen offset:3072 sc1\n"
  "v_mfma_f32_16x16x32_bf16 v[124:127], a[0:3], a[88:91], v[124:127]\n"
  "s_waitcnt lgkmcnt(4)\n"
  "v_mfma_f32_16x16x32_bf16 v[128:131], a[4:7], a[76:79], v[128:131]\n"
  "ds_read_b64_tr_b16 a[16:17], v232 offset:21664\n"
  "v_mfma_f32_16x16x32_bf16 v[132:135], a[4:7], a[80:83], v[132:135]\n"
  "ds_read_b64_tr_b16 a[18:19], v232 offset:25248\n"
  "v_mfma_f32_16x16x32_bf16 v[136:139], a[4:7], a[84:87], v[136:139]\n"
  "v_mfma_f32_16x16x32_bf16 v[140:143], a[4:7], a[88:91], v[140:143]\n"
  "s_waitcnt lgkmcnt(4)\n"
  "v_mfma_f32_16x16x32_bf16 v[144:147], a[8:11], a[76:79], v[144:147]\n"
  "ds_read_b64_tr_b16 a[20:21], v232 offset:14528\n"
  "v_mfma_f32_16x16x32_bf16 v[148:151], a[8:11], a[80:83], v[148:151]\n"
  "ds_read_b64_tr_b16 a[22:23], v232 offset:18112\n"
  "v_mfma_f32_16x16x32_bf16 v[152:155], a[8:11], a[84:87], v[152:155]\n"
  "v_mfma_f32_16x16x32_bf16 v[156:159], a[8:11], a[88:91], v[156:159]\n"
  "s_waitcnt lgkmcnt(4)\n"
  "v_mfma_f32_16x16x32_bf16 v[160:163], a[12:15], a[76:79], v[160:163]\n"
  "ds_read_b64_tr_b16 a[24:25], v232 offset:21696\n"
  "v_mfma_f32_16x16x32_bf16 v[164:167], a[12:15], a[80:83], v[164:167]\n"
  "ds_read_b64_tr_b16 a[26:27], v232 offset:25280\n"
  "v_mfma_f32_16x16x32_bf16 v[168:171], a[12:15], a[84:87], v[168:171]\n"
  "v_mfma_f32_16x16x32_bf16 v[172:175], a[12:15], a[88:91], v[172:175]\n"
  "s_waitcnt vmcnt(42)\n"
  "s_barrier\n"
  "s_waitcnt lgkmcnt(4)\n"
  "v_mfma_f32_16x16x32_bf16 v[176:179], a[16:19], a[76:79], v[176:179]\n"
  "ds_read_b64_tr_b16 a[0:1], v232 offset:28672\n"
  "v_mfma_f32_16x16x32_bf16 v[180:183], a[16:19], a[80:83], v[180:183]\n"
  "ds_read_b64_tr_b16 a[2:3], v232 offset:32256\n"
  "v_mfma_f32_16x16x32_bf16 v[184:187], a[16:19], a[84:87], v[184:187]\n"
  "s_mov_b32 s20, 4128768\n"
  "s_add_u32 m0, s23, 114688\n"
  "s_nop 0\n"
  "buffer_load_dwordx4 v224, s[8:11], s20 offen offset:0 lds\n"
  "v_mfma_f32_16x16x32_bf16 v[188:191], a[16:19], a[88:91], v[188:191]\n"
  "s_waitcnt lgkmcnt(4)\n"
  "v_mfma_f32_16x16x32_bf16 v[192:195], a[20:23], a[76:79], v[192:195]\n"
  "ds_read_b64_tr_b16 a[4:5], v232 offset:35840\n"
  "v_mfma_f32_16x16x32_bf16 v[196:199], a[20:23], a[80:83], v[196:199]\n"
  "ds_read_b64_tr_b16 a[6:7], v232 offset:39424\n"
  "v_mfma_f32_16x16x32_bf16 v[200:203], a[20:23], a[84:87], v[200:203]\n"
  "buffer_load_dwordx4 v225, s[8:11], s20 offen offset:1024 lds\n"
  "v_mfma_f32_16x16x32_bf16 v[204:207], a[20:23], a[88:91], v[204:207]\n"
  "s_waitcnt lgkmcnt(4)\n"
  "v_mfma_f32_16x16x32_bf16 v[208:211], a[24:27], a[76:79], v[208:211]\n"
  "ds_read_b64_tr_b16 a[8:9], v232 offset:28704\n"
  "v_mfma_f32_16x16x32_bf16 v[212:215], a[24:27], a[80:83], v[212:215]\n"
  "ds_read_b64_tr_b16 a[10:11], v232 offset:32288\n"
  "v_mfma_f32_16x16x32_bf16 v[216:219], a[24:27], a[84:87], v[216:219]\n"
  "v_mfma_f32_16x16x32_bf16 v[220:223], a[24:27], a[88:91], v[220:223]\n"
  "s_waitcnt vmcnt(40) lgkmcnt(4)\n"
  "v_mfma_f32_16x16x32_bf16 v[0:3], a[0:3], a[92:95], v[0:3]\n"
  "ds_read_b64_tr_b16 a[12:13], v232 offset:35872\n"
  "v_mfma_f32_16x16x32_bf16 v[4:7], a[0:3], a[96:99], v[4:7]\n"
  "ds_read_b64_tr_b16 a[14:15], v232 offset:39456\n"
  "v_mfma_f32_16x16x32_bf16 v[8:11], a[0:3], a[100:103], v[8:11]\n"
  "buffer_load_dwordx4 v226, s[8:11], s20 offen offset:2048 lds\n"
  "v_mfma_f32_16x16x32_bf16 v[12:15], a[0:3], a[104:107], v[12:15]\n"
  "s_waitcnt lgkmcnt(4)\n"
  "v_mfma_f32_16x16x32_bf16 v[16:19], a[4:7], a[92:95], v[16:19]\n"
  "ds_read_b64_tr_b16 a[16:17], v232 offset:28736\n"
  "v_mfma_f32_16x16x32_bf16 v[20:23], a[4:7], a[96:99], v[20:23]\n"
  "ds_read_b64_tr_b16 a[18:19], v232 offset:32320\n"
  "v_mfma_f32_16x16x32_bf16 v[24:27], a[4:7], a[100:103], v[24:27]\n"
  "v_mfma_f32_16x16x32_bf16 v[28:31], a[4:7], a[104:107], v[28:31]\n"
  "s_waitcnt lgkmcnt(4)\n"
  "v_mfma_f32_16x16x32_bf16 v[32:35], a[8:11], a[92:95], v[32:35]\n"
  "ds_read_b64_tr_b16 a[20:21], v232 offset:35904\n"
  "v_mfma_f32_16x16x32_bf16 v[36:39], a[8:11], a[96:99], v[36:39]\n"
  "ds_read_b64_tr_b16 a[22:23], v232 offset:39488\n"
  "v_mfma_f32_16x16x32_bf16 v[40:43], a[8:11], a[100:103], v[40:43]\n"
  "buffer_load_dwordx4 v227, s[8:11], s20 offen offset:3072 lds\n"
  "v_mfma_f32_16x16x32_bf16 v[44:47], a[8:11], a[104:107], v[44:47]\n"
  "s_waitcnt lgkmcnt(4)\n"
  "v_mfma_f32_16x16x32_bf16 v[48:51], a[12:15], a[92:95], v[48:51]\n"
  "ds_read_b64_tr_b16 a[24:25], v232 offset:28768\n"
  "v_mfma_f32_16x16x32_bf16 v[52:55], a[12:15], a[96:99], v[52:55]\n"
  "ds_read_b64_tr_b16 a[26:27], v232 offset:32352\n"
  "v_mfma_f32_16x16x32_bf16 v[56:59], a[12:15], a[100:103], v[56:59]\n"
  "v_mfma_f32_16x16x32_bf16 v[60:63], a[12:15], a[104:107], v[60:63]\n"
  "s_waitcnt lgkmcnt(4)\n"
  "v_mfma_f32_16x16x32_bf16 v[64:67], a[16:19], a[92:95], v[64:67]\n"
  "ds_read_b64_tr_b16 a[0:1], v232 offset:35936\n"
  "v_mfma_f32_16x16x32_bf16 v[68:71], a[16:19], a[96:99], v[68:71]\n"
  "ds_read_b64_tr_b16 a[2:3], v232 offset:39520\n"
  "v_mfma_f32_16x16x32_bf16 v[72:75], a[16:19], a[100:103], v[72:75]\n"
  "s_add_u32 m0, s23, 118784\n"
  "s_nop 0\n"
  "buffer_load_dwordx4 v228, s[8:11], s20 offen offset:0 lds\n"
  "v_mfma_f32_16x16x32_bf16 v[76:79], a[16:19], a[104:107], v[76:79]\n"
  "s_waitcnt lgkmcnt(4)\n"
  "v_mfma_f32_16x16x32_bf16 v[80:83], a[20:23], a[92:95], v[80:83]\n"
  "ds_read_b64_tr_b16 a[4:5], v232 offset:28800\n"
  "v_mfma_f32_16x16x32_bf16 v[84:87], a[20:23], a[96:99], v[84:87]\n"
  "ds_read_b64_tr_b16 a[6:7], v232 offset:32384\n"
  "v_mfma_f32_16x16x32_bf16 v[88:91], a[20:23], a[100:103], v[88:91]\n"
  "buffer_load_dwordx4 v229, s[8:11], s20 offen offset:1024 lds\n"
  "v_mfma_f32_16x16x32_bf16 v[92:95], a[20:23], a[104:107], v[92:95]\n"
  "s_waitcnt lgkmcnt(4)\n"
  "v_mfma_f32_16x16x32_bf16 v[96:99], a[24:27], a[92:95], v[96:99]\n"
  "ds_read_b64_tr_b16 a[8:9], v232 offset:35968\n"
  "v_mfma_f32_16x16x32_bf16 v[100:103], a[24:27], a[96:99], v[100:103]\n"
  "ds_read_b64_tr_b16 a[10:11], v232 offset:39552\n"
  "v_mfma_f32_16x16x32_bf16 v[104:107], a[24:27], a[100:103], v[104:107]\n"
  "v_mfma_f32_16x16x32_bf16 v[108:111], a[24:27], a[104:107], v[108:111]\n"
  "s_waitcnt lgkmcnt(4)\n"
  "v_mfma_f32_16x16x32_bf16 v[112:115], a[0:3], a[92:95], v[112:115]\n"
  "ds_read_b64_tr_b16 a[12:13], v232 offset:28832\n"
  "v_mfma_f32_16x16x32_bf16 v[116:119], a[0:3], a[96:99], v[116:119]\n"
  "ds_read_b64_tr_b16 a[14:15], v232 offset:32416\n"
  "v_mfma_f32_16x16x32_bf16 v[120:123], a[0:3], a[100:103], v[120:123]\n"
  "buffer_load_dwordx4 v230, s[8:11], s20 offen offset:2048 lds\n"
  "v_mfma_f32_16x16x32_bf16 v[124:127], a[0:3], a[104:107], v[124:127]\n"
  "s_waitcnt lgkmcnt(4)\n"
  "v_mfma_f32_16x16x32_bf16 v[128:131], a[4:7], a[92:95], v[128:131]\n"
  "ds_read_b64_tr_b16 a[16:17], v232 offset:36000\n"
  "v_mfma_f32_16x16x32_bf16 v[132:135], a[4:7], a[96:99], v[132:135]\n"
  "ds_read_b64_tr_b16 a[18:19], v232 offset:39584\n"
  "v_mfma_f32_16x16x32_bf16 v[136:139], a[4:7], a[100:103], v[136:139]\n"
  "v_mfma_f32_16x16x32_bf16 v[140:143], a[4:7], a[104:107], v[140:143]\n"
  "s_waitcnt lgkmcnt(4)\n"
  "v_mfma_f32_16x16x32_bf16 v[144:147], a[8:11], a[92:95], v[144:147]\n"
  "ds_read_b64_tr_b16 a[20:21], v232 offset:28864\n"
  "v_mfma_f32_16x16x32_bf16 v[148:151], a[8:11], a[96:99], v[148:151]\n"
  "ds_read_b64_tr_b16 a[22:23], v232 offset:32448\n"
  "v_mfma_f32_16x16x32_bf16 v[152:155], a[8:11], a[100:103], v[152:155]\n"
  "s_mov_b32 s21, 77824\n"
  "buffer_load_dwordx4 a[76:79], v231, s[12:15], s21 offen offset:0 sc1\n"
  "v_mfma_f32_16x16x32_bf16 v[156:159], a[8:11], a[104:107], v[156:159]\n"
  "s_waitcnt lgkmcnt(4)\n"
  "v_mfma_f32_16x16x32_bf16 v[160:163], a[12:15], a[92:95], v[160:163]\n"
  "ds_read_b64_tr_b16 a[24:25], v232 offset:36032\n"
  "v_mfma_f32_16x16x32_bf16 v[164:167], a[12:15], a[96:99], v[164:167]\n"
  "ds_read_b64_tr_b16 a[26:27], v232 offset:39616\n"
  "v_mfma_f32_16x16x32_bf16 v[168:171], a[12:15], a[100:103], v[168:171]\n"
  "v_mfma_f32_16x16x32_bf16 v[172:175], a[12:15], a[104:107], v[172:175]\n"
  "s_waitcnt lgkmcnt(4)\n"
  "v_mfma_f32_16x16x32_bf16 v[176:179], a[16:19], a[92:95], v[176:179]\n"
  "ds_read_b64_tr_b16 a[0:1], v232 offset:43008\n"
  "v_mfma_f32_16x16x32_bf16 v[180:183], a[16:19], a[96:99], v[180:183]\n"
  "ds_read_b64_tr_b16 a[2:3], v232 offset:46592\n"
  "v_mfma_f32_16x16x32_bf16 v[184:187], a[16:19], a[100:103], v[184:187]\n"
  "buffer_load_dwordx4 a[80:83], v231, s[12:15], s21 offen offset:1024 sc1\n"
  "v_mfma_f32_16x16x32_bf16 v[188:191], a[16:19], a[104:107], v[188:191]\n"
  "s_waitcnt lgkmcnt(4)\n"
  "v_mfma_f32_16x16x32_bf16 v[192:195], a[20:23], a[92:95], v[192:195]\n"
  "ds_read_b64_tr_b16 a[4:5], v232 offset:50176\n"
  "v_mfma_f32_16x16x32_bf16 v[196:199], a[20:23], a[96:99], v[196:199]\n"
  "ds_read_b64_tr_b16 a[6:7], v232 offset:53760\n"
  "v_mfma_f32_16x16x32_bf16 v[200:203], a[20:23], a[100:103], v[200:203]\n"
  "buffer_load_dwordx4 a[84:87], v231, s[12:15], s21 offen offset:2048 sc1\n"
  "v_mfma_f32_16x16x32_bf16 v[204:207], a[20:23], a[104:107], v[204:207]\n"
  "s_waitcnt lgkmcnt(4)\n"
  "v_mfma_f32_16x16x32_bf16 v[208:211], a[24:27], a[92:95], v[208:211]\n"
  "ds_read_b64_tr_b16 a[8:9], v232 offset:43040\n"
  "v_mfma_f32_16x16x32_bf16 v[212:215], a[24:27], a[96:99], v[212:215]\n"
  "ds_read_b64_tr_b16 a[10:11], v232 offset:46624\n"
  "v_mfma_f32_16x16x32_bf16 v[216:219], a[24:27], a[100:103], v[216:219]\n"
  "v_mfma_f32_16x16x32_bf16 v[220:223], a[24:27], a[104:107], v[220:223]\n"
  "s_waitcnt vmcnt(44) lgkmcnt(4)\n"
  "v_mfma_f32_16x16x32_bf16 v[0:3], a[0:3], a[108:111], v[0:3]\n"
  "ds_read_b64_tr_b16 a[12:13], v232 offset:50208\n"
  "v_mfma_f32_16x16x32_bf16 v[4:7], a[0:3], a[112:115], v[4:7]\n"
  "ds_read_b64_tr_b16 a[14:15], v232 offset:53792\n"
  "v_mfma_f32_16x16x32_bf16 v[8:11], a[0:3], a[116:119], v[8:11]\n"
  "buffer_load_dwordx4 a[88:91], v231, s[12:15], s21 offen offset:3072 sc1\n"
  "v_mfma_f32_16x16x32_bf16 v[12:15], a[0:3], a[120:123], v[12:15]\n"
  "s_waitcnt lgkmcnt(4)\n"
  "v_mfma_f32_16x16x32_bf16 v[16:19], a[4:7], a[108:111], v[16:19]\n"
  "ds_read_b64_tr_b16 a[16:17], v232 offset:43072\n"
  "v_mfma_f32_16x16x32_bf16 v[20:23], a[4:7], a[112:115], v[20:23]\n"
  "ds_read_b64_tr_b16 a[18:19], v232 offset:46656\n"
  "v_mfma_f32_16x16x32_bf16 v[24:27], a[4:7], a[116:119], v[24:27]\n"
  "v_mfma_f32_16x16x32_bf16 v[28:31], a[4:7], a[120:123], v[28:31]\n"
  "s_waitcnt lgkmcnt(4)\n"
  "v_mfma_f32_16x16x32_bf16 v[32:35], a[8:11], a[108:111], v[32:35]\n"
  "ds_read_b64_tr_b16 a[20:21], v232 offset:50240\n"
  "v_mfma_f32_16x16x32_bf16 v[36:39], a[8:11], a[112:115], v[36:39]\n"
  "ds_read_b64_tr_b16 a[22:23], v232 offset:53824\n"
  "v_mfma_f32_16x16x32_bf16 v[40:43], a[8:11], a[116:119], v[40:43]\n"
  "s_mov_b32 s21, 81920\n"
  "buffer_load_dwordx4 a[92:95], v231, s[12:15], s21 offen offset:0 sc1\n"
  "v_mfma_f32_16x16x32_bf16 v[44:47], a[8:11], a[120:123], v[44:47]\n"
  "s_waitcnt lgkmcnt(4)\n"
  "v_mfma_f32_16x16x32_bf16 v[48:51], a[12:15], a[108:111], v[48:51]\n"
  "ds_read_b64_tr_b16 a[24:25], v232 offset:43104\n"
  "v_mfma_f32_16x16x32_bf16 v[52:55], a[12:15], a[112:115], v[52:55]\n"
  "ds_read_b64_tr_b16 a[26:27], v232 offset:46688\n"
  "v_mfma_f32_16x16x32_bf16 v[56:59], a[12:15], a[116:119], v[56:59]\n"
  "buffer_load_dwordx4 a[96:99], v231, s[12:15], s21 offen offset:1024 sc1\n"
  "v_mfma_f32_16x16x32_bf16 v[60:63], a[12:15], a[120:123], v[60:63]\n"
  "s_waitcnt lgkmcnt(4)\n"
  "v_mfma_f32_16x16x32_bf16 v[64:67], a[16:19], a[108:111], v[64:67]\n"
  "ds_read_b64_tr_b16 a[0:1], v232 offset:50272\n"
  "v_mfma_f32_16x16x32_bf16 v[68:71], a[16:19], a[112:115], v[68:71]\n"
  "ds_read_b64_tr_b16 a[2:3], v232 offset:53856\n"
  "v_mfma_f32_16x16x32_bf16 v[72:75], a[16:19], a[116:119], v[72:75]\n"
  "v_mfma_f32_16x16x32_bf16 v[76:79], a[16:19], a[120:123], v[76:79]\n"
  "s_waitcnt lgkmcnt(4)\n"
  "v_mfma_f32_16x16x32_bf16 v[80:83], a[20:23], a[108:111], v[80:83]\n"
  "ds_read_b64_tr_b16 a[4:5], v232 offset:43136\n"
  "v_mfma_f32_16x16x32_bf16 v[84:87], a[20:23], a[112:115], v[84:87]\n"
  "ds_read_b64_tr_b16 a[6:7], v232 offset:46720\n"
  "v_mfma_f32_16x16x32_bf16 v[88:91], a[20:23], a[116:119], v[88:91]\n"
  "buffer_load_dwordx4 a[100:103], v231, s[12:15], s21 offen offset:2048 sc1\n"
  "v_mfma_f32_16x16x32_bf16 v[92:95], a[20:23], a[120:123], v[92:95]\n"
  "s_waitcnt lgkmcnt(4)\n"
  "v_mfma_f32_16x16x32_bf16 v[96:99], a[24:27], a[108:111], v[96:99]\n"
  "ds_read_b64_tr_b16 a[8:9], v232 offset:50304\n"
  "v_mfma_f32_16x16x32_bf16 v[100:103], a[24:27], a[112:115], v[100:103]\n"
  "ds_read_b64_tr_b16 a[10:11], v232 offset:53888\n"
  "v_mfma_f32_16x16x32_bf16 v[104:107], a[24:27], a[116:119], v[104:107]\n"
  "v_mfma_f32_16x16x32_bf16 v[108:111], a[24:27], a[120:123], v[108:111]\n"
  "s_waitcnt lgkmcnt(4)\n"
  "v_mfma_f32_16x16x32_bf16 v[112:115], a[0:3], a[108:111], v[112:115]\n"
  "ds_read_b64_tr_b16 a[12:13], v232 offset:43168\n"
  "v_mfma_f32_16x16x32_bf16 v[116:119], a[0:3], a[112:115], v[116:119]\n"
  "ds_read_b64_tr_b16 a[14:15], v232 offset:46752\n"
  "v_mfma_f32_16x16x32_bf16 v[120:123], a[0:3], a[116:119], v[120:123]\n"
  "buffer_load_dwordx4 a[104:107], v231, s[12:15], s21 offen offset:3072 sc1\n"
  "v_mfma_f32_16x16x32_bf16 v[124:127], a[0:3], a[120:123], v[124:127]\n"
  "s_waitcnt lgkmcnt(4)\n"
  "v_mfma_f32_16x16x32_bf16 v[128:131], a[4:7], a[108:111], v[128:131]\n"
  "ds_read_b64_tr_b16 a[16:17], v232 offset:50336\n"
  "v_mfma_f32_16x16x32_bf16 v[132:135], a[4:7], a[112:115], v[132:135]\n"
  "ds_read_b64_tr_b16 a[18:19], v232 offset:53920\n"
  "v_mfma_f32_16x16x32_bf16 v[136:139], a[4:7], a[116:119], v[136:139]\n"
  "v_mfma_f32_16x16x32_bf16 v[140:143], a[4:7], a[120:123], v[140:143]\n"
  "s_waitcnt lgkmcnt(4)\n"
  "v_mfma_f32_16x16x32_bf16 v[144:147], a[8:11], a[108:111], v[144:147]\n"
  "ds_read_b64_tr_b16 a[20:21], v232 offset:43200\n"
  "v_mfma_f32_16x16x32_bf16 v[148:151], a[8:11], a[112:115], v[148:151]\n"
  "ds_read_b64_tr_b16 a[22:23], v232 offset:46784\n"
  "v_mfma_f32_16x16x32_bf16 v[152:155], a[8:11], a[116:119], v[152:155]\n"
  "v_mfma_f32_16x16x32_bf16 v[156:159], a[8:11], a[120:123], v[156:159]\n"
  "s_waitcnt lgkmcnt(4)\n"
  "v_mfma_f32_16x16x32_bf16 v[160:163], a[12:15], a[108:111], v[160:163]\n"
  "ds_read_b64_tr_b16 a[24:25], v232 offset:50368\n"
  "v_mfma_f32_16x16x32_bf16 v[164:167], a[12:15], a[112:115], v[164:167]\n"
  "ds_read_b64_tr_b16 a[26:27], v232 offset:53952\n"
  "v_mfma_f32_16x16x32_bf16 v[168:171], a[12:15], a[116:119], v[168:171]\n"
  "v_mfma_f32_16x16x32_bf16 v[172:175], a[12:15], a[120:123], v[172:175]\n"
  "s_waitcnt vmcnt(41)\n"
  "s_barrier\n"
  "s_waitcnt lgkmcnt(4)\n"
  "v_mfma_f32_16x16x32_bf16 v[176:179], a[16:19], a[108:111], v[176:179]\n"
  "ds_read_b64_tr_b16 a[0:1], v233 offset:0\n"
  "v_mfma_f32_16x16x32_bf16 v[180:183], a[16:19], a[112:115], v[180:183]\n"
  "ds_read_b64_tr_b16 a[2:3], v233 offset:3584\n"
  "v_mfma_f32_16x16x32_bf16 v[184:187], a[16:19], a[116:119], v[184:187]\n"
  "s_mov_b32 s20, 4587520\n"
  "s_add_u32 m0, s23, 0\n"
  "s_nop 0\n"
  "buffer_load_dwordx4 v224, s[8:11], s20 offen offset:0 lds\n"
  "v_mfma_f32_16x16x32_bf16 v[188:191], a[16:19], a[120:123], v[188:191]\n"
  "s_waitcnt lgkmcnt(4)\n"
  "v_mfma_f32_16x16x32_bf16 v[192:195], a[20:23], a[108:111], v[192:195]\n"
  "ds_read_b64_tr_b16 a[4:5], v233 offset:7168\n"
  "v_mfma_f32_16x16x32_bf16 v[196:199], a[20:23], a[112:115], v[196:199]\n"
  "ds_read_b64_tr_b16 a[6:7], v233 offset:10752\n"
  "v_mfma_f32_16x16x32_bf16 v[200:203], a[20:23], a[116:119], v[200:203]\n"
  "buffer_load_dwordx4 v225, s[8:11], s20 offen offset:1024 lds\n"
  "v_mfma_f32_16x16x32_bf16 v[204:207], a[20:23], a[120:123], v[204:207]\n"
  "s_waitcnt lgkmcnt(4)\n"
  "v_mfma_f32_16x16x32_bf16 v[208:211], a[24:27], a[108:111], v[208:211]\n"
  "ds_read_b64_tr_b16 a[8:9], v233 offset:32\n"
  "v_mfma_f32_16x16x32_bf16 v[212:215], a[24:27], a[112:115], v[212:215]\n"
  "ds_read_b64_tr_b16 a[10:11], v233 offset:3616\n"
  "v_mfma_f32_16x16x32_bf16 v[216:219], a[24:27], a[116:119], v[216:219]\n"
  "v_mfma_f32_16x16x32_bf16 v[220:223], a[24:27], a[120:123], v[220:223]\n"
  "s_waitcnt vmcnt(40) lgkmcnt(4)\n"
  "v_mfma_f32_16x16x32_bf16 v[0:3], a[0:3], a[124:127], v[0:3]\n"
  "ds_read_b64_tr_b16 a[12:13], v233 offset:7200\n"
  "v_mfma_f32_16x16x32_bf16 v[4:7], a[0:3], a[128:131], v[4:7]\n"
  "ds_read_b64_tr_b16 a[14:15], v233 offset:10784\n"
  "v_mfma_f32_16x16x32_bf16 v[8:11], a[0:3], a[132:135], v[8:11]\n"
  "buffer_load_dwordx4 v226, s[8:11], s20 offen offset:2048 lds\n"
  "v_mfma_f32_16x16x32_bf16 v[12:15], a[0:3], a[136:139], v[12:15]\n"
  "s_waitcnt lgkmcnt(4)\n"
  "v_mfma_f32_16x16x32_bf16 v[16:19], a[4:7], a[124:127], v[16:19]\n"
  "ds_read_b64_tr_b16 a[16:17], v233 offset:64\n"
  "v_mfma_f32_16x16x32_bf16 v[20:23], a[4:7], a[128:131], v[20:23]\n"
  "ds_read_b64_tr_b16 a[18:19], v233 offset:3648\n"
  "v_mfma_f32_16x16x32_bf16 v[24:27], a[4:7], a[132:135], v[24:27]\n"
  "v_mfma_f32_16x16x32_bf16 v[28:31], a[4:7], a[136:139], v[28:31]\n"
  "s_waitcnt lgkmcnt(4)\n"
  "v_mfma_f32_16x16x32_bf16 v[32:35], a[8:11], a[124:127], v[32:35]\n"
  "ds_read_b64_tr_b16 a[20:21], v233 offset:7232\n"
  "v_mfma_f32_16x16x32_bf16 v[36:39], a[8:11], a[128:131], v[36:39]\n"
  "ds_read_b64_tr_b16 a[22:23], v233 offset:10816\n"
  "v_mfma_f32_16x16x32_bf16 v[40:43], a[8:11], a[132:135], v[40:43]\n"
  "buffer_load_dwordx4 v227, s[8:11], s20 offen offset:3072 lds\n"
  "v_mfma_f32_16x16x32_bf16 v[44:47], a[8:11], a[136:139], v[44:47]\n"
  "s_waitcnt lgkmcnt(4)\n"
  "v_mfma_f32_16x16x32_bf16 v[48:51], a[12:15], a[124:127], v[48:51]\n"
  "ds_read_b64_tr_b16 a[24:25], v233 offset:96\n"
  "v_mfma_f32_16x16x32_bf16 v[52:55], a[12:15], a[128:131], v[52:55]\n"
  "ds_read_b64_tr_b16 a[26:27], v233 offset:3680\n"
  "v_mfma_f32_16x16x32_bf16 v[56:59], a[12:15], a[132:135], v[56:59]\n"
  "v_mfma_f32_16x16x32_bf16 v[60:63], a[12:15], a[136:139], v[60:63]\n"
  "s_waitcnt lgkmcnt(4)\n"
  "v_mfma_f32_16x16x32_bf16 v[64:67], a[16:19], a[124:127], v[64:67]\n"
  "ds_read_b64_tr_b16 a[0:1], v233 offset:7264\n"
  "v_mfma_f32_16x16x32_bf16 v[68:71], a[16:19], a[128:131], v[68:71]\n"
  "ds_read_b64_tr_b16 a[2:3], v233 offset:10848\n"
  "v_mfma_f32_16x16x32_bf16 v[72:75], a[16:19], a[132:135], v[72:75]\n"
  "s_add_u32 m0, s23, 4096\n"
  "s_nop 0\n"
  "buffer_load_dwordx4 v228, s[8:11], s20 offen offset:0 lds\n"
  "v_mfma_f32_16x16x32_bf16 v[76:79], a[16:19], a[136:139], v[76:79]\n"
  "s_waitcnt lgkmcnt(4)\n"
  "v_mfma_f32_16x16x32_bf16 v[80:83], a[20:23], a[124:127], v[80:83]\n"
  "ds_read_b64_tr_b16 a[4:5], v233 offset:128\n"
  "v_mfma_f32_16x16x32_bf16 v[84:87], a[20:23], a[128:131], v[84:87]\n"
  "ds_read_b64_tr_b16 a[6:7], v233 offset:3712\n"
  "v_mfma_f32_16x16x32_bf16 v[88:91], a[20:23], a[132:135], v[88:91]\n"
  "buffer_load_dwordx4 v229, s[8:11], s20 offen offset:1024 lds\n"
  "v_mfma_f32_16x16x32_bf16 v[92:95], a[20:23], a[136:139], v[92:95]\n"
  "s_waitcnt lgkmcnt(4)\n"
  "v_mfma_f32_16x16x32_bf16 v[96:99], a[24:27], a[124:127], v[96:99]\n"
  "ds_read_b64_tr_b16 a[8:9], v233 offset:7296\n"
  "v_mfma_f32_16x16x32_bf16 v[100:103], a[24:27], a[128:131], v[100:103]\n"
  "ds_read_b64_tr_b16 a[10:11], v233 offset:10880\n"
  "v_mfma_f32_16x16x32_bf16 v[104:107], a[24:27], a[132:135], v[104:107]\n"
  "v_mfma_f32_16x16x32_bf16 v[108:111], a[24:27], a[136:139], v[108:111]\n"
  "s_waitcnt lgkmcnt(4)\n"
  "v_mfma_f32_16x16x32_bf16 v[112:115], a[0:3], a[124:127], v[112:115]\n"
  "ds_read_b64_tr_b16 a[12:13], v233 offset:160\n"
  "v_mfma_f32_16x16x32_bf16 v[116:119], a[0:3], a[128:131], v[116:119]\n"
  "ds_read_b64_tr_b16 a[14:15], v233 offset:3744\n"
  "v_mfma_f32_16x16x32_bf16 v[120:123], a[0:3], a[132:135], v[120:123]\n"
  "buffer_load_dwordx4 v230, s[8:11], s20 offen offset:2048 lds\n"
  "v_mfma_f32_16x16x32_bf16 v[124:127], a[0:3], a[136:139], v[124:127]\n"
  "s_waitcnt lgkmcnt(4)\n"
  "v_mfma_f32_16x16x32_bf16 v[128:131], a[4:7], a[124:127], v[128:131]\n"
  "ds_read_b64_tr_b16 a[16:17], v233 offset:7328\n"
  "v_mfma_f32_16x16x32_bf16 v[132:135], a[4:7], a[128:131], v[132:135]\n"
  "ds_read_b64_tr_b16 a[18:19], v233 offset:10912\n"
  "v_mfma_f32_16x16x32_bf16 v[136:139], a[4:7], a[132:135], v[136:139]\n"
  "v_mfma_f32_16x16x32_bf16 v[140:143], a[4:7], a[136:139], v[140:143]\n"
  "s_waitcnt lgkmcnt(4)\n"
  "v_mfma_f32_16x16x32_bf16 v[144:147], a[8:11], a[124:127], v[144:147]\n"
  "ds_read_b64_tr_b16 a[20:21], v233 offset:192\n"
  "v_mfma_f32_16x16x32_bf16 v[148:151], a[8:11], a[128:131], v[148:151]\n"
  "ds_read_b64_tr_b16 a[22:23], v233 offset:3776\n"
  "v_mfma_f32_16x16x32_bf16 v[152:155], a[8:11], a[132:135], v[152:155]\n"
  "s_mov_b32 s21, 86016\n"
  "buffer_load_dwordx4 a[108:111], v231, s[12:15], s21 offen offset:0 sc1\n"
  "v_mfma_f32_16x16x32_bf16 v[156:159], a[8:11], a[136:139], v[156:159]\n"
  "s_waitcnt lgkmcnt(4)\n"
  "v_mfma_f32_16x16x32_bf16 v[160:163], a[12:15], a[124:127], v[160:163]\n"
  "ds_read_b64_tr_b16 a[24:25], v233 offset:7360\n"
  "v_mfma_f32_16x16x32_bf16 v[164:167], a[12:15], a[128:131], v[164:167]\n"
  "ds_read_b64_tr_b16 a[26:27], v233 offset:10944\n"
  "v_mfma_f32_16x16x32_bf16 v[168:171], a[12:15], a[132:135], v[168:171]\n"
  "v_mfma_f32_16x16x32_bf16 v[172:175], a[12:15], a[136:139], v[172:175]\n"
  "s_waitcnt lgkmcnt(4)\n"
  "v_mfma_f32_16x16x32_bf16 v[176:179], a[16:19], a[124:127], v[176:179]\n"
  "ds_read_b64_tr_b16 a[0:1], v233 offset:14336\n"
  "v_mfma_f32_16x16x32_bf16 v[180:183], a[16:19], a[128:131], v[180:183]\n"
  "ds_read_b64_tr_b16 a[2:3], v233 offset:17920\n"
  "v_mfma_f32_16x16x32_bf16 v[184:187], a[16:19], a[132:135], v[184:187]\n"
  "buffer_load_dwordx4 a[112:115], v231, s[12:15], s21 offen offset:1024 sc1\n"
  "v_mfma_f32_16x16x32_bf16 v[188:191], a[16:19], a[136:139], v[188:191]\n"
  "s_waitcnt lgkmcnt(4)\n"
  "v_mfma_f32_16x16x32_bf16 v[192:195], a[20:23], a[124:127], v[192:195]\n"
  "ds_read_b64_tr_b16 a[4:5], v233 offset:21504\n"
  "v_mfma_f32_16x16x32_bf16 v[196:199], a[20:23], a[128:131], v[196:199]\n"
  "ds_read_b64_tr_b16 a[6:7], v233 offset:25088\n"
  "v_mfma_f32_16x16x32_bf16 v[200:203], a[20:23], a[132:135], v[200:203]\n"
  "buffer_load_dwordx4 a[116:119], v231, s[12:15], s21 offen offset:2048 sc1\n"
  "v_mfma_f32_16x16x32_bf16 v[204:207], a[20:23], a[136:139], v[204:207]\n"
  "s_waitcnt lgkmcnt(4)\n"
  "v_mfma_f32_16x16x32_bf16 v[208:211], a[24:27], a[124:127], v[208:211]\n"
  "ds_read_b64_tr_b16 a[8:9], v233 offset:14368\n"
  "v_mfma_f32_16x16x32_bf16 v[212:215], a[24:27], a[128:131], v[212:215]\n"
  "ds_read_b64_tr_b16 a[10:11], v233 offset:17952\n"
  "v_mfma_f32_16x16x32_bf16 v[216:219], a[24:27], a[132:135], v[216:219]\n"
  "v_mfma_f32_16x16x32_bf16 v[220:223], a[24:27], a[136:139], v[220:223]\n"
  "s_waitcnt vmcnt(44) lgkmcnt(4)\n"
  "v_mfma_f32_16x16x32_bf16 v[0:3], a[0:3], a[140:143], v[0:3]\n"
  "ds_read_b64_tr_b16 a[12:13], v233 offset:21536\n"
  "v_mfma_f32_16x16x32_bf16 v[4:7], a[0:3], a[144:147], v[4:7]\n"
  "ds_read_b64_tr_b16 a[14:15], v233 offset:25120\n"
  "v_mfma_f32_16x16x32_bf16 v[8:11], a[0:3], a[148:151], v[8:11]\n"
  "buffer_load_dwordx4 a[120:123], v231, s[12:15], s21 offen offset:3072 sc1\n"
  "v_mfma_f32_16x16x32_bf16 v[12:15], a[0:3], a[152:155], v[12:15]\n"
  "s_waitcnt lgkmcnt(4)\n"
  "v_mfma_f32_16x16x32_bf16 v[16:19], a[4:7], a[140:143], v[16:19]\n"
  "ds_read_b64_tr_b16 a[16:17], v233 offset:14400\n"
  "v_mfma_f32_16x16x32_bf16 v[20:23], a[4:7], a[144:147], v[20:23]\n"
  "ds_read_b64_tr_b16 a[18:19], v233 offset:17984\n"
  "v_mfma_f32_16x16x32_bf16 v[24:27], a[4:7], a[148:151], v[24:27]\n"
  "v_mfma_f32_16x16x32_bf16 v[28:31], a[4:7], a[152:155], v[28:31]\n"
  "s_waitcnt lgkmcnt(4)\n"
  "v_mfma_f32_16x16x32_bf16 v[32:35], a[8:11], a[140:143], v[32:35]\n"
  "ds_read_b64_tr_b16 a[20:21], v233 offset:21568\n"
  "v_mfma_f32_16x16x32_bf16 v[36:39], a[8:11], a[144:147], v[36:39]\n"
  "ds_read_b64_tr_b16 a[22:23], v233 offset:25152\n"
  "v_mfma_f32_16x16x32_bf16 v[40:43], a[8:11], a[148:151], v[40:43]\n"
  "s_mov_b32 s21, 90112\n"
  "buffer_load_dwordx4 a[124:127], v231, s[12:15], s21 offen offset:0 sc1\n"
  "v_mfma_f32_16x16x32_bf16 v[44:47], a[8:11], a[152:155], v[44:47]\n"
  "s_waitcnt lgkmcnt(4)\n"
  "v_mfma_f32_16x16x32_bf16 v[48:51], a[12:15], a[140:143], v[48:51]\n"
  "ds_read_b64_tr_b16 a[24:25], v233 offset:14432\n"
  "v_mfma_f32_16x16x32_bf16 v[52:55], a[12:15], a[144:147], v[52:55]\n"
  "ds_read_b64_tr_b16 a[26:27], v233 offset:18016\n"
  "v_mfma_f32_16x16x32_bf16 v[56:59], a[12:15], a[148:151], v[56:59]\n"
  "buffer_load_dwordx4 a[128:131], v231, s[12:15], s21 offen offset:1024 sc1\n"
  "v_mfma_f32_16x16x32_bf16 v[60:63], a[12:15], a[152:155], v[60:63]\n"
  "s_waitcnt lgkmcnt(4)\n"
  "v_mfma_f32_16x16x32_bf16 v[64:67], a[16:19], a[140:143], v[64:67]\n"
  "ds_read_b64_tr_b16 a[0:1], v233 offset:21600\n"
  "v_mfma_f32_16x16x32_bf16 v[68:71], a[16:19], a[144:147], v[68:71]\n"
  "ds_read_b64_tr_b16 a[2:3], v233 offset:25184\n"
  "v_mfma_f32_16x16x32_bf16 v[72:75], a[16:19], a[148:151], v[72:75]\n"
  "v_mfma_f32_16x16x32_bf16 v[76:79], a[16:19], a[152:155], v[76:79]\n"
  "s_waitcnt lgkmcnt(4)\n"
  "v_mfma_f32_16x16x32_bf16 v[80:83], a[20:23], a[140:143], v[80:83]\n"
  "ds_read_b64_tr_b16 a[4:5], v233 offset:14464\n"
  "v_mfma_f32_16x16x32_bf16 v[84:87], a[20:23], a[144:147], v[84:87]\n"
  "ds_read_b64_tr_b16 a[6:7], v233 offset:18048\n"
  "v_mfma_f32_16x16x32_bf16 v[88:91], a[20:23], a[148:151], v[88:91]\n"
  "buffer_load_dwordx4 a[132:135], v231, s[12:15], s21 offen offset:2048 sc1\n"
  "v_mfma_f32_16x16x32_bf16 v[92:95], a[20:23], a[152:155], v[92:95]\n"
  "s_waitcnt lgkmcnt(4)\n"
  "v_mfma_f32_16x16x32_bf16 v[96:99], a[24:27], a[140:143], v[96:99]\n"
  "ds_read_b64_tr_b16 a[8:9], v233 offset:21632\n"
  "v_mfma_f32_16x16x32_bf16 v[100:103], a[24:27], a[144:147], v[100:103]\n"
  "ds_read_b64_tr_b16 a[10:11], v233 offset:25216\n"
  "v_mfma_f32_16x16x32_bf16 v[104:107], a[24:27], a[148:151], v[104:107]\n"
  "v_mfma_f32_16x16x32_bf16 v[108:111], a[24:27], a[152:155], v[108:111]\n"
  "s_waitcnt lgkmcnt(4)\n"
  "v_mfma_f32_16x16x32_bf16 v[112:115], a[0:3], a[140:143], v[112:115]\n"
  "ds_read_b64_tr_b16 a[12:13], v233 offset:14496\n"
  "v_mfma_f32_16x16x32_bf16 v[116:119], a[0:3], a[144:147], v[116:119]\n"
  "ds_read_b64_tr_b16 a[14:15], v233 offset:18080\n"
  "v_mfma_f32_16x16x32_bf16 v[120:123], a[0:3], a[148:151], v[120:123]\n"
  "buffer_load_dwordx4 a[136:139], v231, s[12:15], s21 offen offset:3072 sc1\n"
  "v_mfma_f32_16x16x32_bf16 v[124:127], a[0:3], a[152:155], v[124:127]\n"
  "s_waitcnt lgkmcnt(4)\n"
  "v_mfma_f32_16x16x32_bf16 v[128:131], a[4:7], a[140:143], v[128:131]\n"
  "ds_read_b64_tr_b16 a[16:17], v233 offset:21664\n"
  "v_mfma_f32_16x16x32_bf16 v[132:135], a[4:7], a[144:147], v[132:135]\n"
  "ds_read_b64_tr_b16 a[18:19], v233 offset:25248\n"
  "v_mfma_f32_16x16x32_bf16 v[136:139], a[4:7], a[148:151], v[136:139]\n"
  "v_mfma_f32_16x16x32_bf16 v[140:143], a[4:7], a[152:155], v[140:143]\n"
  "s_waitcnt lgkmcnt(4)\n"
  "v_mfma_f32_16x16x32_bf16 v[144:147], a[8:11], a[140:143], v[144:147]\n"
  "ds_read_b64_tr_b16 a[20:21], v233 offset:14528\n"
  "v_mfma_f32_16x16x32_bf16 v[148:151], a[8:11], a[144:147], v[148:151]\n"
  "ds_read_b64_tr_b16 a[22:23], v233 offset:18112\n"
  "v_mfma_f32_16x16x32_bf16 v[152:155], a[8:11], a[148:151], v[152:155]\n"
  "v_mfma_f32_16x16x32_bf16 v[156:159], a[8:11], a[152:155], v[156:159]\n"
  "s_waitcnt lgkmcnt(4)\n"
  "v_mfma_f32_16x16x32_bf16 v[160:163], a[12:15], a[140:143], v[160:163]\n"
  "ds_read_b64_tr_b16 a[24:25], v233 offset:21696\n"
  "v_mfma_f32_16x16x32_bf16 v[164:167], a[12:15], a[144:147], v[164:167]\n"
  "ds_read_b64_tr_b16 a[26:27], v233 offset:25280\n"
  "v_mfma_f32_16x16x32_bf16 v[168:171], a[12:15], a[148:151], v[168:171]\n"
  "v_mfma_f32_16x16x32_bf16 v[172:175], a[12:15], a[152:155], v[172:175]\n"
  "s_waitcnt vmcnt(38)\n"
  "s_barrier\n"
  "s_waitcnt lgkmcnt(4)\n"
  "v_mfma_f32_16x16x32_bf16 v[176:179], a[16:19], a[140:143], v[176:179]\n"
  "ds_read_b64_tr_b16 a[0:1], v233 offset:28672\n"
  "v_mfma_f32_16x16x32_bf16 v[180:183], a[16:19], a[144:147], v[180:183]\n"
  "ds_read_b64_tr_b16 a[2:3], v233 offset:32256\n"
  "v_mfma_f32_16x16x32_bf16 v[184:187], a[16:19], a[148:151], v[184:187]\n"
  "s_mov_b32 s20, 5046272\n"
  "s_add_u32 m0, s23, 28672\n"
  "s_nop 0\n"
  "buffer_load_dwordx4 v224, s[8:11], s20 offen offset:0 lds\n"
  "v_mfma_f32_16x16x32_bf16 v[188:191], a[16:19], a[152:155], v[188:191]\n"
  "s_waitcnt lgkmcnt(4)\n"
  "v_mfma_f32_16x16x32_bf16 v[192:195], a[20:23], a[140:143], v[192:195]\n"
  "ds_read_b64_tr_b16 a[4:5], v233 offset:35840\n"
  "v_mfma_f32_16x16x32_bf16 v[196:199], a[20:23], a[144:147], v[196:199]\n"
  "ds_read_b64_tr_b16 a[6:7], v233 offset:39424\n"
  "v_mfma_f32_16x16x32_bf16 v[200:203], a[20:23], a[148:151], v[200:203]\n"
  "buffer_load_dwordx4 v225, s[8:11], s20 offen offset:1024 lds\n"
  "v_mfma_f32_16x16x32_bf16 v[204:207], a[20:23], a[152:155], v[204:207]\n"
  "s_waitcnt lgkmcnt(4)\n"
  "v_mfma_f32_16x16x32_bf16 v[208:211], a[24:27], a[140:143], v[208:211]\n"
  "ds_read_b64_tr_b16 a[8:9], v233 offset:28704\n"
  "v_mfma_f32_16x16x32_bf16 v[212:215], a[24:27], a[144:147], v[212:215]\n"
  "ds_read_b64_tr_b16 a[10:11], v233 offset:32288\n"
  "v_mfma_f32_16x16x32_bf16 v[216:219], a[24:27], a[148:151], v[216:219]\n"
  "v_mfma_f32_16x16x32_bf16 v[220:223], a[24:27], a[152:155], v[220:223]\n"
  "s_waitcnt lgkmcnt(4)\n"
  "v_mfma_f32_16x16x32_bf16 v[0:3], a[0:3], a[28:31], v[0:3]\n"
  "ds_read_b64_tr_b16 a[12:13], v233 offset:35872\n"
  "v_mfma_f32_16x16x32_bf16 v[4:7], a[0:3], a[32:35], v[4:7]\n"
  "ds_read_b64_tr_b16 a[14:15], v233 offset:39456\n"
  "v_mfma_f32_16x16x32_bf16 v[8:11], a[0:3], a[36:39], v[8:11]\n"
  "buffer_load_dwordx4 v226, s[8:11], s20 offen offset:2048 lds\n"
  "v_mfma_f32_16x16x32_bf16 v[12:15], a[0:3], a[40:43], v[12:15]\n"
  "s_waitcnt lgkmcnt(4)\n"
  "v_mfma_f32_16x16x32_bf16 v[16:19], a[4:7], a[28:31], v[16:19]\n"
  "ds_read_b64_tr_b16 a[16:17], v233 offset:28736\n"
  "v_mfma_f32_16x16x32_bf16 v[20:23], a[4:7], a[32:35], v[20:23]\n"
  "ds_read_b64_tr_b16 a[18:19], v233 offset:32320\n"
  "v_mfma_f32_16x16x32_bf16 v[24:27], a[4:7], a[36:39], v[24:27]\n"
  "v_mfma_f32_16x16x32_bf16 v[28:31], a[4:7], a[40:43], v[28:31]\n"
  "s_waitcnt lgkmcnt(4)\n"
  "v_mfma_f32_16x16x32_bf16 v[32:35], a[8:11], a[28:31], v[32:35]\n"
  "ds_read_b64_tr_b16 a[20:21], v233 offset:35904\n"
  "v_mfma_f32_16x16x32_bf16 v[36:39], a[8:11], a[32:35], v[36:39]\n"
  "ds_read_b64_tr_b16 a[22:23], v233 offset:39488\n"
  "v_mfma_f32_16x16x32_bf16 v[40:43], a[8:11], a[36:39], v[40:43]\n"
  "buffer_load_dwordx4 v227, s[8:11], s20 offen offset:3072 lds\n"
  "v_mfma_f32_16x16x32_bf16 v[44:47], a[8:11], a[40:43], v[44:47]\n"
  "s_waitcnt lgkmcnt(4)\n"
  "v_mfma_f32_16x16x32_bf16 v[48:51], a[12:15], a[28:31], v[48:51]\n"
  "ds_read_b64_tr_b16 a[24:25], v233 offset:28768\n"
  "v_mfma_f32_16x16x32_bf16 v[52:55], a[12:15], a[32:35], v[52:55]\n"
  "ds_read_b64_tr_b16 a[26:27], v233 offset:32352\n"
  "v_mfma_f32_16x16x32_bf16 v[56:59], a[12:15], a[36:39], v[56:59]\n"
  "v_mfma_f32_16x16x32_bf16 v[60:63], a[12:15], a[40:43], v[60:63]\n"
  "s_waitcnt lgkmcnt(4)\n"
  "v_mfma_f32_16x16x32_bf16 v[64:67], a[16:19], a[28:31], v[64:67]\n"
  "ds_read_b64_tr_b16 a[0:1], v233 offset:35936\n"
  "v_mfma_f32_16x16x32_bf16 v[68:71], a[16:19], a[32:35], v[68:71]\n"
  "ds_read_b64_tr_b16 a[2:3], v233 offset:39520\n"
  "v_mfma_f32_16x16x32_bf16 v[72:75], a[16:19], a[36:39], v[72:75]\n"
  "s_add_u32 m0, s23, 32768\n"
  "s_nop 0\n"
  "buffer_load_dwordx4 v228, s[8:11], s20 offen offset:0 lds\n"
  "v_mfma_f32_16x16x32_bf16 v[76:79], a[16:19], a[40:43], v[76:79]\n"
  "s_waitcnt lgkmcnt(4)\n"
  "v_mfma_f32_16x16x32_bf16 v[80:83], a[20:23], a[28:31], v[80:83]\n"
  "ds_read_b64_tr_b16 a[4:5], v233 offset:28800\n"
  "v_mfma_f32_16x16x32_bf16 v[84:87], a[20:23], a[32:35], v[84:87]\n"
  "ds_read_b64_tr_b16 a[6:7], v233 offset:32384\n"
  "v_mfma_f32_16x16x32_bf16 v[88:91], a[20:23], a[36:39], v[88:91]\n"
  "buffer_load_dwordx4 v229, s[8:11], s20 offen offset:1024 lds\n"
  "v_mfma_f32_16x16x32_bf16 v[92:95], a[20:23], a[40:43], v[92:95]\n"
  "s_waitcnt lgkmcnt(4)\n"
  "v_mfma_f32_16x16x32_bf16 v[96:99], a[24:27], a[28:31], v[96:99]\n"
  "ds_read_b64_tr_b16 a[8:9], v233 offset:35968\n"
  "v_mfma_f32_16x16x32_bf16 v[100:103], a[24:27], a[32:35], v[100:103]\n"
  "ds_read_b64_tr_b16 a[10:11], v233 offset:39552\n"
  "v_mfma_f32_16x16x32_bf16 v[104:107], a[24:27], a[36:39], v[104:107]\n"
  "v_mfma_f32_16x16x32_bf16 v[108:111], a[24:27], a[40:43], v[108:111]\n"
  "s_waitcnt lgkmcnt(4)\n"
  "v_mfma_f32_16x16x32_bf16 v[112:115], a[0:3], a[28:31], v[112:115]\n"
  "ds_read_b64_tr_b16 a[12:13], v233 offset:28832\n"
  "v_mfma_f32_16x16x32_bf16 v[116:119], a[0:3], a[32:35], v[116:119]\n"
  "ds_read_b64_tr_b16 a[14:15], v233 offset:32416\n"
  "v_mfma_f32_16x16x32_bf16 v[120:123], a[0:3], a[36:39], v[120:123]\n"
  "buffer_load_dwordx4 v230, s[8:11], s20 offen offset:2048 lds\n"
  "v_mfma_f32_16x16x32_bf16 v[124:127], a[0:3], a[40:43], v[124:127]\n"
  "s_waitcnt lgkmcnt(4)\n"
  "v_mfma_f32_16x16x32_bf16 v[128:131], a[4:7], a[28:31], v[128:131]\n"
  "ds_read_b64_tr_b16 a[16:17], v233 offset:36000\n"
  "v_mfma_f32_16x16x32_bf16 v[132:135], a[4:7], a[32:35], v[132:135]\n"
  "ds_read_b64_tr_b16 a[18:19], v233 offset:39584\n"
  "v_mfma_f32_16x16x32_bf16 v[136:139], a[4:7], a[36:39], v[136:139]\n"
  "v_mfma_f32_16x16x32_bf16 v[140:143], a[4:7], a[40:43], v[140:143]\n"
  "s_waitcnt lgkmcnt(4)\n"
  "v_mfma_f32_16x16x32_bf16 v[144:147], a[8:11], a[28:31], v[144:147]\n"
  "ds_read_b64_tr_b16 a[20:21], v233 offset:28864\n"
  "v_mfma_f32_16x16x32_bf16 v[148:151], a[8:11], a[32:35], v[148:151]\n"
  "ds_read_b64_tr_b16 a[22:23], v233 offset:32448\n"
  "v_mfma_f32_16x16x32_bf16 v[152:155], a[8:11], a[36:39], v[152:155]\n"
  "s_mov_b32 s21, 94208\n"
  "buffer_load_dwordx4 a[140:143], v231, s[12:15], s21 offen offset:0 sc1\n"
  "v_mfma_f32_16x16x32_bf16 v[156:159], a[8:11], a[40:43], v[156:159]\n"
  "s_waitcnt lgkmcnt(4)\n"
  "v_mfma_f32_16x16x32_bf16 v[160:163], a[12:15], a[28:31], v[160:163]\n"
  "ds_read_b64_tr_b16 a[24:25], v233 offset:36032\n"
  "v_mfma_f32_16x16x32_bf16 v[164:167], a[12:15], a[32:35], v[164:167]\n"
  "ds_read_b64_tr_b16 a[26:27], v233 offset:39616\n"
  "v_mfma_f32_16x16x32_bf16 v[168:171], a[12:15], a[36:39], v[168:171]\n"
  "v_mfma_f32_16x16x32_bf16 v[172:175], a[12:15], a[40:43], v[172:175]\n"
  "s_waitcnt lgkmcnt(4)\n"
  "v_mfma_f32_16x16x32_bf16 v[176:179], a[16:19], a[28:31], v[176:179]\n"
  "ds_read_b64_tr_b16 a[0:1], v233 offset:43008\n"
  "v_mfma_f32_16x16x32_bf16 v[180:183], a[16:19], a[32:35], v[180:183]\n"
  "ds_read_b64_tr_b16 a[2:3], v233 offset:46592\n"
  "v_mfma_f32_16x16x32_bf16 v[184:187], a[16:19], a[36:39], v[184:187]\n"
  "buffer_load_dwordx4 a[144:147], v231, s[12:15], s21 offen offset:1024 sc1\n"
  "v_mfma_f32_16x16x32_bf16 v[188:191], a[16:19], a[40:43], v[188:191]\n"
  "s_waitcnt lgkmcnt(4)\n"
  "v_mfma_f32_16x16x32_bf16 v[192:195], a[20:23], a[28:31], v[192:195]\n"
  "ds_read_b64_tr_b16 a[4:5], v233 offset:50176\n"
  "v_mfma_f32_16x16x32_bf16 v[196:199], a[20:23], a[32:35], v[196:199]\n"
  "ds_read_b64_tr_b16 a[6:7], v233 offset:53760\n"
  "v_mfma_f32_16x16x32_bf16 v[200:203], a[20:23], a[36:39], v[200:203]\n"
  "buffer_load_dwordx4 a[148:151], v231, s[12:15], s21 offen offset:2048 sc1\n"
  "v_mfma_f32_16x16x32_bf16 v[204:207], a[20:23], a[40:43], v[204:207]\n"
  "s_waitcnt lgkmcnt(4)\n"
  "v_mfma_f32_16x16x32_bf16 v[208:211], a[24:27], a[28:31], v[208:211]\n"
  "ds_read_b64_tr_b16 a[8:9], v233 offset:43040\n"
  "v_mfma_f32_16x16x32_bf16 v[212:215], a[24:27], a[32:35], v[212:215]\n"
  "ds_read_b64_tr_b16 a[10:11], v233 offset:46624\n"
  "v_mfma_f32_16x16x32_bf16 v[216:219], a[24:27], a[36:39], v[216:219]\n"
  "v_mfma_f32_16x16x32_bf16 v[220:223], a[24:27], a[40:43], v[220:223]\n"
  "s_waitcnt vmcnt(44) lgkmcnt(4)\n"
  "v_mfma_f32_16x16x32_bf16 v[0:3], a[0:3], a[44:47], v[0:3]\n"
  "ds_read_b64_tr_b16 a[12:13], v233 offset:50208\n"
  "v_mfma_f32_16x16x32_bf16 v[4:7], a[0:3], a[48:51], v[4:7]\n"
  "ds_read_b64_tr_b16 a[14:15], v233 offset:53792\n"
  "v_mfma_f32_16x16x32_bf16 v[8:11], a[0:3], a[52:55], v[8:11]\n"
  "buffer_load_dwordx4 a[152:155], v231, s[12:15], s21 offen offset:3072 sc1\n"
  "v_mfma_f32_16x16x32_bf16 v[12:15], a[0:3], a[56:59], v[12:15]\n"
  "s_waitcnt lgkmcnt(4)\n"
  "v_mfma_f32_16x16x32_bf16 v[16:19], a[4:7], a[44:47], v[16:19]\n"
  "ds_read_b64_tr_b16 a[16:17], v233 offset:43072\n"
  "v_mfma_f32_16x16x32_bf16 v[20:23], a[4:7], a[48:51], v[20:23]\n"
  "ds_read_b64_tr_b16 a[18:19], v233 offset:46656\n"
  "v_mfma_f32_16x16x32_bf16 v[24:27], a[4:7], a[52:55], v[24:27]\n"
  "v_mfma_f32_16x16x32_bf16 v[28:31], a[4:7], a[56:59], v[28:31]\n"
  "s_waitcnt lgkmcnt(4)\n"
  "v_mfma_f32_16x16x32_bf16 v[32:35], a[8:11], a[44:47], v[32:35]\n"
  "ds_read_b64_tr_b16 a[20:21], v233 offset:50240\n"
  "v_mfma_f32_16x16x32_bf16 v[36:39], a[8:11], a[48:51], v[36:39]\n"
  "ds_read_b64_tr_b16 a[22:23], v233 offset:53824\n"
  "v_mfma_f32_16x16x32_bf16 v[40:43], a[8:11], a[52:55], v[40:43]\n"
  "s_mov_b32 s21, 98304\n"
  "buffer_load_dwordx4 a[28:31], v231, s[12:15], s21 offen offset:0 sc1\n"
  "v_mfma_f32_16x16x32_bf16 v[44:47], a[8:11], a[56:59], v[44:47]\n"
  "s_waitcnt lgkmcnt(4)\n"
  "v_mfma_f32_16x16x32_bf16 v[48:51], a[12:15], a[44:47], v[48:51]\n"
  "ds_read_b64_tr_b16 a[24:25], v233 offset:43104\n"
  "v_mfma_f32_16x16x32_bf16 v[52:55], a[12:15], a[48:51], v[52:55]\n"
  "ds_read_b64_tr_b16 a[26:27], v233 offset:46688\n"
  "v_mfma_f32_16x16x32_bf16 v[56:59], a[12:15], a[52:55], v[56:59]\n"
  "buffer_load_dwordx4 a[32:35], v231, s[12:15], s21 offen offset:1024 sc1\n"
  "v_mfma_f32_16x16x32_bf16 v[60:63], a[12:15], a[56:59], v[60:63]\n"
  "s_waitcnt lgkmcnt(4)\n"
  "v_mfma_f32_16x16x32_bf16 v[64:67], a[16:19], a[44:47], v[64:67]\n"
  "ds_read_b64_tr_b16 a[0:1], v233 offset:50272\n"
  "v_mfma_f32_16x16x32_bf16 v[68:71], a[16:19], a[48:51], v[68:71]\n"
  "ds_read_b64_tr_b16 a[2:3], v233 offset:53856\n"
  "v_mfma_f32_16x16x32_bf16 v[72:75], a[16:19], a[52:55], v[72:75]\n"
  "v_mfma_f32_16x16x32_bf16 v[76:79], a[16:19], a[56:59], v[76:79]\n"
  "s_waitcnt lgkmcnt(4)\n"
  "v_mfma_f32_16x16x32_bf16 v[80:83], a[20:23], a[44:47], v[80:83]\n"
  "ds_read_b64_tr_b16 a[4:5], v233 offset:43136\n"
  "v_mfma_f32_16x16x32_bf16 v[84:87], a[20:23], a[48:51], v[84:87]\n"
  "ds_read_b64_tr_b16 a[6:7], v233 offset:46720\n"
  "v_mfma_f32_16x16x32_bf16 v[88:91], a[20:23], a[52:55], v[88:91]\n"
  "buffer_load_dwordx4 a[36:39], v231, s[12:15], s21 offen offset:2048 sc1\n"
  "v_mfma_f32_16x16x32_bf16 v[92:95], a[20:23], a[56:59], v[92:95]\n"
  "s_waitcnt lgkmcnt(4)\n"
  "v_mfma_f32_16x16x32_bf16 v[96:99], a[24:27], a[44:47], v[96:99]\n"
  "ds_read_b64_tr_b16 a[8:9], v233 offset:50304\n"
  "v_mfma_f32_16x16x32_bf16 v[100:103], a[24:27], a[48:51], v[100:103]\n"
  "ds_read_b64_tr_b16 a[10:11], v233 offset:53888\n"
  "v_mfma_f32_16x16x32_bf16 v[104:107], a[24:27], a[52:55], v[104:107]\n"
  "v_mfma_f32_16x16x32_bf16 v[108:111], a[24:27], a[56:59], v[108:111]\n"
  "s_waitcnt lgkmcnt(4)\n"
  "v_mfma_f32_16x16x32_bf16 v[112:115], a[0:3], a[44:47], v[112:115]\n"
  "ds_read_b64_tr_b16 a[12:13], v233 offset:43168\n"
  "v_mfma_f32_16x16x32_bf16 v[116:119], a[0:3], a[48:51], v[116:119]\n"
  "ds_read_b64_tr_b16 a[14:15], v233 offset:46752\n"
  "v_mfma_f32_16x16x32_bf16 v[120:123], a[0:3], a[52:55], v[120:123]\n"
  "buffer_load_dwordx4 a[40:43], v231, s[12:15], s21 offen offset:3072 sc1\n"
  "v_mfma_f32_16x16x32_bf16 v[124:127], a[0:3], a[56:59], v[124:127]\n"
  "s_waitcnt lgkmcnt(4)\n"
  "v_mfma_f32_16x16x32_bf16 v[128:131], a[4:7], a[44:47], v[128:131]\n"
  "ds_read_b64_tr_b16 a[16:17], v233 offset:50336\n"
  "v_mfma_f32_16x16x32_bf16 v[132:135], a[4:7], a[48:51], v[132:135]\n"
  "ds_read_b64_tr_b16 a[18:19], v233 offset:53920\n"
  "v_mfma_f32_16x16x32_bf16 v[136:139], a[4:7], a[52:55], v[136:139]\n"
  "v_mfma_f32_16x16x32_bf16 v[140:143], a[4:7], a[56:59], v[140:143]\n"
  "s_waitcnt lgkmcnt(4)\n"
  "v_mfma_f32_16x16x32_bf16 v[144:147], a[8:11], a[44:47], v[144:147]\n"
  "ds_read_b64_tr_b16 a[20:21], v233 offset:43200\n"
  "v_mfma_f32_16x16x32_bf16 v[148:151], a[8:11], a[48:51], v[148:151]\n"
  "ds_read_b64_tr_b16 a[22:23], v233 offset:46784\n"
  "v_mfma_f32_16x16x32_bf16 v[152:155], a[8:11], a[52:55], v[152:155]\n"
  "v_mfma_f32_16x16x32_bf16 v[156:159], a[8:11], a[56:59], v[156:159]\n"
  "s_waitcnt lgkmcnt(4)\n"
  "v_mfma_f32_16x16x32_bf16 v[160:163], a[12:15], a[44:47], v[160:163]\n"
  "ds_read_b64_tr_b16 a[24:25], v233 offset:50368\n"
  "v_mfma_f32_16x16x32_bf16 v[164:167], a[12:15], a[48:51], v[164:167]\n"
  "ds_read_b64_tr_b16 a[26:27], v233 offset:53952\n"
  "v_mfma_f32_16x16x32_bf16 v[168:171], a[12:15], a[52:55], v[168:171]\n"
  "v_mfma_f32_16x16x32_bf16 v[172:175], a[12:15], a[56:59], v[172:175]\n"
  "s_waitcnt vmcnt(38)\n"
  "s_barrier\n"
  "s_waitcnt lgkmcnt(4)\n"
  "v_mfma_f32_16x16x32_bf16 v[176:179], a[16:19], a[44:47], v[176:179]\n"
  "ds_read_b64_tr_b16 a[0:1], v234 offset:0\n"
  "v_mfma_f32_16x16x32_bf16 v[180:183], a[16:19], a[48:51], v[180:183]\n"
  "ds_read_b64_tr_b16 a[2:3], v234 offset:3584\n"
  "v_mfma_f32_16x16x32_bf16 v[184:187], a[16:19], a[52:55], v[184:187]\n"
  "s_mov_b32 s20, 5505024\n"
  "s_add_u32 m0, s23, 57344\n"
  "s_nop 0\n"
  "buffer_load_dwordx4 v224, s[8:11], s20 offen offset:0 lds\n"
  "v_mfma_f32_16x16x32_bf16 v[188:191], a[16:19], a[56:59], v[188:191]\n"
  "s_waitcnt lgkmcnt(4)\n"
  "v_mfma_f32_16x16x32_bf16 v[192:195], a[20:23], a[44:47], v[192:195]\n"
  "ds_read_b64_tr_b16 a[4:5], v234 offset:7168\n"
  "v_mfma_f32_16x16x32_bf16 v[196:199], a[20:23], a[48:51], v[196:199]\n"
  "ds_read_b64_tr_b16 a[6:7], v234 offset:10752\n"
  "v_mfma_f32_16x16x32_bf16 v[200:203], a[20:23], a[52:55], v[200:203]\n"
  "buffer_load_dwordx4 v225, s[8:11], s20 offen offset:1024 lds\n"
  "v_mfma_f32_16x16x32_bf16 v[204:207], a[20:23], a[56:59], v[204:207]\n"
  "s_waitcnt lgkmcnt(4)\n"
  "v_mfma_f32_16x16x32_bf16 v[208:211], a[24:27], a[44:47], v[208:211]\n"
  "ds_read_b64_tr_b16 a[8:9], v234 offset:32\n"
  "v_mfma_f32_16x16x32_bf16 v[212:215], a[24:27], a[48:51], v[212:215]\n"
  "ds_read_b64_tr_b16 a[10:11], v234 offset:3616\n"
  "v_mfma_f32_16x16x32_bf16 v[216:219], a[24:27], a[52:55], v[216:219]\n"
  "v_mfma_f32_16x16x32_bf16 v[220:223], a[24:27], a[56:59], v[220:223]\n"
  "s_waitcnt lgkmcnt(4)\n"
  "v_mfma_f32_16x16x32_bf16 v[0:3], a[0:3], a[60:63], v[0:3]\n"
  "ds_read_b64_tr_b16 a[12:13], v234 offset:7200\n"
  "v_mfma_f32_16x16x32_bf16 v[4:7], a[0:3], a[64:67], v[4:7]\n"
  "ds_read_b64_tr_b16 a[14:15], v234 offset:10784\n"
  "v_mfma_f32_16x16x32_bf16 v[8:11], a[0:3], a[68:71], v[8:11]\n"
  "buffer_load_dwordx4 v226, s[8:11], s20 offen offset:2048 lds\n"
  "v_mfma_f32_16x16x32_bf16 v[12:15], a[0:3], a[72:75], v[12:15]\n"
  "s_waitcnt lgkmcnt(4)\n"
  "v_mfma_f32_16x16x32_bf16 v[16:19], a[4:7], a[60:63], v[16:19]\n"
  "ds_read_b64_tr_b16 a[16:17], v234 offset:64\n"
  "v_mfma_f32_16x16x32_bf16 v[20:23], a[4:7], a[64:67], v[20:23]\n"
  "ds_read_b64_tr_b16 a[18:19], v234 offset:3648\n"
  "v_mfma_f32_16x16x32_bf16 v[24:27], a[4:7], a[68:71], v[24:27]\n"
  "v_mfma_f32_16x16x32_bf16 v[28:31], a[4:7], a[72:75], v[28:31]\n"
  "s_waitcnt lgkmcnt(4)\n"
  "v_mfma_f32_16x16x32_bf16 v[32:35], a[8:11], a[60:63], v[32:35]\n"
  "ds_read_b64_tr_b16 a[20:21], v234 offset:7232\n"
  "v_mfma_f32_16x16x32_bf16 v[36:39], a[8:11], a[64:67], v[36:39]\n"
  "ds_read_b64_tr_b16 a[22:23], v234 offset:10816\n"
  "v_mfma_f32_16x16x32_bf16 v[40:43], a[8:11], a[68:71], v[40:43]\n"
  "buffer_load_dwordx4 v227, s[8:11], s20 offen offset:3072 lds\n"
  "v_mfma_f32_16x16x32_bf16 v[44:47], a[8:11], a[72:75], v[44:47]\n"
  "s_waitcnt lgkmcnt(4)\n"
  "v_mfma_f32_16x16x32_bf16 v[48:51], a[12:15], a[60:63], v[48:51]\n"
  "ds_read_b64_tr_b16 a[24:25], v234 offset:96\n"
  "v_mfma_f32_16x16x32_bf16 v[52:55], a[12:15], a[64:67], v[52:55]\n"
  "ds_read_b64_tr_b16 a[26:27], v234 offset:3680\n"
  "v_mfma_f32_16x16x32_bf16 v[56:59], a[12:15], a[68:71], v[56:59]\n"
  "v_mfma_f32_16x16x32_bf16 v[60:63], a[12:15], a[72:75], v[60:63]\n"
  "s_waitcnt lgkmcnt(4)\n"
  "v_mfma_f32_16x16x32_bf16 v[64:67], a[16:19], a[60:63], v[64:67]\n"
  "ds_read_b64_tr_b16 a[0:1], v234 offset:7264\n"
  "v_mfma_f32_16x16x32_bf16 v[68:71], a[16:19], a[64:67], v[68:71]\n"
  "ds_read_b64_tr_b16 a[2:3], v234 offset:10848\n"
  "v_mfma_f32_16x16x32_bf16 v[72:75], a[16:19], a[68:71], v[72:75]\n"
  "s_add_u32 m0, s23, 61440\n"
  "s_nop 0\n"
  "buffer_load_dwordx4 v228, s[8:11], s20 offen offset:0 lds\n"
  "v_mfma_f32_16x16x32_bf16 v[76:79], a[16:19], a[72:75], v[76:79]\n"
  "s_waitcnt lgkmcnt(4)\n"
  "v_mfma_f32_16x16x32_bf16 v[80:83], a[20:23], a[60:63], v[80:83]\n"
  "ds_read_b64_tr_b16 a[4:5], v234 offset:128\n"
  "v_mfma_f32_16x16x32_bf16 v[84:87], a[20:23], a[64:67], v[84:87]\n"
  "ds_read_b64_tr_b16 a[6:7], v234 offset:3712\n"
  "v_mfma_f32_16x16x32_bf16 v[88:91], a[20:23], a[68:71], v[88:91]\n"
  "buffer_load_dwordx4 v229, s[8:11], s20 offen offset:1024 lds\n"
  "v_mfma_f32_16x16x32_bf16 v[92:95], a[20:23], a[72:75], v[92:95]\n"
  "s_waitcnt lgkmcnt(4)\n"
  "v_mfma_f32_16x16x32_bf16 v[96:99], a[24:27], a[60:63], v[96:99]\n"
  "ds_read_b64_tr_b16 a[8:9], v234 offset:7296\n"
  "v_mfma_f32_16x16x32_bf16 v[100:103], a[24:27], a[64:67], v[100:103]\n"
  "ds_read_b64_tr_b16 a[10:11], v234 offset:10880\n"
  "v_mfma_f32_16x16x32_bf16 v[104:107], a[24:27], a[68:71], v[104:107]\n"
  "v_mfma_f32_16x16x32_bf16 v[108:111], a[24:27], a[72:75], v[108:111]\n"
  "s_waitcnt lgkmcnt(4)\n"
  "v_mfma_f32_16x16x32_bf16 v[112:115], a[0:3], a[60:63], v[112:115]\n"
  "ds_read_b64_tr_b16 a[12:13], v234 offset:160\n"
  "v_mfma_f32_16x16x32_bf16 v[116:119], a[0:3], a[64:67], v[116:119]\n"
  "ds_read_b64_tr_b16 a[14:15], v234 offset:3744\n"
  "v_mfma_f32_16x16x32_bf16 v[120:123], a[0:3], a[68:71], v[120:123]\n"
  "buffer_load_dwordx4 v230, s[8:11], s20 offen offset:2048 lds\n"
  "v_mfma_f32_16x16x32_bf16 v[124:127], a[0:3], a[72:75], v[124:127]\n"
  "s_waitcnt lgkmcnt(4)\n"
  "v_mfma_f32_16x16x32_bf16 v[128:131], a[4:7], a[60:63], v[128:131]\n"
  "ds_read_b64_tr_b16 a[16:17], v234 offset:7328\n"
  "v_mfma_f32_16x16x32_bf16 v[132:135], a[4:7], a[64:67], v[132:135]\n"
  "ds_read_b64_tr_b16 a[18:19], v234 offset:10912\n"
  "v_mfma_f32_16x16x32_bf16 v[136:139], a[4:7], a[68:71], v[136:139]\n"
  "v_mfma_f32_16x16x32_bf16 v[140:143], a[4:7], a[72:75], v[140:143]\n"
  "s_waitcnt lgkmcnt(4)\n"
  "v_mfma_f32_16x16x32_bf16 v[144:147], a[8:11], a[60:63], v[144:147]\n"
  "ds_read_b64_tr_b16 a[20:21], v234 offset:192\n"
  "v_mfma_f32_16x16x32_bf16 v[148:151], a[8:11], a[64:67], v[148:151]\n"
  "ds_read_b64_tr_b16 a[22:23], v234 offset:3776\n"
  "v_mfma_f32_16x16x32_bf16 v[152:155], a[8:11], a[68:71], v[152:155]\n"
  "s_mov_b32 s21, 102400\n"
  "buffer_load_dwordx4 a[44:47], v231, s[12:15], s21 offen offset:0 sc1\n"
  "v_mfma_f32_16x16x32_bf16 v[156:159], a[8:11], a[72:75], v[156:159]\n"
  "s_waitcnt lgkmcnt(4)\n"
  "v_mfma_f32_16x16x32_bf16 v[160:163], a[12:15], a[60:63], v[160:163]\n"
  "ds_read_b64_tr_b16 a[24:25], v234 offset:7360\n"
  "v_mfma_f32_16x16x32_bf16 v[164:167], a[12:15], a[64:67], v[164:167]\n"
  "ds_read_b64_tr_b16 a[26:27], v234 offset:10944\n"
  "v_mfma_f32_16x16x32_bf16 v[168:171], a[12:15], a[68:71], v[168:171]\n"
  "v_mfma_f32_16x16x32_bf16 v[172:175], a[12:15], a[72:75], v[172:175]\n"
  "s_waitcnt lgkmcnt(4)\n"
  "v_mfma_f32_16x16x32_bf16 v[176:179], a[16:19], a[60:63], v[176:179]\n"
  "ds_read_b64_tr_b16 a[0:1], v234 offset:14336\n"
  "v_mfma_f32_16x16x32_bf16 v[180:183], a[16:19], a[64:67], v[180:183]\n"
  "ds_read_b64_tr_b16 a[2:3], v234 offset:17920\n"
  "v_mfma_f32_16x16x32_bf16 v[184:187], a[16:19], a[68:71], v[184:187]\n"
  "buffer_load_dwordx4 a[48:51], v231, s[12:15], s21 offen offset:1024 sc1\n"
  "v_mfma_f32_16x16x32_bf16 v[188:191], a[16:19], a[72:75], v[188:191]\n"
  "s_waitcnt lgkmcnt(4)\n"
  "v_mfma_f32_16x16x32_bf16 v[192:195], a[20:23], a[60:63], v[192:195]\n"
  "ds_read_b64_tr_b16 a[4:5], v234 offset:21504\n"
  "v_mfma_f32_16x16x32_bf16 v[196:199], a[20:23], a[64:67], v[196:199]\n"
  "ds_read_b64_tr_b16 a[6:7], v234 offset:25088\n"
  "v_mfma_f32_16x16x32_bf16 v[200:203], a[20:23], a[68:71], v[200:203]\n"
  "buffer_load_dwordx4 a[52:55], v231, s[12:15], s21 offen offset:2048 sc1\n"
  "v_mfma_f32_16x16x32_bf16 v[204:207], a[20:23], a[72:75], v[204:207]\n"
  "s_waitcnt lgkmcnt(4)\n"
  "v_mfma_f32_16x16x32_bf16 v[208:211], a[24:27], a[60:63], v[208:211]\n"
  "ds_read_b64_tr_b16 a[8:9], v234 offset:14368\n"
  "v_mfma_f32_16x16x32_bf16 v[212:215], a[24:27], a[64:67], v[212:215]\n"
  "ds_read_b64_tr_b16 a[10:11], v234 offset:17952\n"
  "v_mfma_f32_16x16x32_bf16 v[216:219], a[24:27], a[68:71], v[216:219]\n"
  "v_mfma_f32_16x16x32_bf16 v[220:223], a[24:27], a[72:75], v[220:223]\n"
  "s_waitcnt vmcnt(44) lgkmcnt(4)\n"
  "v_mfma_f32_16x16x32_bf16 v[0:3], a[0:3], a[76:79], v[0:3]\n"
  "ds_read_b64_tr_b16 a[12:13], v234 offset:21536\n"
  "v_mfma_f32_16x16x32_bf16 v[4:7], a[0:3], a[80:83], v[4:7]\n"
  "ds_read_b64_tr_b16 a[14:15], v234 offset:25120\n"
  "v_mfma_f32_16x16x32_bf16 v[8:11], a[0:3], a[84:87], v[8:11]\n"
  "buffer_load_dwordx4 a[56:59], v231, s[12:15], s21 offen offset:3072 sc1\n"
  "v_mfma_f32_16x16x32_bf16 v[12:15], a[0:3], a[88:91], v[12:15]\n"
  "s_waitcnt lgkmcnt(4)\n"
  "v_mfma_f32_16x16x32_bf16 v[16:19], a[4:7], a[76:79], v[16:19]\n"
  "ds_read_b64_tr_b16 a[16:17], v234 offset:14400\n"
  "v_mfma_f32_16x16x32_bf16 v[20:23], a[4:7], a[80:83], v[20:23]\n"
  "ds_read_b64_tr_b16 a[18:19], v234 offset:17984\n"
  "v_mfma_f32_16x16x32_bf16 v[24:27], a[4:7], a[84:87], v[24:27]\n"
  "v_mfma_f32_16x16x32_bf16 v[28:31], a[4:7], a[88:91], v[28:31]\n"
  "s_waitcnt lgkmcnt(4)\n"
  "v_mfma_f32_16x16x32_bf16 v[32:35], a[8:11], a[76:79], v[32:35]\n"
  "ds_read_b64_tr_b16 a[20:21], v234 offset:21568\n"
  "v_mfma_f32_16x16x32_bf16 v[36:39], a[8:11], a[80:83], v[36:39]\n"
  "ds_read_b64_tr_b16 a[22:23], v234 offset:25152\n"
  "v_mfma_f32_16x16x32_bf16 v[40:43], a[8:11], a[84:87], v[40:43]\n"
  "s_mov_b32 s21, 106496\n"
  "buffer_load_dwordx4 a[60:63], v231, s[12:15], s21 offen offset:0 sc1\n"
  "v_mfma_f32_16x16x32_bf16 v[44:47], a[8:11], a[88:91], v[44:47]\n"
  "s_waitcnt lgkmcnt(4)\n"
  "v_mfma_f32_16x16x32_bf16 v[48:51], a[12:15], a[76:79], v[48:51]\n"
  "ds_read_b64_tr_b16 a[24:25], v234 offset:14432\n"
  "v_mfma_f32_16x16x32_bf16 v[52:55], a[12:15], a[80:83], v[52:55]\n"
  "ds_read_b64_tr_b16 a[26:27], v234 offset:18016\n"
  "v_mfma_f32_16x16x32_bf16 v[56:59], a[12:15], a[84:87], v[56:59]\n"
  "buffer_load_dwordx4 a[64:67], v231, s[12:15], s21 offen offset:1024 sc1\n"
  "v_mfma_f32_16x16x32_bf16 v[60:63], a[12:15], a[88:91], v[60:63]\n"
  "s_waitcnt lgkmcnt(4)\n"
  "v_mfma_f32_16x16x32_bf16 v[64:67], a[16:19], a[76:79], v[64:67]\n"
  "ds_read_b64_tr_b16 a[0:1], v234 offset:21600\n"
  "v_mfma_f32_16x16x32_bf16 v[68:71], a[16:19], a[80:83], v[68:71]\n"
  "ds_read_b64_tr_b16 a[2:3], v234 offset:25184\n"
  "v_mfma_f32_16x16x32_bf16 v[72:75], a[16:19], a[84:87], v[72:75]\n"
  "v_mfma_f32_16x16x32_bf16 v[76:79], a[16:19], a[88:91], v[76:79]\n"
  "s_waitcnt lgkmcnt(4)\n"
  "v_mfma_f32_16x16x32_bf16 v[80:83], a[20:23], a[76:79], v[80:83]\n"
  "ds_read_b64_tr_b16 a[4:5], v234 offset:14464\n"
  "v_mfma_f32_16x16x32_bf16 v[84:87], a[20:23], a[80:83], v[84:87]\n"
  "ds_read_b64_tr_b16 a[6:7], v234 offset:18048\n"
  "v_mfma_f32_16x16x32_bf16 v[88:91], a[20:23], a[84:87], v[88:91]\n"
  "buffer_load_dwordx4 a[68:71], v231, s[12:15], s21 offen offset:2048 sc1\n"
  "v_mfma_f32_16x16x32_bf16 v[92:95], a[20:23], a[88:91], v[92:95]\n"
  "s_waitcnt lgkmcnt(4)\n"
  "v_mfma_f32_16x16x32_bf16 v[96:99], a[24:27], a[76:79], v[96:99]\n"
  "ds_read_b64_tr_b16 a[8:9], v234 offset:21632\n"
  "v_mfma_f32_16x16x32_bf16 v[100:103], a[24:27], a[80:83], v[100:103]\n"
  "ds_read_b64_tr_b16 a[10:11], v234 offset:25216\n"
  "v_mfma_f32_16x16x32_bf16 v[104:107], a[24:27], a[84:87], v[104:107]\n"
  "v_mfma_f32_16x16x32_bf16 v[108:111], a[24:27], a[88:91], v[108:111]\n"
  "s_waitcnt lgkmcnt(4)\n"
  "v_mfma_f32_16x16x32_bf16 v[112:115], a[0:3], a[76:79], v[112:115]\n"
  "ds_read_b64_tr_b16 a[12:13], v234 offset:14496\n"
  "v_mfma_f32_16x16x32_bf16 v[116:119], a[0:3], a[80:83], v[116:119]\n"
  "ds_read_b64_tr_b16 a[14:15], v234 offset:18080\n"
  "v_mfma_f32_16x16x32_bf16 v[120:123], a[0:3], a[84:87], v[120:123]\n"
  "buffer_load_dwordx4 a[72:75], v231, s[12:15], s21 offen offset:3072 sc1\n"
  "v_mfma_f32_16x16x32_bf16 v[124:127], a[0:3], a[88:91], v[124:127]\n"
  "s_waitcnt lgkmcnt(4)\n"
  "v_mfma_f32_16x16x32_bf16 v[128:131], a[4:7], a[76:79], v[128:131]\n"
  "ds_read_b64_tr_b16 a[16:17], v234 offset:21664\n"
  "v_mfma_f32_16x16x32_bf16 v[132:135], a[4:7], a[80:83], v[132:135]\n"
  "ds_read_b64_tr_b16 a[18:19], v234 offset:25248\n"
  "v_mfma_f32_16x16x32_bf16 v[136:139], a[4:7], a[84:87], v[136:139]\n"
  "v_mfma_f32_16x16x32_bf16 v[140:143], a[4:7], a[88:91], v[140:143]\n"
  "s_waitcnt lgkmcnt(4)\n"
  "v_mfma_f32_16x16x32_bf16 v[144:147], a[8:11], a[76:79], v[144:147]\n"
  "ds_read_b64_tr_b16 a[20:21], v234 offset:14528\n"
  "v_mfma_f32_16x16x32_bf16 v[148:151], a[8:11], a[80:83], v[148:151]\n"
  "ds_read_b64_tr_b16 a[22:23], v234 offset:18112\n"
  "v_mfma_f32_16x16x32_bf16 v[152:155], a[8:11], a[84:87], v[152:155]\n"
  "v_mfma_f32_16x16x32_bf16 v[156:159], a[8:11], a[88:91], v[156:159]\n"
  "s_waitcnt lgkmcnt(4)\n"
  "v_mfma_f32_16x16x32_bf16 v[160:163], a[12:15], a[76:79], v[160:163]\n"
  "ds_read_b64_tr_b16 a[24:25], v234 offset:21696\n"
  "v_mfma_f32_16x16x32_bf16 v[164:167], a[12:15], a[80:83], v[164:167]\n"
  "ds_read_b64_tr_b16 a[26:27], v234 offset:25280\n"
  "v_mfma_f32_16x16x32_bf16 v[168:171], a[12:15], a[84:87], v[168:171]\n"
  "v_mfma_f32_16x16x32_bf16 v[172:175], a[12:15], a[88:91], v[172:175]\n"
  "s_waitcnt vmcnt(38)\n"
  "s_barrier\n"
  "s_waitcnt lgkmcnt(4)\n"
  "v_mfma_f32_16x16x32_bf16 v[176:179], a[16:19], a[76:79], v[176:179]\n"
  "ds_read_b64_tr_b16 a[0:1], v232 offset:0\n"
  "v_mfma_f32_16x16x32_bf16 v[180:183], a[16:19], a[80:83], v[180:183]\n"
  "ds_read_b64_tr_b16 a[2:3], v232 offset:3584\n"
  "v_mfma_f32_16x16x32_bf16 v[184:187], a[16:19], a[84:87], v[184:187]\n"
  "s_mov_b32 s20, 5963776\n"
  "s_add_u32 m0, s23, 86016\n"
  "s_nop 0\n"
  "buffer_load_dwordx4 v224, s[8:11], s20 offen offset:0 lds\n"
  "v_mfma_f32_16x16x32_bf16 v[188:191], a[16:19], a[88:91], v[188:191]\n"
  "s_waitcnt lgkmcnt(4)\n"
  "v_mfma_f32_16x16x32_bf16 v[192:195], a[20:23], a[76:79], v[192:195]\n"
  "ds_read_b64_tr_b16 a[4:5], v232 offset:7168\n"
  "v_mfma_f32_16x16x32_bf16 v[196:199], a[20:23], a[80:83], v[196:199]\n"
  "ds_read_b64_tr_b16 a[6:7], v232 offset:10752\n"
  "v_mfma_f32_16x16x32_bf16 v[200:203], a[20:23], a[84:87], v[200:203]\n"
  "buffer_load_dwordx4 v225, s[8:11], s20 offen offset:1024 lds\n"
  "v_mfma_f32_16x16x32_bf16 v[204:207], a[20:23], a[88:91], v[204:207]\n"
  "s_waitcnt lgkmcnt(4)\n"
  "v_mfma_f32_16x16x32_bf16 v[208:211], a[24:27], a[76:79], v[208:211]\n"
  "ds_read_b64_tr_b16 a[8:9], v232 offset:32\n"
  "v_mfma_f32_16x16x32_bf16 v[212:215], a[24:27], a[80:83], v[212:215]\n"
  "ds_read_b64_tr_b16 a[10:11], v232 offset:3616\n"
  "v_mfma_f32_16x16x32_bf16 v[216:219], a[24:27], a[84:87], v[216:219]\n"
  "v_mfma_f32_16x16x32_bf16 v[220:223], a[24:27], a[88:91], v[220:223]\n"
  "s_waitcnt lgkmcnt(4)\n"
  "v_mfma_f32_16x16x32_bf16 v[0:3], a[0:3], a[92:95], v[0:3]\n"
  "ds_read_b64_tr_b16 a[12:13], v232 offset:7200\n"
  "v_mfma_f32_16x16x32_bf16 v[4:7], a[0:3], a[96:99], v[4:7]\n"
  "ds_read_b64_tr_b16 a[14:15], v232 offset:10784\n"
  "v_mfma_f32_16x16x32_bf16 v[8:11], a[0:3], a[100:103], v[8:11]\n"
  "buffer_load_dwordx4 v226, s[8:11], s20 offen offset:2048 lds\n"
  "v_mfma_f32_16x16x32_bf16 v[12:15], a[0:3], a[104:107], v[12:15]\n"
  "s_waitcnt lgkmcnt(4)\n"
  "v_mfma_f32_16x16x32_bf16 v[16:19], a[4:7], a[92:95], v[16:19]\n"
  "ds_read_b64_tr_b16 a[16:17], v232 offset:64\n"
  "v_mfma_f32_16x16x32_bf16 v[20:23], a[4:7], a[96:99], v[20:23]\n"
  "ds_read_b64_tr_b16 a[18:19], v232 offset:3648\n"
  "v_mfma_f32_16x16x32_bf16 v[24:27], a[4:7], a[100:103], v[24:27]\n"
  "v_mfma_f32_16x16x32_bf16 v[28:31], a[4:7], a[104:107], v[28:31]\n"
  "s_waitcnt lgkmcnt(4)\n"
  "v_mfma_f32_16x16x32_bf16 v[32:35], a[8:11], a[92:95], v[32:35]\n"
  "ds_read_b64_tr_b16 a[20:21], v232 offset:7232\n"
  "v_mfma_f32_16x16x32_bf16 v[36:39], a[8:11], a[96:99], v[36:39]\n"
  "ds_read_b64_tr_b16 a[22:23], v232 offset:10816\n"
  "v_mfma_f32_16x16x32_bf16 v[40:43], a[8:11], a[100:103], v[40:43]\n"
  "buffer_load_dwordx4 v227, s[8:11], s20 offen offset:3072 lds\n"
  "v_mfma_f32_16x16x32_bf16 v[44:47], a[8:11], a[104:107], v[44:47]\n"
  "s_waitcnt lgkmcnt(4)\n"
  "v_mfma_f32_16x16x32_bf16 v[48:51], a[12:15], a[92:95], v[48:51]\n"
  "ds_read_b64_tr_b16 a[24:25], v232 offset:96\n"
  "v_mfma_f32_16x16x32_bf16 v[52:55], a[12:15], a[96:99], v[52:55]\n"
  "ds_read_b64_tr_b16 a[26:27], v232 offset:3680\n"
  "v_mfma_f32_16x16x32_bf16 v[56:59], a[12:15], a[100:103], v[56:59]\n"
  "v_mfma_f32_16x16x32_bf16 v[60:63], a[12:15], a[104:107], v[60:63]\n"
  "s_waitcnt lgkmcnt(4)\n"
  "v_mfma_f32_16x16x32_bf16 v[64:67], a[16:19], a[92:95], v[64:67]\n"
  "ds_read_b64_tr_b16 a[0:1], v232 offset:7264\n"
  "v_mfma_f32_16x16x32_bf16 v[68:71], a[16:19], a[96:99], v[68:71]\n"
  "ds_read_b64_tr_b16 a[2:3], v232 offset:10848\n"
  "v_mfma_f32_16x16x32_bf16 v[72:75], a[16:19], a[100:103], v[72:75]\n"
  "s_add_u32 m0, s23, 90112\n"
  "s_nop 0\n"
  "buffer_load_dwordx4 v228, s[8:11], s20 offen offset:0 lds\n"
  "v_mfma_f32_16x16x32_bf16 v[76:79], a[16:19], a[104:107], v[76:79]\n"
  "s_waitcnt lgkmcnt(4)\n"
  "v_mfma_f32_16x16x32_bf16 v[80:83], a[20:23], a[92:95], v[80:83]\n"
  "ds_read_b64_tr_b16 a[4:5], v232 offset:128\n"
  "v_mfma_f32_16x16x32_bf16 v[84:87], a[20:23], a[96:99], v[84:87]\n"
  "ds_read_b64_tr_b16 a[6:7], v232 offset:3712\n"
  "v_mfma_f32_16x16x32_bf16 v[88:91], a[20:23], a[100:103], v[88:91]\n"
  "buffer_load_dwordx4 v229, s[8:11], s20 offen offset:1024 lds\n"
  "v_mfma_f32_16x16x32_bf16 v[92:95], a[20:23], a[104:107], v[92:95]\n"
  "s_waitcnt lgkmcnt(4)\n"
  "v_mfma_f32_16x16x32_bf16 v[96:99], a[24:27], a[92:95], v[96:99]\n"
  "ds_read_b64_tr_b16 a[8:9], v232 offset:7296\n"
  "v_mfma_f32_16x16x32_bf16 v[100:103], a[24:27], a[96:99], v[100:103]\n"
  "ds_read_b64_tr_b16 a[10:11], v232 offset:10880\n"
  "v_mfma_f32_16x16x32_bf16 v[104:107], a[24:27], a[100:103], v[104:107]\n"
  "v_mfma_f32_16x16x32_bf16 v[108:111], a[24:27], a[104:107], v[108:111]\n"
  "s_waitcnt lgkmcnt(4)\n"
  "v_mfma_f32_16x16x32_bf16 v[112:115], a[0:3], a[92:95], v[112:115]\n"
  "ds_read_b64_tr_b16 a[12:13], v232 offset:160\n"
  "v_mfma_f32_16x16x32_bf16 v[116:119], a[0:3], a[96:99], v[116:119]\n"
  "ds_read_b64_tr_b16 a[14:15], v232 offset:3744\n"
  "v_mfma_f32_16x16x32_bf16 v[120:123], a[0:3], a[100:103], v[120:123]\n"
  "buffer_load_dwordx4 v230, s[8:11], s20 offen offset:2048 lds\n"
  "v_mfma_f32_16x16x32_bf16 v[124:127], a[0:3], a[104:107], v[124:127]\n"
  "s_waitcnt lgkmcnt(4)\n"
  "v_mfma_f32_16x16x32_bf16 v[128:131], a[4:7], a[92:95], v[128:131]\n"
  "ds_read_b64_tr_b16 a[16:17], v232 offset:7328\n"
  "v_mfma_f32_16x16x32_bf16 v[132:135], a[4:7], a[96:99], v[132:135]\n"
  "ds_read_b64_tr_b16 a[18:19], v232 offset:10912\n"
  "v_mfma_f32_16x16x32_bf16 v[136:139], a[4:7], a[100:103], v[136:139]\n"
  "v_mfma_f32_16x16x32_bf16 v[140:143], a[4:7], a[104:107], v[140:143]\n"
  "s_waitcnt lgkmcnt(4)\n"
  "v_mfma_f32_16x16x32_bf16 v[144:147], a[8:11], a[92:95], v[144:147]\n"
  "ds_read_b64_tr_b16 a[20:21], v232 offset:192\n"
  "v_mfma_f32_16x16x32_bf16 v[148:151], a[8:11], a[96:99], v[148:151]\n"
  "ds_read_b64_tr_b16 a[22:23], v232 offset:3776\n"
  "v_mfma_f32_16x16x32_bf16 v[152:155], a[8:11], a[100:103], v[152:155]\n"
  "s_mov_b32 s21, 110592\n"
  "buffer_load_dwordx4 a[76:79], v231, s[12:15], s21 offen offset:0 sc1\n"
  "v_mfma_f32_16x16x32_bf16 v[156:159], a[8:11], a[104:107], v[156:159]\n"
  "s_waitcnt lgkmcnt(4)\n"
  "v_mfma_f32_16x16x32_bf16 v[160:163], a[12:15], a[92:95], v[160:163]\n"
  "ds_read_b64_tr_b16 a[24:25], v232 offset:7360\n"
  "v_mfma_f32_16x16x32_bf16 v[164:167], a[12:15], a[96:99], v[164:167]\n"
  "ds_read_b64_tr_b16 a[26:27], v232 offset:10944\n"
  "v_mfma_f32_16x16x32_bf16 v[168:171], a[12:15], a[100:103], v[168:171]\n"
  "v_mfma_f32_16x16x32_bf16 v[172:175], a[12:15], a[104:107], v[172:175]\n"
  "s_waitcnt lgkmcnt(4)\n"
  "v_mfma_f32_16x16x32_bf16 v[176:179], a[16:19], a[92:95], v[176:179]\n"
  "ds_read_b64_tr_b16 a[0:1], v232 offset:14336\n"
  "v_mfma_f32_16x16x32_bf16 v[180:183], a[16:19], a[96:99], v[180:183]\n"
  "ds_read_b64_tr_b16 a[2:3], v232 offset:17920\n"
  "v_mfma_f32_16x16x32_bf16 v[184:187], a[16:19], a[100:103], v[184:187]\n"
  "buffer_load_dwordx4 a[80:83], v231, s[12:15], s21 offen offset:1024 sc1\n"
  "v_mfma_f32_16x16x32_bf16 v[188:191], a[16:19], a[104:107], v[188:191]\n"
  "s_waitcnt lgkmcnt(4)\n"
  "v_mfma_f32_16x16x32_bf16 v[192:195], a[20:23], a[92:95], v[192:195]\n"
  "ds_read_b64_tr_b16 a[4:5], v232 offset:21504\n"
  "v_mfma_f32_16x16x32_bf16 v[196:199], a[20:23], a[96:99], v[196:199]\n"
  "ds_read_b64_tr_b16 a[6:7], v232 offset:25088\n"
  "v_mfma_f32_16x16x32_bf16 v[200:203], a[20:23], a[100:103], v[200:203]\n"
  "buffer_load_dwordx4 a[84:87], v231, s[12:15], s21 offen offset:2048 sc1\n"
  "v_mfma_f32_16x16x32_bf16 v[204:207], a[20:23], a[104:107], v[204:207]\n"
  "s_waitcnt lgkmcnt(4)\n"
  "v_mfma_f32_16x16x32_bf16 v[208:211], a[24:27], a[92:95], v[208:211]\n"
  "ds_read_b64_tr_b16 a[8:9], v232 offset:14368\n"
  "v_mfma_f32_16x16x32_bf16 v[212:215], a[24:27], a[96:99], v[212:215]\n"
  "ds_read_b64_tr_b16 a[10:11], v232 offset:17952\n"
  "v_mfma_f32_16x16x32_bf16 v[216:219], a[24:27], a[100:103], v[216:219]\n"
  "v_mfma_f32_16x16x32_bf16 v[220:223], a[24:27], a[104:107], v[220:223]\n"
  "s_waitcnt vmcnt(44) lgkmcnt(4)\n"
  "v_mfma_f32_16x16x32_bf16 v[0:3], a[0:3], a[108:111], v[0:3]\n"
  "ds_read_b64_tr_b16 a[12:13], v232 offset:21536\n"
  "v_mfma_f32_16x16x32_bf16 v[4:7], a[0:3], a[112:115], v[4:7]\n"
  "ds_read_b64_tr_b16 a[14:15], v232 offset:25120\n"
  "v_mfma_f32_16x16x32_bf16 v[8:11], a[0:3], a[116:119], v[8:11]\n"
  "buffer_load_dwordx4 a[156:159], v231, s[64:67], s68 offen\n"
  "v_mfma_f32_16x16x32_bf16 v[12:15], a[0:3], a[120:123], v[12:15]\n"
  "s_waitcnt lgkmcnt(4)\n"
  "v_mfma_f32_16x16x32_bf16 v[16:19], a[4:7], a[108:111], v[16:19]\n"
  "ds_read_b64_tr_b16 a[16:17], v232 offset:14400\n"
  "v_mfma_f32_16x16x32_bf16 v[20:23], a[4:7], a[112:115], v[20:23]\n"
  "ds_read_b64_tr_b16 a[18:19], v232 offset:17984\n"
  "v_mfma_f32_16x16x32_bf16 v[24:27], a[4:7], a[116:119], v[24:27]\n"
  "buffer_load_dwordx4 a[88:91], v231, s[12:15], s21 offen offset:3072 sc1\n"
  "v_mfma_f32_16x16x32_bf16 v[28:31], a[4:7], a[120:123], v[28:31]\n"
  "s_waitcnt lgkmcnt(4)\n"
  "v_mfma_f32_16x16x32_bf16 v[32:35], a[8:11], a[108:111], v[32:35]\n"
  "ds_read_b64_tr_b16 a[20:21], v232 offset:21568\n"
  "v_mfma_f32_16x16x32_bf16 v[36:39], a[8:11], a[112:115], v[36:39]\n"
  "ds_read_b64_tr_b16 a[22:23], v232 offset:25152\n"
  "v_mfma_f32_16x16x32_bf16 v[40:43], a[8:11], a[116:119], v[40:43]\n"
  "v_mfma_f32_16x16x32_bf16 v[44:47], a[8:11], a[120:123], v[44:47]\n"
  "s_waitcnt lgkmcnt(4)\n"
  "v_mfma_f32_16x16x32_bf16 v[48:51], a[12:15], a[108:111], v[48:51]\n"
  "ds_read_b64_tr_b16 a[24:25], v232 offset:14432\n"
  "v_mfma_f32_16x16x32_bf16 v[52:55], a[12:15], a[112:115], v[52:55]\n"
  "ds_read_b64_tr_b16 a[26:27], v232 offset:18016\n"
  "v_mfma_f32_16x16x32_bf16 v[56:59], a[12:15], a[116:119], v[56:59]\n"
  "s_mov_b32 s21, 114688\n"
  "buffer_load_dwordx4 a[92:95], v231, s[12:15], s21 offen offset:0 sc1\n"
  "v_mfma_f32_16x16x32_bf16 v[60:63], a[12:15], a[120:123], v[60:63]\n"
  "s_waitcnt lgkmcnt(4)\n"
  "v_mfma_f32_16x16x32_bf16 v[64:67], a[16:19], a[108:111], v[64:67]\n"
  "ds_read_b64_tr_b16 a[0:1], v232 offset:21600\n"
  "v_mfma_f32_16x16x32_bf16 v[68:71], a[16:19], a[112:115], v[68:71]\n"
  "ds_read_b64_tr_b16 a[2:3], v232 offset:25184\n"
  "v_mfma_f32_16x16x32_bf16 v[72:75], a[16:19], a[116:119], v[72:75]\n"
  "v_mfma_f32_16x16x32_bf16 v[76:79], a[16:19], a[120:123], v[76:79]\n"
  "s_waitcnt lgkmcnt(4)\n"
  "v_mfma_f32_16x16x32_bf16 v[80:83], a[20:23], a[108:111], v[80:83]\n"
  "ds_read_b64_tr_b16 a[4:5], v232 offset:14464\n"
  "v_mfma_f32_16x16x32_bf16 v[84:87], a[20:23], a[112:115], v[84:87]\n"
  "ds_read_b64_tr_b16 a[6:7], v232 offset:18048\n"
  "v_mfma_f32_16x16x32_bf16 v[88:91], a[20:23], a[116:119], v[88:91]\n"
  "buffer_load_dwordx4 a[96:99], v231, s[12:15], s21 offen offset:1024 sc1\n"
  "v_mfma_f32_16x16x32_bf16 v[92:95], a[20:23], a[120:123], v[92:95]\n"
  "s_waitcnt lgkmcnt(4)\n"
  "v_mfma_f32_16x16x32_bf16 v[96:99], a[24:27], a[108:111], v[96:99]\n"
  "ds_read_b64_tr_b16 a[8:9], v232 offset:21632\n"
  "v_mfma_f32_16x16x32_bf16 v[100:103], a[24:27], a[112:115], v[100:103]\n"
  "ds_read_b64_tr_b16 a[10:11], v232 offset:25216\n"
  "v_mfma_f32_16x16x32_bf16 v[104:107], a[24:27], a[116:119], v[104:107]\n"
  "v_mfma_f32_16x16x32_bf16 v[108:111], a[24:27], a[120:123], v[108:111]\n"
  "s_waitcnt lgkmcnt(4)\n"
  "v_mfma_f32_16x16x32_bf16 v[112:115], a[0:3], a[108:111], v[112:115]\n"
  "ds_read_b64_tr_b16 a[12:13], v232 offset:14496\n"
  "v_mfma_f32_16x16x32_bf16 v[116:119], a[0:3], a[112:115], v[116:119]\n"
  "ds_read_b64_tr_b16 a[14:15], v232 offset:18080\n"
  "v_mfma_f32_16x16x32_bf16 v[120:123], a[0:3], a[116:119], v[120:123]\n"
  "buffer_load_dwordx4 a[100:103], v231, s[12:15], s21 offen offset:2048 sc1\n"
  "v_mfma_f32_16x16x32_bf16 v[124:127], a[0:3], a[120:123], v[124:127]\n"
  "s_waitcnt lgkmcnt(4)\n"
  "v_mfma_f32_16x16x32_bf16 v[128:131], a[4:7], a[108:111], v[128:131]\n"
  "ds_read_b64_tr_b16 a[16:17], v232 offset:21664\n"
  "v_mfma_f32_16x16x32_bf16 v[132:135], a[4:7], a[112:115], v[132:135]\n"
  "ds_read_b64_tr_b16 a[18:19], v232 offset:25248\n"
  "v_mfma_f32_16x16x32_bf16 v[136:139], a[4:7], a[116:119], v[136:139]\n"
  "buffer_load_dwordx4 a[104:107], v231, s[12:15], s21 offen offset:3072 sc1\n"
  "v_mfma_f32_16x16x32_bf16 v[140:143], a[4:7], a[120:123], v[140:143]\n"
  "s_waitcnt lgkmcnt(4)\n"
  "v_mfma_f32_16x16x32_bf16 v[144:147], a[8:11], a[108:111], v[144:147]\n"
  "ds_read_b64_tr_b16 a[20:21], v232 offset:14528\n"
  "v_mfma_f32_16x16x32_bf16 v[148:151], a[8:11], a[112:115], v[148:151]\n"
  "ds_read_b64_tr_b16 a[22:23], v232 offset:18112\n"
  "v_mfma_f32_16x16x32_bf16 v[152:155], a[8:11], a[116:119], v[152:155]\n"
  "v_mfma_f32_16x16x32_bf16 v[156:159], a[8:11], a[120:123], v[156:159]\n"
  "s_waitcnt lgkmcnt(4)\n"
  "v_mfma_f32_16x16x32_bf16 v[160:163], a[12:15], a[108:111], v[160:163]\n"
  "ds_read_b64_tr_b16 a[24:25], v232 offset:21696\n"
  "v_mfma_f32_16x16x32_bf16 v[164:167], a[12:15], a[112:115], v[164:167]\n"
  "ds_read_b64_tr_b16 a[26:27], v232 offset:25280\n"
  "v_mfma_f32_16x16x32_bf16 v[168:171], a[12:15], a[116:119], v[168:171]\n"
  "v_mfma_f32_16x16x32_bf16 v[172:175], a[12:15], a[120:123], v[172:175]\n"
  "s_waitcnt vmcnt(39)\n"
  "s_barrier\n"
  "s_waitcnt lgkmcnt(4)\n"
  "v_mfma_f32_16x16x32_bf16 v[176:179], a[16:19], a[108:111], v[176:179]\n"
  "ds_read_b64_tr_b16 a[0:1], v232 offset:28672\n"
  "v_mfma_f32_16x16x32_bf16 v[180:183], a[16:19], a[112:115], v[180:183]\n"
  "ds_read_b64_tr_b16 a[2:3], v232 offset:32256\n"
  "v_mfma_f32_16x16x32_bf16 v[184:187], a[16:19], a[116:119], v[184:187]\n"
  "s_mov_b32 s20, 6422528\n"
  "s_add_u32 m0, s23, 114688\n"
  "s_nop 0\n"
  "buffer_load_dwordx4 v224, s[8:11], s20 offen offset:0 lds\n"
  "v_mfma_f32_16x16x32_bf16 v[188:191], a[16:19], a[120:123], v[188:191]\n"
  "s_waitcnt lgkmcnt(4)\n"
  "v_mfma_f32_16x16x32_bf16 v[192:195], a[20:23], a[108:111], v[192:195]\n"
  "ds_read_b64_tr_b16 a[4:5], v232 offset:35840\n"
  "v_mfma_f32_16x16x32_bf16 v[196:199], a[20:23], a[112:115], v[196:199]\n"
  "ds_read_b64_tr_b16 a[6:7], v232 offset:39424\n"
  "v_mfma_f32_16x16x32_bf16 v[200:203], a[20:23], a[116:119], v[200:203]\n"
  "buffer_load_dwordx4 v225, s[8:11], s20 offen offset:1024 lds\n"
  "v_mfma_f32_16x16x32_bf16 v[204:207], a[20:23], a[120:123], v[204:207]\n"
  "s_waitcnt lgkmcnt(4)\n"
  "v_mfma_f32_16x16x32_bf16 v[208:211], a[24:27], a[108:111], v[208:211]\n"
  "ds_read_b64_tr_b16 a[8:9], v232 offset:28704\n"
  "v_mfma_f32_16x16x32_bf16 v[212:215], a[24:27], a[112:115], v[212:215]\n"
  "ds_read_b64_tr_b16 a[10:11], v232 offset:32288\n"
  "v_mfma_f32_16x16x32_bf16 v[216:219], a[24:27], a[116:119], v[216:219]\n"
  "v_mfma_f32_16x16x32_bf16 v[220:223], a[24:27], a[120:123], v[220:223]\n"
  "s_waitcnt lgkmcnt(4)\n"
  "v_mfma_f32_16x16x32_bf16 v[0:3], a[0:3], a[124:127], v[0:3]\n"
  "ds_read_b64_tr_b16 a[12:13], v232 offset:35872\n"
  "v_mfma_f32_16x16x32_bf16 v[4:7], a[0:3], a[128:131], v[4:7]\n"
  "ds_read_b64_tr_b16 a[14:15], v232 offset:39456\n"
  "v_mfma_f32_16x16x32_bf16 v[8:11], a[0:3], a[132:135], v[8:11]\n"
  "buffer_load_dwordx4 v226, s[8:11], s20 offen offset:2048 lds\n"
  "v_mfma_f32_16x16x32_bf16 v[12:15], a[0:3], a[136:139], v[12:15]\n"
  "s_waitcnt lgkmcnt(4)\n"
  "v_mfma_f32_16x16x32_bf16 v[16:19], a[4:7], a[124:127], v[16:19]\n"
  "ds_read_b64_tr_b16 a[16:17], v232 offset:28736\n"
  "v_mfma_f32_16x16x32_bf16 v[20:23], a[4:7], a[128:131], v[20:23]\n"
  "ds_read_b64_tr_b16 a[18:19], v232 offset:32320\n"
  "v_mfma_f32_16x16x32_bf16 v[24:27], a[4:7], a[132:135], v[24:27]\n"
  "v_mfma_f32_16x16x32_bf16 v[28:31], a[4:7], a[136:139], v[28:31]\n"
  "s_waitcnt lgkmcnt(4)\n"
  "v_mfma_f32_16x16x32_bf16 v[32:35], a[8:11], a[124:127], v[32:35]\n"
  "ds_read_b64_tr_b16 a[20:21], v232 offset:35904\n"
  "v_mfma_f32_16x16x32_bf16 v[36:39], a[8:11], a[128:131], v[36:39]\n"
  "ds_read_b64_tr_b16 a[22:23], v232 offset:39488\n"
  "v_mfma_f32_16x16x32_bf16 v[40:43], a[8:11], a[132:135], v[40:43]\n"
  "buffer_load_dwordx4 v227, s[8:11], s20 offen offset:3072 lds\n"
  "v_mfma_f32_16x16x32_bf16 v[44:47], a[8:11], a[136:139], v[44:47]\n"
  "s_waitcnt lgkmcnt(4)\n"
  "v_mfma_f32_16x16x32_bf16 v[48:51], a[12:15], a[124:127], v[48:51]\n"
  "ds_read_b64_tr_b16 a[24:25], v232 offset:28768\n"
  "v_mfma_f32_16x16x32_bf16 v[52:55], a[12:15], a[128:131], v[52:55]\n"
  "ds_read_b64_tr_b16 a[26:27], v232 offset:32352\n"
  "v_mfma_f32_16x16x32_bf16 v[56:59], a[12:15], a[132:135], v[56:59]\n"
  "v_mfma_f32_16x16x32_bf16 v[60:63], a[12:15], a[136:139], v[60:63]\n"
  "s_waitcnt lgkmcnt(4)\n"
  "v_mfma_f32_16x16x32_bf16 v[64:67], a[16:19], a[124:127], v[64:67]\n"
  "ds_read_b64_tr_b16 a[0:1], v232 offset:35936\n"
  "v_mfma_f32_16x16x32_bf16 v[68:71], a[16:19], a[128:131], v[68:71]\n"
  "ds_read_b64_tr_b16 a[2:3], v232 offset:39520\n"
  "v_mfma_f32_16x16x32_bf16 v[72:75], a[16:19], a[132:135], v[72:75]\n"
  "s_add_u32 m0, s23, 118784\n"
  "s_nop 0\n"
  "buffer_load_dwordx4 v228, s[8:11], s20 offen offset:0 lds\n"
  "v_mfma_f32_16x16x32_bf16 v[76:79], a[16:19], a[136:139], v[76:79]\n"
  "s_waitcnt lgkmcnt(4)\n"
  "v_mfma_f32_16x16x32_bf16 v[80:83], a[20:23], a[124:127], v[80:83]\n"
  "ds_read_b64_tr_b16 a[4:5], v232 offset:28800\n"
  "v_mfma_f32_16x16x32_bf16 v[84:87], a[20:23], a[128:131], v[84:87]\n"
  "ds_read_b64_tr_b16 a[6:7], v232 offset:32384\n"
  "v_mfma_f32_16x16x32_bf16 v[88:91], a[20:23], a[132:135], v[88:91]\n"
  "buffer_load_dwordx4 v229, s[8:11], s20 offen offset:1024 lds\n"
  "v_mfma_f32_16x16x32_bf16 v[92:95], a[20:23], a[136:139], v[92:95]\n"
  "s_waitcnt lgkmcnt(4)\n"
  "v_mfma_f32_16x16x32_bf16 v[96:99], a[24:27], a[124:127], v[96:99]\n"
  "ds_read_b64_tr_b16 a[8:9], v232 offset:35968\n"
  "v_mfma_f32_16x16x32_bf16 v[100:103], a[24:27], a[128:131], v[100:103]\n"
  "ds_read_b64_tr_b16 a[10:11], v232 offset:39552\n"
  "v_mfma_f32_16x16x32_bf16 v[104:107], a[24:27], a[132:135], v[104:107]\n"
  "v_mfma_f32_16x16x32_bf16 v[108:111], a[24:27], a[136:139], v[108:111]\n"
  "s_waitcnt lgkmcnt(4)\n"
  "v_mfma_f32_16x16x32_bf16 v[112:115], a[0:3], a[124:127], v[112:115]\n"
  "ds_read_b64_tr_b16 a[12:13], v232 offset:28832\n"
  "v_mfma_f32_16x16x32_bf16 v[116:119], a[0:3], a[128:131], v[116:119]\n"
  "ds_read_b64_tr_b16 a[14:15], v232 offset:32416\n"
  "v_mfma_f32_16x16x32_bf16 v[120:123], a[0:3], a[132:135], v[120:123]\n"
  "buffer_load_dwordx4 v230, s[8:11], s20 offen offset:2048 lds\n"
  "v_mfma_f32_16x16x32_bf16 v[124:127], a[0:3], a[136:139], v[124:127]\n"
  "s_waitcnt lgkmcnt(4)\n"
  "v_mfma_f32_16x16x32_bf16 v[128:131], a[4:7], a[124:127], v[128:131]\n"
  "ds_read_b64_tr_b16 a[16:17], v232 offset:36000\n"
  "v_mfma_f32_16x16x32_bf16 v[132:135], a[4:7], a[128:131], v[132:135]\n"
  "ds_read_b64_tr_b16 a[18:19], v232 offset:39584\n"
  "v_mfma_f32_16x16x32_bf16 v[136:139], a[4:7], a[132:135], v[136:139]\n"
  "v_mfma_f32_16x16x32_bf16 v[140:143], a[4:7], a[136:139], v[140:143]\n"
  "s_waitcnt lgkmcnt(4)\n"
  "v_mfma_f32_16x16x32_bf16 v[144:147], a[8:11], a[124:127], v[144:147]\n"
  "ds_read_b64_tr_b16 a[20:21], v232 offset:28864\n"
  "v_mfma_f32_16x16x32_bf16 v[148:151], a[8:11], a[128:131], v[148:151]\n"
  "ds_read_b64_tr_b16 a[22:23], v232 offset:32448\n"
  "v_mfma_f32_16x16x32_bf16 v[152:155], a[8:11], a[132:135], v[152:155]\n"
  "s_mov_b32 s21, 118784\n"
  "buffer_load_dwordx4 a[108:111], v231, s[12:15], s21 offen offset:0 sc1\n"
  "v_mfma_f32_16x16x32_bf16 v[156:159], a[8:11], a[136:139], v[156:159]\n"
  "s_waitcnt lgkmcnt(4)\n"
  "v_mfma_f32_16x16x32_bf16 v[160:163], a[12:15], a[124:127], v[160:163]\n"
  "ds_read_b64_tr_b16 a[24:25], v232 offset:36032\n"
  "v_mfma_f32_16x16x32_bf16 v[164:167], a[12:15], a[128:131], v[164:167]\n"
  "ds_read_b64_tr_b16 a[26:27], v232 offset:39616\n"
  "v_mfma_f32_16x16x32_bf16 v[168:171], a[12:15], a[132:135], v[168:171]\n"
  "v_mfma_f32_16x16x32_bf16 v[172:175], a[12:15], a[136:139], v[172:175]\n"
  "s_waitcnt lgkmcnt(4)\n"
  "v_mfma_f32_16x16x32_bf16 v[176:179], a[16:19], a[124:127], v[176:179]\n"
  "ds_read_b64_tr_b16 a[0:1], v232 offset:43008\n"
  "v_mfma_f32_16x16x32_bf16 v[180:183], a[16:19], a[128:131], v[180:183]\n"
  "ds_read_b64_tr_b16 a[2:3], v232 offset:46592\n"
  "v_mfma_f32_16x16x32_bf16 v[184:187], a[16:19], a[132:135], v[184:187]\n"
  "buffer_load_dwordx4 a[112:115], v231, s[12:15], s21 offen offset:1024 sc1\n"
  "v_mfma_f32_16x16x32_bf16 v[188:191], a[16:19], a[136:139], v[188:191]\n"
  "s_waitcnt lgkmcnt(4)\n"
  "v_mfma_f32_16x16x32_bf16 v[192:195], a[20:23], a[124:127], v[192:195]\n"
  "ds_read_b64_tr_b16 a[4:5], v232 offset:50176\n"
  "v_mfma_f32_16x16x32_bf16 v[196:199], a[20:23], a[128:131], v[196:199]\n"
  "ds_read_b64_tr_b16 a[6:7], v232 offset:53760\n"
  "v_mfma_f32_16x16x32_bf16 v[200:203], a[20:23], a[132:135], v[200:203]\n"
  "buffer_load_dwordx4 a[116:119], v231, s[12:15], s21 offen offset:2048 sc1\n"
  "v_mfma_f32_16x16x32_bf16 v[204:207], a[20:23], a[136:139], v[204:207]\n"
  "s_waitcnt lgkmcnt(4)\n"
  "v_mfma_f32_16x16x32_bf16 v[208:211], a[24:27], a[124:127], v[208:211]\n"
  "ds_read_b64_tr_b16 a[8:9], v232 offset:43040\n"
  "v_mfma_f32_16x16x32_bf16 v[212:215], a[24:27], a[128:131], v[212:215]\n"
  "ds_read_b64_tr_b16 a[10:11], v232 offset:46624\n"
  "v_mfma_f32_16x16x32_bf16 v[216:219], a[24:27], a[132:135], v[216:219]\n"
  "v_mfma_f32_16x16x32_bf16 v[220:223], a[24:27], a[136:139], v[220:223]\n"
  "s_waitcnt vmcnt(45) lgkmcnt(4)\n"
  "v_mfma_f32_16x16x32_bf16 v[0:3], a[0:3], a[140:143], v[0:3]\n"
  "ds_read_b64_tr_b16 a[12:13], v232 offset:50208\n"
  "v_mfma_f32_16x16x32_bf16 v[4:7], a[0:3], a[144:147], v[4:7]\n"
  "ds_read_b64_tr_b16 a[14:15], v232 offset:53792\n"
  "v_mfma_f32_16x16x32_bf16 v[8:11], a[0:3], a[148:151], v[8:11]\n"
  "buffer_load_dwordx4 a[120:123], v231, s[12:15], s21 offen offset:3072 sc1\n"
  "v_mfma_f32_16x16x32_bf16 v[12:15], a[0:3], a[152:155], v[12:15]\n"
  "s_waitcnt lgkmcnt(4)\n"
  "v_mfma_f32_16x16x32_bf16 v[16:19], a[4:7], a[140:143], v[16:19]\n"
  "ds_read_b64_tr_b16 a[16:17], v232 offset:43072\n"
  "v_mfma_f32_16x16x32_bf16 v[20:23], a[4:7], a[144:147], v[20:23]\n"
  "ds_read_b64_tr_b16 a[18:19], v232 offset:46656\n"
  "v_mfma_f32_16x16x32_bf16 v[24:27], a[4:7], a[148:151], v[24:27]\n"
  "v_mfma_f32_16x16x32_bf16 v[28:31], a[4:7], a[152:155], v[28:31]\n"
  "s_waitcnt lgkmcnt(4)\n"
  "v_mfma_f32_16x16x32_bf16 v[32:35], a[8:11], a[140:143], v[32:35]\n"
  "ds_read_b64_tr_b16 a[20:21], v232 offset:50240\n"
  "v_mfma_f32_16x16x32_bf16 v[36:39], a[8:11], a[144:147], v[36:39]\n"
  "ds_read_b64_tr_b16 a[22:23], v232 offset:53824\n"
  "v_mfma_f32_16x16x32_bf16 v[40:43], a[8:11], a[148:151], v[40:43]\n"
  "s_mov_b32 s21, 122880\n"
  "buffer_load_dwordx4 a[124:127], v231, s[12:15], s21 offen offset:0 sc1\n"
  "v_mfma_f32_16x16x32_bf16 v[44:47], a[8:11], a[152:155], v[44:47]\n"
  "s_waitcnt lgkmcnt(4)\n"
  "v_mfma_f32_16x16x32_bf16 v[48:51], a[12:15], a[140:143], v[48:51]\n"
  "ds_read_b64_tr_b16 a[24:25], v232 offset:43104\n"
  "v_mfma_f32_16x16x32_bf16 v[52:55], a[12:15], a[144:147], v[52:55]\n"
  "ds_read_b64_tr_b16 a[26:27], v232 offset:46688\n"
  "v_mfma_f32_16x16x32_bf16 v[56:59], a[12:15], a[148:151], v[56:59]\n"
  "buffer_load_dwordx4 a[128:131], v231, s[12:15], s21 offen offset:1024 sc1\n"
  "v_mfma_f32_16x16x32_bf16 v[60:63], a[12:15], a[152:155], v[60:63]\n"
  "s_waitcnt lgkmcnt(4)\n"
  "v_mfma_f32_16x16x32_bf16 v[64:67], a[16:19], a[140:143], v[64:67]\n"
  "ds_read_b64_tr_b16 a[0:1], v232 offset:50272\n"
  "v_mfma_f32_16x16x32_bf16 v[68:71], a[16:19], a[144:147], v[68:71]\n"
  "ds_read_b64_tr_b16 a[2:3], v232 offset:53856\n"
  "v_mfma_f32_16x16x32_bf16 v[72:75], a[16:19], a[148:151], v[72:75]\n"
  "v_mfma_f32_16x16x32_bf16 v[76:79], a[16:19], a[152:155], v[76:79]\n"
  "s_waitcnt lgkmcnt(4)\n"
  "v_mfma_f32_16x16x32_bf16 v[80:83], a[20:23], a[140:143], v[80:83]\n"
  "ds_read_b64_tr_b16 a[4:5], v232 offset:43136\n"
  "v_mfma_f32_16x16x32_bf16 v[84:87], a[20:23], a[144:147], v[84:87]\n"
  "ds_read_b64_tr_b16 a[6:7], v232 offset:46720\n"
  "v_mfma_f32_16x16x32_bf16 v[88:91], a[20:23], a[148:151], v[88:91]\n"
  "buffer_load_dwordx4 a[132:135], v231, s[12:15], s21 offen offset:2048 sc1\n"
  "v_mfma_f32_16x16x32_bf16 v[92:95], a[20:23], a[152:155], v[92:95]\n"
  "s_waitcnt lgkmcnt(4)\n"
  "v_mfma_f32_16x16x32_bf16 v[96:99], a[24:27], a[140:143], v[96:99]\n"
  "ds_read_b64_tr_b16 a[8:9], v232 offset:50304\n"
  "v_mfma_f32_16x16x32_bf16 v[100:103], a[24:27], a[144:147], v[100:103]\n"
  "ds_read_b64_tr_b16 a[10:11], v232 offset:53888\n"
  "v_mfma_f32_16x16x32_bf16 v[104:107], a[24:27], a[148:151], v[104:107]\n"
  "v_mfma_f32_16x16x32_bf16 v[108:111], a[24:27], a[152:155], v[108:111]\n"
  "s_waitcnt lgkmcnt(4)\n"
  "v_mfma_f32_16x16x32_bf16 v[112:115], a[0:3], a[140:143], v[112:115]\n"
  "ds_read_b64_tr_b16 a[12:13], v232 offset:43168\n"
  "v_mfma_f32_16x16x32_bf16 v[116:119], a[0:3], a[144:147], v[116:119]\n"
  "ds_read_b64_tr_b16 a[14:15], v232 offset:46752\n"
  "v_mfma_f32_16x16x32_bf16 v[120:123], a[0:3], a[148:151], v[120:123]\n"
  "buffer_load_dwordx4 a[136:139], v231, s[12:15], s21 offen offset:3072 sc1\n"
  "v_mfma_f32_16x16x32_bf16 v[124:127], a[0:3], a[152:155], v[124:127]\n"
  "s_waitcnt lgkmcnt(4)\n"
  "v_mfma_f32_16x16x32_bf16 v[128:131], a[4:7], a[140:143], v[128:131]\n"
  "ds_read_b64_tr_b16 a[16:17], v232 offset:50336\n"
  "v_mfma_f32_16x16x32_bf16 v[132:135], a[4:7], a[144:147], v[132:135]\n"
  "ds_read_b64_tr_b16 a[18:19], v232 offset:53920\n"
  "v_mfma_f32_16x16x32_bf16 v[136:139], a[4:7], a[148:151], v[136:139]\n"
  "v_mfma_f32_16x16x32_bf16 v[140:143], a[4:7], a[152:155], v[140:143]\n"
  "s_waitcnt lgkmcnt(4)\n"
  "v_mfma_f32_16x16x32_bf16 v[144:147], a[8:11], a[140:143], v[144:147]\n"
  "ds_read_b64_tr_b16 a[20:21], v232 offset:43200\n"
  "v_mfma_f32_16x16x32_bf16 v[148:151], a[8:11], a[144:147], v[148:151]\n"
  "ds_read_b64_tr_b16 a[22:23], v232 offset:46784\n"
  "v_mfma_f32_16x16x32_bf16 v[152:155], a[8:11], a[148:151], v[152:155]\n"
  "v_mfma_f32_16x16x32_bf16 v[156:159], a[8:11], a[152:155], v[156:159]\n"
  "s_waitcnt lgkmcnt(4)\n"
  "v_mfma_f32_16x16x32_bf16 v[160:163], a[12:15], a[140:143], v[160:163]\n"
  "ds_read_b64_tr_b16 a[24:25], v232 offset:50368\n"
  "v_mfma_f32_16x16x32_bf16 v[164:167], a[12:15], a[144:147], v[164:167]\n"
  "ds_read_b64_tr_b16 a[26:27], v232 offset:53952\n"
  "v_mfma_f32_16x16x32_bf16 v[168:171], a[12:15], a[148:151], v[168:171]\n"
  "v_mfma_f32_16x16x32_bf16 v[172:175], a[12:15], a[152:155], v[172:175]\n"
  "s_waitcnt vmcnt(39)\n"
  "s_barrier\n"
  "s_waitcnt lgkmcnt(4)\n"
  "v_mfma_f32_16x16x32_bf16 v[176:179], a[16:19], a[140:143], v[176:179]\n"
  "ds_read_b64_tr_b16 a[0:1], v233 offset:0\n"
  "v_mfma_f32_16x16x32_bf16 v[180:183], a[16:19], a[144:147], v[180:183]\n"
  "ds_read_b64_tr_b16 a[2:3], v233 offset:3584\n"
  "v_mfma_f32_16x16x32_bf16 v[184:187], a[16:19], a[148:151], v[184:187]\n"
  "s_mov_b32 s20, 6881280\n"
  "s_add_u32 m0, s23, 0\n"
  "s_nop 0\n"
  "buffer_load_dwordx4 v224, s[8:11], s20 offen offset:0 lds\n"
  "v_mfma_f32_16x16x32_bf16 v[188:191], a[16:19], a[152:155], v[188:191]\n"
  "s_waitcnt lgkmcnt(4)\n"
  "v_mfma_f32_16x16x32_bf16 v[192:195], a[20:23], a[140:143], v[192:195]\n"
  "ds_read_b64_tr_b16 a[4:5], v233 offset:7168\n"
  "v_mfma_f32_16x16x32_bf16 v[196:199], a[20:23], a[144:147], v[196:199]\n"
  "ds_read_b64_tr_b16 a[6:7], v233 offset:10752\n"
  "v_mfma_f32_16x16x32_bf16 v[200:203], a[20:23], a[148:151], v[200:203]\n"
  "buffer_load_dwordx4 v225, s[8:11], s20 offen offset:1024 lds\n"
  "v_mfma_f32_16x16x32_bf16 v[204:207], a[20:23], a[152:155], v[204:207]\n"
  "s_waitcnt lgkmcnt(4)\n"
  "v_mfma_f32_16x16x32_bf16 v[208:211], a[24:27], a[140:143], v[208:211]\n"
  "ds_read_b64_tr_b16 a[8:9], v233 offset:32\n"
  "v_mfma_f32_16x16x32_bf16 v[212:215], a[24:27], a[144:147], v[212:215]\n"
  "ds_read_b64_tr_b16 a[10:11], v233 offset:3616\n"
  "v_mfma_f32_16x16x32_bf16 v[216:219], a[24:27], a[148:151], v[216:219]\n"
  "v_mfma_f32_16x16x32_bf16 v[220:223], a[24:27], a[152:155], v[220:223]\n"
  "s_waitcnt lgkmcnt(4)\n"
  "v_mfma_f32_16x16x32_bf16 v[0:3], a[0:3], a[28:31], v[0:3]\n"
  "ds_read_b64_tr_b16 a[12:13], v233 offset:7200\n"
  "v_mfma_f32_16x16x32_bf16 v[4:7], a[0:3], a[32:35], v[4:7]\n"
  "ds_read_b64_tr_b16 a[14:15], v233 offset:10784\n"
  "v_mfma_f32_16x16x32_bf16 v[8:11], a[0:3], a[36:39], v[8:11]\n"
  "buffer_load_dwordx4 v226, s[8:11], s20 offen offset:2048 lds\n"
  "v_mfma_f32_16x16x32_bf16 v[12:15], a[0:3], a[40:43], v[12:15]\n"
  "s_waitcnt lgkmcnt(4)\n"
  "v_mfma_f32_16x16x32_bf16 v[16:19], a[4:7], a[28:31], v[16:19]\n"
  "ds_read_b64_tr_b16 a[16:17], v233 offset:64\n"
  "v_mfma_f32_16x16x32_bf16 v[20:23], a[4:7], a[32:35], v[20:23]\n"
  "ds_read_b64_tr_b16 a[18:19], v233 offset:3648\n"
  "v_mfma_f32_16x16x32_bf16 v[24:27], a[4:7], a[36:39], v[24:27]\n"
  "v_mfma_f32_16x16x32_bf16 v[28:31], a[4:7], a[40:43], v[28:31]\n"
  "s_waitcnt lgkmcnt(4)\n"
  "v_mfma_f32_16x16x32_bf16 v[32:35], a[8:11], a[28:31], v[32:35]\n"
  "ds_read_b64_tr_b16 a[20:21], v233 offset:7232\n"
  "v_mfma_f32_16x16x32_bf16 v[36:39], a[8:11], a[32:35], v[36:39]\n"
  "ds_read_b64_tr_b16 a[22:23], v233 offset:10816\n"
  "v_mfma_f32_16x16x32_bf16 v[40:43], a[8:11], a[36:39], v[40:43]\n"
  "buffer_load_dwordx4 v227, s[8:11], s20 offen offset:3072 lds\n"
  "v_mfma_f32_16x16x32_bf16 v[44:47], a[8:11], a[40:43], v[44:47]\n"
  "s_waitcnt lgkmcnt(4)\n"
  "v_mfma_f32_16x16x32_bf16 v[48:51], a[12:15], a[28:31], v[48:51]\n"
  "ds_read_b64_tr_b16 a[24:25], v233 offset:96\n"
  "v_mfma_f32_16x16x32_bf16 v[52:55], a[12:15], a[32:35], v[52:55]\n"
  "ds_read_b64_tr_b16 a[26:27], v233 offset:3680\n"
  "v_mfma_f32_16x16x32_bf16 v[56:59], a[12:15], a[36:39], v[56:59]\n"
  "v_mfma_f32_16x16x32_bf16 v[60:63], a[12:15], a[40:43], v[60:63]\n"
  "s_waitcnt lgkmcnt(4)\n"
  "v_mfma_f32_16x16x32_bf16 v[64:67], a[16:19], a[28:31], v[64:67]\n"
  "ds_read_b64_tr_b16 a[0:1], v233 offset:7264\n"
  "v_mfma_f32_16x16x32_bf16 v[68:71], a[16:19], a[32:35], v[68:71]\n"
  "ds_read_b64_tr_b16 a[2:3], v233 offset:10848\n"
  "v_mfma_f32_16x16x32_bf16 v[72:75], a[16:19], a[36:39], v[72:75]\n"
  "s_add_u32 m0, s23, 4096\n"
  "s_nop 0\n"
  "buffer_load_dwordx4 v228, s[8:11], s20 offen offset:0 lds\n"
  "v_mfma_f32_16x16x32_bf16 v[76:79], a[16:19], a[40:43], v[76:79]\n"
  "s_waitcnt lgkmcnt(4)\n"
  "v_mfma_f32_16x16x32_bf16 v[80:83], a[20:23], a[28:31], v[80:83]\n"
  "ds_read_b64_tr_b16 a[4:5], v233 offset:128\n"
  "v_mfma_f32_16x16x32_bf16 v[84:87], a[20:23], a[32:35], v[84:87]\n"
  "ds_read_b64_tr_b16 a[6:7], v233 offset:3712\n"
  "v_mfma_f32_16x16x32_bf16 v[88:91], a[20:23], a[36:39], v[88:91]\n"
  "buffer_load_dwordx4 v229, s[8:11], s20 offen offset:1024 lds\n"
  "v_mfma_f32_16x16x32_bf16 v[92:95], a[20:23], a[40:43], v[92:95]\n"
  "s_waitcnt lgkmcnt(4)\n"
  "v_mfma_f32_16x16x32_bf16 v[96:99], a[24:27], a[28:31], v[96:99]\n"
  "ds_read_b64_tr_b16 a[8:9], v233 offset:7296\n"
  "v_mfma_f32_16x16x32_bf16 v[100:103], a[24:27], a[32:35], v[100:103]\n"
  "ds_read_b64_tr_b16 a[10:11], v233 offset:10880\n"
  "v_mfma_f32_16x16x32_bf16 v[104:107], a[24:27], a[36:39], v[104:107]\n"
  "v_mfma_f32_16x16x32_bf16 v[108:111], a[24:27], a[40:43], v[108:111]\n"
  "s_waitcnt lgkmcnt(4)\n"
  "v_mfma_f32_16x16x32_bf16 v[112:115], a[0:3], a[28:31], v[112:115]\n"
  "ds_read_b64_tr_b16 a[12:13], v233 offset:160\n"
  "v_mfma_f32_16x16x32_bf16 v[116:119], a[0:3], a[32:35], v[116:119]\n"
  "ds_read_b64_tr_b16 a[14:15], v233 offset:3744\n"
  "v_mfma_f32_16x16x32_bf16 v[120:123], a[0:3], a[36:39], v[120:123]\n"
  "buffer_load_dwordx4 v230, s[8:11], s20 offen offset:2048 lds\n"
  "v_mfma_f32_16x16x32_bf16 v[124:127], a[0:3], a[40:43], v[124:127]\n"
  "s_waitcnt lgkmcnt(4)\n"
  "v_mfma_f32_16x16x32_bf16 v[128:131], a[4:7], a[28:31], v[128:131]\n"
  "ds_read_b64_tr_b16 a[16:17], v233 offset:7328\n"
  "v_mfma_f32_16x16x32_bf16 v[132:135], a[4:7], a[32:35], v[132:135]\n"
  "ds_read_b64_tr_b16 a[18:19], v233 offset:10912\n"
  "v_mfma_f32_16x16x32_bf16 v[136:139], a[4:7], a[36:39], v[136:139]\n"
  "v_mfma_f32_16x16x32_bf16 v[140:143], a[4:7], a[40:43], v[140:143]\n"
  "s_waitcnt lgkmcnt(4)\n"
  "v_mfma_f32_16x16x32_bf16 v[144:147], a[8:11], a[28:31], v[144:147]\n"
  "ds_read_b64_tr_b16 a[20:21], v233 offset:192\n"
  "v_mfma_f32_16x16x32_bf16 v[148:151], a[8:11], a[32:35], v[148:151]\n"
  "ds_read_b64_tr_b16 a[22:23], v233 offset:3776\n"
  "v_mfma_f32_16x16x32_bf16 v[152:155], a[8:11], a[36:39], v[152:155]\n"
  "s_mov_b32 s21, 126976\n"
  "buffer_load_dwordx4 a[140:143], v231, s[12:15], s21 offen offset:0 sc1\n"
  "v_mfma_f32_16x16x32_bf16 v[156:159], a[8:11], a[40:43], v[156:159]\n"
  "s_waitcnt lgkmcnt(4)\n"
  "v_mfma_f32_16x16x32_bf16 v[160:163], a[12:15], a[28:31], v[160:163]\n"
  "ds_read_b64_tr_b16 a[24:25], v233 offset:7360\n"
  "v_mfma_f32_16x16x32_bf16 v[164:167], a[12:15], a[32:35], v[164:167]\n"
  "ds_read_b64_tr_b16 a[26:27], v233 offset:10944\n"
  "v_mfma_f32_16x16x32_bf16 v[168:171], a[12:15], a[36:39], v[168:171]\n"
  "v_mfma_f32_16x16x32_bf16 v[172:175], a[12:15], a[40:43], v[172:175]\n"
  "s_waitcnt lgkmcnt(4)\n"
  "v_mfma_f32_16x16x32_bf16 v[176:179], a[16:19], a[28:31], v[176:179]\n"
  "ds_read_b64_tr_b16 a[0:1], v233 offset:14336\n"
  "v_mfma_f32_16x16x32_bf16 v[180:183], a[16:19], a[32:35], v[180:183]\n"
  "ds_read_b64_tr_b16 a[2:3], v233 offset:17920\n"
  "v_mfma_f32_16x16x32_bf16 v[184:187], a[16:19], a[36:39], v[184:187]\n"
  "buffer_load_dwordx4 a[144:147], v231, s[12:15], s21 offen offset:1024 sc1\n"
  "v_mfma_f32_16x16x32_bf16 v[188:191], a[16:19], a[40:43], v[188:191]\n"
  "s_waitcnt lgkmcnt(4)\n"
  "v_mfma_f32_16x16x32_bf16 v[192:195], a[20:23], a[28:31], v[192:195]\n"
  "ds_read_b64_tr_b16 a[4:5], v233 offset:21504\n"
  "v_mfma_f32_16x16x32_bf16 v[196:199], a[20:23], a[32:35], v[196:199]\n"
  "ds_read_b64_tr_b16 a[6:7], v233 offset:25088\n"
  "v_mfma_f32_16x16x32_bf16 v[200:203], a[20:23], a[36:39], v[200:203]\n"
  "buffer_load_dwordx4 a[148:151], v231, s[12:15], s21 offen offset:2048 sc1\n"
  "v_mfma_f32_16x16x32_bf16 v[204:207], a[20:23], a[40:43], v[204:207]\n"
  "s_waitcnt lgkmcnt(4)\n"
  "v_mfma_f32_16x16x32_bf16 v[208:211], a[24:27], a[28:31], v[208:211]\n"
  "ds_read_b64_tr_b16 a[8:9], v233 offset:14368\n"
  "v_mfma_f32_16x16x32_bf16 v[212:215], a[24:27], a[32:35], v[212:215]\n"
  "ds_read_b64_tr_b16 a[10:11], v233 offset:17952\n"
  "v_mfma_f32_16x16x32_bf16 v[216:219], a[24:27], a[36:39], v[216:219]\n"
  "v_mfma_f32_16x16x32_bf16 v[220:223], a[24:27], a[40:43], v[220:223]\n"
  "s_waitcnt vmcnt(45) lgkmcnt(4)\n"
  "v_mfma_f32_16x16x32_bf16 v[0:3], a[0:3], a[44:47], v[0:3]\n"
  "ds_read_b64_tr_b16 a[12:13], v233 offset:21536\n"
  "v_mfma_f32_16x16x32_bf16 v[4:7], a[0:3], a[48:51], v[4:7]\n"
  "ds_read_b64_tr_b16 a[14:15], v233 offset:25120\n"
  "v_mfma_f32_16x16x32_bf16 v[8:11], a[0:3], a[52:55], v[8:11]\n"
  "buffer_load_dwordx4 a[152:155], v231, s[12:15], s21 offen offset:3072 sc1\n"
  "v_mfma_f32_16x16x32_bf16 v[12:15], a[0:3], a[56:59], v[12:15]\n"
  "s_waitcnt lgkmcnt(4)\n"
  "v_mfma_f32_16x16x32_bf16 v[16:19], a[4:7], a[44:47], v[16:19]\n"
  "ds_read_b64_tr_b16 a[16:17], v233 offset:14400\n"
  "v_mfma_f32_16x16x32_bf16 v[20:23], a[4:7], a[48:51], v[20:23]\n"
  "ds_read_b64_tr_b16 a[18:19], v233 offset:17984\n"
  "v_mfma_f32_16x16x32_bf16 v[24:27], a[4:7], a[52:55], v[24:27]\n"
  "v_mfma_f32_16x16x32_bf16 v[28:31], a[4:7], a[56:59], v[28:31]\n"
  "s_waitcnt lgkmcnt(4)\n"
  "v_mfma_f32_16x16x32_bf16 v[32:35], a[8:11], a[44:47], v[32:35]\n"
  "ds_read_b64_tr_b16 a[20:21], v233 offset:21568\n"
  "v_mfma_f32_16x16x32_bf16 v[36:39], a[8:11], a[48:51], v[36:39]\n"
  "ds_read_b64_tr_b16 a[22:23], v233 offset:25152\n"
  "v_mfma_f32_16x16x32_bf16 v[40:43], a[8:11], a[52:55], v[40:43]\n"
  "buffer_load_dwordx4 a[156:159], v231, s[52:55], s27 offen offset:0\n"
  "v_mfma_f32_16x16x32_bf16 v[44:47], a[8:11], a[56:59], v[44:47]\n"
  "s_waitcnt lgkmcnt(4)\n"
  "v_mfma_f32_16x16x32_bf16 v[48:51], a[12:15], a[44:47], v[48:51]\n"
  "ds_read_b64_tr_b16 a[24:25], v233 offset:14432\n"
  "v_mfma_f32_16x16x32_bf16 v[52:55], a[12:15], a[48:51], v[52:55]\n"
  "ds_read_b64_tr_b16 a[26:27], v233 offset:18016\n"
  "v_mfma_f32_16x16x32_bf16 v[56:59], a[12:15], a[52:55], v[56:59]\n"
  "v_mfma_f32_16x16x32_bf16 v[60:63], a[12:15], a[56:59], v[60:63]\n"
  "s_waitcnt lgkmcnt(4)\n"
  "v_mfma_f32_16x16x32_bf16 v[64:67], a[16:19], a[44:47], v[64:67]\n"
  "ds_read_b64_tr_b16 a[0:1], v233 offset:21600\n"
  "v_mfma_f32_16x16x32_bf16 v[68:71], a[16:19], a[48:51], v[68:71]\n"
  "ds_read_b64_tr_b16 a[2:3], v233 offset:25184\n"
  "v_mfma_f32_16x16x32_bf16 v[72:75], a[16:19], a[52:55], v[72:75]\n"
  "v_mfma_f32_16x16x32_bf16 v[76:79], a[16:19], a[56:59], v[76:79]\n"
  "s_waitcnt lgkmcnt(4)\n"
  "v_mfma_f32_16x16x32_bf16 v[80:83], a[20:23], a[44:47], v[80:83]\n"
  "ds_read_b64_tr_b16 a[4:5], v233 offset:14464\n"
  "v_mfma_f32_16x16x32_bf16 v[84:87], a[20:23], a[48:51], v[84:87]\n"
  "ds_read_b64_tr_b16 a[6:7], v233 offset:18048\n"
  "v_mfma_f32_16x16x32_bf16 v[88:91], a[20:23], a[52:55], v[88:91]\n"
  "v_mfma_f32_16x16x32_bf16 v[92:95], a[20:23], a[56:59], v[92:95]\n"
  "s_waitcnt lgkmcnt(4)\n"
  "v_mfma_f32_16x16x32_bf16 v[96:99], a[24:27], a[44:47], v[96:99]\n"
  "ds_read_b64_tr_b16 a[8:9], v233 offset:21632\n"
  "v_mfma_f32_16x16x32_bf16 v[100:103], a[24:27], a[48:51], v[100:103]\n"
  "ds_read_b64_tr_b16 a[10:11], v233 offset:25216\n"
  "v_mfma_f32_16x16x32_bf16 v[104:107], a[24:27], a[52:55], v[104:107]\n"
  "buffer_load_dwordx4 a[156:159], v231, s[52:55], s27 offen offset:1024\n"
  "v_mfma_f32_16x16x32_bf16 v[108:111], a[24:27], a[56:59], v[108:111]\n"
  "s_waitcnt lgkmcnt(4)\n"
  "v_mfma_f32_16x16x32_bf16 v[112:115], a[0:3], a[44:47], v[112:115]\n"
  "ds_read_b64_tr_b16 a[12:13], v233 offset:14496\n"
  "v_mfma_f32_16x16x32_bf16 v[116:119], a[0:3], a[48:51], v[116:119]\n"
  "ds_read_b64_tr_b16 a[14:15], v233 offset:18080\n"
  "v_mfma_f32_16x16x32_bf16 v[120:123], a[0:3], a[52:55], v[120:123]\n"
  "v_mfma_f32_16x16x32_bf16 v[124:127], a[0:3], a[56:59], v[124:127]\n"
  "s_waitcnt lgkmcnt(4)\n"
  "v_mfma_f32_16x16x32_bf16 v[128:131], a[4:7], a[44:47], v[128:131]\n"
  "ds_read_b64_tr_b16 a[16:17], v233 offset:21664\n"
  "v_mfma_f32_16x16x32_bf16 v[132:135], a[4:7], a[48:51], v[132:135]\n"
  "ds_read_b64_tr_b16 a[18:19], v233 offset:25248\n"
  "v_mfma_f32_16x16x32_bf16 v[136:139], a[4:7], a[52:55], v[136:139]\n"
  "v_mfma_f32_16x16x32_bf16 v[140:143], a[4:7], a[56:59], v[140:143]\n"
  "s_waitcnt lgkmcnt(4)\n"
  "v_mfma_f32_16x16x32_bf16 v[144:147], a[8:11], a[44:47], v[144:147]\n"
  "ds_read_b64_tr_b16 a[20:21], v233 offset:14528\n"
  "v_mfma_f32_16x16x32_bf16 v[148:151], a[8:11], a[48:51], v[148:151]\n"
  "ds_read_b64_tr_b16 a[22:23], v233 offset:18112\n"
  "v_mfma_f32_16x16x32_bf16 v[152:155], a[8:11], a[52:55], v[152:155]\n"
  "v_mfma_f32_16x16x32_bf16 v[156:159], a[8:11], a[56:59], v[156:159]\n"
  "s_waitcnt lgkmcnt(4)\n"
  "v_mfma_f32_16x16x32_bf16 v[160:163], a[12:15], a[44:47], v[160:163]\n"
  "ds_read_b64_tr_b16 a[24:25], v233 offset:21696\n"
  "v_mfma_f32_16x16x32_bf16 v[164:167], a[12:15], a[48:51], v[164:167]\n"
  "ds_read_b64_tr_b16 a[26:27], v233 offset:25280\n"
  "v_mfma_f32_16x16x32_bf16 v[168:171], a[12:15], a[52:55], v[168:171]\n"
  "v_mfma_f32_16x16x32_bf16 v[172:175], a[12:15], a[56:59], v[172:175]\n"
  "s_waitcnt vmcnt(37)\n"
  "s_barrier\n"
  "s_waitcnt lgkmcnt(4)\n"
  "v_mfma_f32_16x16x32_bf16 v[176:179], a[16:19], a[44:47], v[176:179]\n"
  "ds_read_b64_tr_b16 a[0:1], v233 offset:28672\n"
  "v_mfma_f32_16x16x32_bf16 v[180:183], a[16:19], a[48:51], v[180:183]\n"
  "ds_read_b64_tr_b16 a[2:3], v233 offset:32256\n"
  "v_mfma_f32_16x16x32_bf16 v[184:187], a[16:19], a[52:55], v[184:187]\n"
  "v_mfma_f32_16x16x32_bf16 v[188:191], a[16:19], a[56:59], v[188:191]\n"
  "s_waitcnt lgkmcnt(4)\n"
  "v_mfma_f32_16x16x32_bf16 v[192:195], a[20:23], a[44:47], v[192:195]\n"
  "ds_read_b64_tr_b16 a[4:5], v233 offset:35840\n"
  "v_mfma_f32_16x16x32_bf16 v[196:199], a[20:23], a[48:51], v[196:199]\n"
  "ds_read_b64_tr_b16 a[6:7], v233 offset:39424\n"
  "v_mfma_f32_16x16x32_bf16 v[200:203], a[20:23], a[52:55], v[200:203]\n"
  "buffer_load_dwordx4 a[156:159], v231, s[52:55], s27 offen offset:2048\n"
  "v_mfma_f32_16x16x32_bf16 v[204:207], a[20:23], a[56:59], v[204:207]\n"
  "s_waitcnt lgkmcnt(4)\n"
  "v_mfma_f32_16x16x32_bf16 v[208:211], a[24:27], a[44:47], v[208:211]\n"
  "ds_read_b64_tr_b16 a[8:9], v233 offset:28704\n"
  "v_mfma_f32_16x16x32_bf16 v[212:215], a[24:27], a[48:51], v[212:215]\n"
  "ds_read_b64_tr_b16 a[10:11], v233 offset:32288\n"
  "v_mfma_f32_16x16x32_bf16 v[216:219], a[24:27], a[52:55], v[216:219]\n"
  "v_mfma_f32_16x16x32_bf16 v[220:223], a[24:27], a[56:59], v[220:223]\n"
  "s_waitcnt lgkmcnt(4)\n"
  "v_mfma_f32_16x16x32_bf16 v[0:3], a[0:3], a[60:63], v[0:3]\n"
  "ds_read_b64_tr_b16 a[12:13], v233 offset:35872\n"
  "v_mfma_f32_16x16x32_bf16 v[4:7], a[0:3], a[64:67], v[4:7]\n"
  "ds_read_b64_tr_b16 a[14:15], v233 offset:39456\n"
  "v_mfma_f32_16x16x32_bf16 v[8:11], a[0:3], a[68:71], v[8:11]\n"
  "v_mfma_f32_16x16x32_bf16 v[12:15], a[0:3], a[72:75], v[12:15]\n"
  "s_waitcnt lgkmcnt(4)\n"
  "v_mfma_f32_16x16x32_bf16 v[16:19], a[4:7], a[60:63], v[16:19]\n"
  "ds_read_b64_tr_b16 a[16:17], v233 offset:28736\n"
  "v_mfma_f32_16x16x32_bf16 v[20:23], a[4:7], a[64:67], v[20:23]\n"
  "ds_read_b64_tr_b16 a[18:19], v233 offset:32320\n"
  "v_mfma_f32_16x16x32_bf16 v[24:27], a[4:7], a[68:71], v[24:27]\n"
  "v_mfma_f32_16x16x32_bf16 v[28:31], a[4:7], a[72:75], v[28:31]\n"
  "s_waitcnt lgkmcnt(4)\n"
  "v_mfma_f32_16x16x32_bf16 v[32:35], a[8:11], a[60:63], v[32:35]\n"
  "ds_read_b64_tr_b16 a[20:21], v233 offset:35904\n"
  "v_mfma_f32_16x16x32_bf16 v[36:39], a[8:11], a[64:67], v[36:39]\n"
  "ds_read_b64_tr_b16 a[22:23], v233 offset:39488\n"
  "v_mfma_f32_16x16x32_bf16 v[40:43], a[8:11], a[68:71], v[40:43]\n"
  "v_mfma_f32_16x16x32_bf16 v[44:47], a[8:11], a[72:75], v[44:47]\n"
  "s_waitcnt lgkmcnt(4)\n"
  "v_mfma_f32_16x16x32_bf16 v[48:51], a[12:15], a[60:63], v[48:51]\n"
  "ds_read_b64_tr_b16 a[24:25], v233 offset:28768\n"
  "v_mfma_f32_16x16x32_bf16 v[52:55], a[12:15], a[64:67], v[52:55]\n"
  "ds_read_b64_tr_b16 a[26:27], v233 offset:32352\n"
  "v_mfma_f32_16x16x32_bf16 v[56:59], a[12:15], a[68:71], v[56:59]\n"
  "v_mfma_f32_16x16x32_bf16 v[60:63], a[12:15], a[72:75], v[60:63]\n"
  "s_waitcnt lgkmcnt(4)\n"
  "v_mfma_f32_16x16x32_bf16 v[64:67], a[16:19], a[60:63], v[64:67]\n"
  "ds_read_b64_tr_b16 a[0:1], v233 offset:35936\n"
  "v_mfma_f32_16x16x32_bf16 v[68:71], a[16:19], a[64:67], v[68:71]\n"
  "ds_read_b64_tr_b16 a[2:3], v233 offset:39520\n"
  "v_mfma_f32_16x16x32_bf16 v[72:75], a[16:19], a[68:71], v[72:75]\n"
  "buffer_load_dwordx4 a[156:159], v231, s[52:55], s27 offen offset:3072\n"
  "v_mfma_f32_16x16x32_bf16 v[76:79], a[16:19], a[72:75], v[76:79]\n"
  "s_waitcnt lgkmcnt(4)\n"
  "v_mfma_f32_16x16x32_bf16 v[80:83], a[20:23], a[60:63], v[80:83]\n"
  "ds_read_b64_tr_b16 a[4:5], v233 offset:28800\n"
  "v_mfma_f32_16x16x32_bf16 v[84:87], a[20:23], a[64:67], v[84:87]\n"
  "ds_read_b64_tr_b16 a[6:7], v233 offset:32384\n"
  "v_mfma_f32_16x16x32_bf16 v[88:91], a[20:23], a[68:71], v[88:91]\n"
  "v_mfma_f32_16x16x32_bf16 v[92:95], a[20:23], a[72:75], v[92:95]\n"
  "s_waitcnt lgkmcnt(4)\n"
  "v_mfma_f32_16x16x32_bf16 v[96:99], a[24:27], a[60:63], v[96:99]\n"
  "ds_read_b64_tr_b16 a[8:9], v233 offset:35968\n"
  "v_mfma_f32_16x16x32_bf16 v[100:103], a[24:27], a[64:67], v[100:103]\n"
  "ds_read_b64_tr_b16 a[10:11], v233 offset:39552\n"
  "v_mfma_f32_16x16x32_bf16 v[104:107], a[24:27], a[68:71], v[104:107]\n"
  "v_mfma_f32_16x16x32_bf16 v[108:111], a[24:27], a[72:75], v[108:111]\n"
  "s_waitcnt lgkmcnt(4)\n"
  "v_mfma_f32_16x16x32_bf16 v[112:115], a[0:3], a[60:63], v[112:115]\n"
  "ds_read_b64_tr_b16 a[12:13], v233 offset:28832\n"
  "v_mfma_f32_16x16x32_bf16 v[116:119], a[0:3], a[64:67], v[116:119]\n"
  "ds_read_b64_tr_b16 a[14:15], v233 offset:32416\n"
  "v_mfma_f32_16x16x32_bf16 v[120:123], a[0:3], a[68:71], v[120:123]\n"
  "v_mfma_f32_16x16x32_bf16 v[124:127], a[0:3], a[72:75], v[124:127]\n"
  "s_waitcnt lgkmcnt(4)\n"
  "v_mfma_f32_16x16x32_bf16 v[128:131], a[4:7], a[60:63], v[128:131]\n"
  "ds_read_b64_tr_b16 a[16:17], v233 offset:36000\n"
  "v_mfma_f32_16x16x32_bf16 v[132:135], a[4:7], a[64:67], v[132:135]\n"
  "ds_read_b64_tr_b16 a[18:19], v233 offset:39584\n"
  "v_mfma_f32_16x16x32_bf16 v[136:139], a[4:7], a[68:71], v[136:139]\n"
  "v_mfma_f32_16x16x32_bf16 v[140:143], a[4:7], a[72:75], v[140:143]\n"
  "s_waitcnt lgkmcnt(4)\n"
  "v_mfma_f32_16x16x32_bf16 v[144:147], a[8:11], a[60:63], v[144:147]\n"
  "ds_read_b64_tr_b16 a[20:21], v233 offset:28864\n"
  "v_mfma_f32_16x16x32_bf16 v[148:151], a[8:11], a[64:67], v[148:151]\n"
  "ds_read_b64_tr_b16 a[22:23], v233 offset:32448\n"
  "v_mfma_f32_16x16x32_bf16 v[152:155], a[8:11], a[68:71], v[152:155]\n"
  "v_mfma_f32_16x16x32_bf16 v[156:159], a[8:11], a[72:75], v[156:159]\n"
  "s_waitcnt lgkmcnt(4)\n"
  "v_mfma_f32_16x16x32_bf16 v[160:163], a[12:15], a[60:63], v[160:163]\n"
  "ds_read_b64_tr_b16 a[24:25], v233 offset:36032\n"
  "v_mfma_f32_16x16x32_bf16 v[164:167], a[12:15], a[64:67], v[164:167]\n"
  "ds_read_b64_tr_b16 a[26:27], v233 offset:39616\n"
  "v_mfma_f32_16x16x32_bf16 v[168:171], a[12:15], a[68:71], v[168:171]\n"
  "s_add_u32 s27, s27, 4096\n"
  "buffer_load_dwordx4 a[156:159], v231, s[52:55], s27 offen offset:0\n"
  "v_mfma_f32_16x16x32_bf16 v[172:175], a[12:15], a[72:75], v[172:175]\n"
  "s_waitcnt lgkmcnt(4)\n"
  "v_mfma_f32_16x16x32_bf16 v[176:179], a[16:19], a[60:63], v[176:179]\n"
  "ds_read_b64_tr_b16 a[0:1], v233 offset:43008\n"
  "v_mfma_f32_16x16x32_bf16 v[180:183], a[16:19], a[64:67], v[180:183]\n"
  "ds_read_b64_tr_b16 a[2:3], v233 offset:46592\n"
  "v_mfma_f32_16x16x32_bf16 v[184:187], a[16:19], a[68:71], v[184:187]\n"
  "v_mfma_f32_16x16x32_bf16 v[188:191], a[16:19], a[72:75], v[188:191]\n"
  "s_waitcnt lgkmcnt(4)\n"
  "v_mfma_f32_16x16x32_bf16 v[192:195], a[20:23], a[60:63], v[192:195]\n"
  "ds_read_b64_tr_b16 a[4:5], v233 offset:50176\n"
  "v_mfma_f32_16x16x32_bf16 v[196:199], a[20:23], a[64:67], v[196:199]\n"
  "ds_read_b64_tr_b16 a[6:7], v233 offset:53760\n"
  "v_mfma_f32_16x16x32_bf16 v[200:203], a[20:23], a[68:71], v[200:203]\n"
  "v_mfma_f32_16x16x32_bf16 v[204:207], a[20:23], a[72:75], v[204:207]\n"
  "s_waitcnt lgkmcnt(4)\n"
  "v_mfma_f32_16x16x32_bf16 v[208:211], a[24:27], a[60:63], v[208:211]\n"
  "ds_read_b64_tr_b16 a[8:9], v233 offset:43040\n"
  "v_mfma_f32_16x16x32_bf16 v[212:215], a[24:27], a[64:67], v[212:215]\n"
  "ds_read_b64_tr_b16 a[10:11], v233 offset:46624\n"
  "v_mfma_f32_16x16x32_bf16 v[216:219], a[24:27], a[68:71], v[216:219]\n"
  "v_mfma_f32_16x16x32_bf16 v[220:223], a[24:27], a[72:75], v[220:223]\n"
  "s_waitcnt vmcnt(35) lgkmcnt(4)\n"
  "v_mfma_f32_16x16x32_bf16 v[0:3], a[0:3], a[76:79], v[0:3]\n"
  "ds_read_b64_tr_b16 a[12:13], v233 offset:50208\n"
  "v_mfma_f32_16x16x32_bf16 v[4:7], a[0:3], a[80:83], v[4:7]\n"
  "ds_read_b64_tr_b16 a[14:15], v233 offset:53792\n"
  "v_mfma_f32_16x16x32_bf16 v[8:11], a[0:3], a[84:87], v[8:11]\n"
  "v_mfma_f32_16x16x32_bf16 v[12:15], a[0:3], a[88:91], v[12:15]\n"
  "s_waitcnt lgkmcnt(4)\n"
  "v_mfma_f32_16x16x32_bf16 v[16:19], a[4:7], a[76:79], v[16:19]\n"
  "ds_read_b64_tr_b16 a[16:17], v233 offset:43072\n"
  "v_mfma_f32_16x16x32_bf16 v[20:23], a[4:7], a[80:83], v[20:23]\n"
  "ds_read_b64_tr_b16 a[18:19], v233 offset:46656\n"
  "v_mfma_f32_16x16x32_bf16 v[24:27], a[4:7], a[84:87], v[24:27]\n"
  "v_mfma_f32_16x16x32_bf16 v[28:31], a[4:7], a[88:91], v[28:31]\n"
  "s_waitcnt lgkmcnt(4)\n"
  "v_mfma_f32_16x16x32_bf16 v[32:35], a[8:11], a[76:79], v[32:35]\n"
  "ds_read_b64_tr_b16 a[20:21], v233 offset:50240\n"
  "v_mfma_f32_16x16x32_bf16 v[36:39], a[8:11], a[80:83], v[36:39]\n"
  "ds_read_b64_tr_b16 a[22:23], v233 offset:53824\n"
  "v_mfma_f32_16x16x32_bf16 v[40:43], a[8:11], a[84:87], v[40:43]\n"
  "buffer_load_dwordx4 a[156:159], v231, s[52:55], s27 offen offset:1024\n"
  "v_mfma_f32_16x16x32_bf16 v[44:47], a[8:11], a[88:91], v[44:47]\n"
  "s_waitcnt lgkmcnt(4)\n"
  "v_mfma_f32_16x16x32_bf16 v[48:51], a[12:15], a[76:79], v[48:51]\n"
  "ds_read_b64_tr_b16 a[24:25], v233 offset:43104\n"
  "v_mfma_f32_16x16x32_bf16 v[52:55], a[12:15], a[80:83], v[52:55]\n"
  "ds_read_b64_tr_b16 a[26:27], v233 offset:46688\n"
  "v_mfma_f32_16x16x32_bf16 v[56:59], a[12:15], a[84:87], v[56:59]\n"
  "v_mfma_f32_16x16x32_bf16 v[60:63], a[12:15], a[88:91], v[60:63]\n"
  "s_waitcnt lgkmcnt(4)\n"
  "v_mfma_f32_16x16x32_bf16 v[64:67], a[16:19], a[76:79], v[64:67]\n"
  "ds_read_b64_tr_b16 a[0:1], v233 offset:50272\n"
  "v_mfma_f32_16x16x32_bf16 v[68:71], a[16:19], a[80:83], v[68:71]\n"
  "ds_read_b64_tr_b16 a[2:3], v233 offset:53856\n"
  "v_mfma_f32_16x16x32_bf16 v[72:75], a[16:19], a[84:87], v[72:75]\n"
  "v_mfma_f32_16x16x32_bf16 v[76:79], a[16:19], a[88:91], v[76:79]\n"
  "s_waitcnt lgkmcnt(4)\n"
  "v_mfma_f32_16x16x32_bf16 v[80:83], a[20:23], a[76:79], v[80:83]\n"
  "ds_read_b64_tr_b16 a[4:5], v233 offset:43136\n"
  "v_mfma_f32_16x16x32_bf16 v[84:87], a[20:23], a[80:83], v[84:87]\n"
  "ds_read_b64_tr_b16 a[6:7], v233 offset:46720\n"
  "v_mfma_f32_16x16x32_bf16 v[88:91], a[20:23], a[84:87], v[88:91]\n"
  "v_mfma_f32_16x16x32_bf16 v[92:95], a[20:23], a[88:91], v[92:95]\n"
  "s_waitcnt lgkmcnt(4)\n"
  "v_mfma_f32_16x16x32_bf16 v[96:99], a[24:27], a[76:79], v[96:99]\n"
  "ds_read_b64_tr_b16 a[8:9], v233 offset:50304\n"
  "v_mfma_f32_16x16x32_bf16 v[100:103], a[24:27], a[80:83], v[100:103]\n"
  "ds_read_b64_tr_b16 a[10:11], v233 offset:53888\n"
  "v_mfma_f32_16x16x32_bf16 v[104:107], a[24:27], a[84:87], v[104:107]\n"
  "v_mfma_f32_16x16x32_bf16 v[108:111], a[24:27], a[88:91], v[108:111]\n"
  "s_waitcnt lgkmcnt(4)\n"
  "v_mfma_f32_16x16x32_bf16 v[112:115], a[0:3], a[76:79], v[112:115]\n"
  "ds_read_b64_tr_b16 a[12:13], v233 offset:43168\n"
  "v_mfma_f32_16x16x32_bf16 v[116:119], a[0:3], a[80:83], v[116:119]\n"
  "ds_read_b64_tr_b16 a[14:15], v233 offset:46752\n"
  "v_mfma_f32_16x16x32_bf16 v[120:123], a[0:3], a[84:87], v[120:123]\n"
  "v_mfma_f32_16x16x32_bf16 v[124:127], a[0:3], a[88:91], v[124:127]\n"
  "s_waitcnt lgkmcnt(4)\n"
  "v_mfma_f32_16x16x32_bf16 v[128:131], a[4:7], a[76:79], v[128:131]\n"
  "ds_read_b64_tr_b16 a[16:17], v233 offset:50336\n"
  "v_mfma_f32_16x16x32_bf16 v[132:135], a[4:7], a[80:83], v[132:135]\n"
  "ds_read_b64_tr_b16 a[18:19], v233 offset:53920\n"
  "v_mfma_f32_16x16x32_bf16 v[136:139], a[4:7], a[84:87], v[136:139]\n"
  "buffer_load_dwordx4 a[156:159], v231, s[52:55], s27 offen offset:2048\n"
  "v_mfma_f32_16x16x32_bf16 v[140:143], a[4:7], a[88:91], v[140:143]\n"
  "s_waitcnt lgkmcnt(4)\n"
  "v_mfma_f32_16x16x32_bf16 v[144:147], a[8:11], a[76:79], v[144:147]\n"
  "ds_read_b64_tr_b16 a[20:21], v233 offset:43200\n"
  "v_mfma_f32_16x16x32_bf16 v[148:151], a[8:11], a[80:83], v[148:151]\n"
  "ds_read_b64_tr_b16 a[22:23], v233 offset:46784\n"
  "v_mfma_f32_16x16x32_bf16 v[152:155], a[8:11], a[84:87], v[152:155]\n"
  "v_mfma_f32_16x16x32_bf16 v[156:159], a[8:11], a[88:91], v[156:159]\n"
  "s_waitcnt lgkmcnt(4)\n"
  "v_mfma_f32_16x16x32_bf16 v[160:163], a[12:15], a[76:79], v[160:163]\n"
  "ds_read_b64_tr_b16 a[24:25], v233 offset:50368\n"
  "v_mfma_f32_16x16x32_bf16 v[164:167], a[12:15], a[80:83], v[164:167]\n"
  "ds_read_b64_tr_b16 a[26:27], v233 offset:53952\n"
  "v_mfma_f32_16x16x32_bf16 v[168:171], a[12:15], a[84:87], v[168:171]\n"
  "v_mfma_f32_16x16x32_bf16 v[172:175], a[12:15], a[88:91], v[172:175]\n"
  "s_waitcnt vmcnt(26)\n"
  "s_barrier\n"
  "s_waitcnt lgkmcnt(4)\n"
  "v_mfma_f32_16x16x32_bf16 v[176:179], a[16:19], a[76:79], v[176:179]\n"
  "ds_read_b64_tr_b16 a[0:1], v234 offset:0\n"
  "v_mfma_f32_16x16x32_bf16 v[180:183], a[16:19], a[80:83], v[180:183]\n"
  "ds_read_b64_tr_b16 a[2:3], v234 offset:3584\n"
  "v_mfma_f32_16x16x32_bf16 v[184:187], a[16:19], a[84:87], v[184:187]\n"
  "v_mfma_f32_16x16x32_bf16 v[188:191], a[16:19], a[88:91], v[188:191]\n"
  "s_waitcnt lgkmcnt(4)\n"
  "v_mfma_f32_16x16x32_bf16 v[192:195], a[20:23], a[76:79], v[192:195]\n"
  "ds_read_b64_tr_b16 a[4:5], v234 offset:7168\n"
  "v_mfma_f32_16x16x32_bf16 v[196:199], a[20:23], a[80:83], v[196:199]\n"
  "ds_read_b64_tr_b16 a[6:7], v234 offset:10752\n"
  "v_mfma_f32_16x16x32_bf16 v[200:203], a[20:23], a[84:87], v[200:203]\n"
  "v_mfma_f32_16x16x32_bf16 v[204:207], a[20:23], a[88:91], v[204:207]\n"
  "s_waitcnt lgkmcnt(4)\n"
  "v_mfma_f32_16x16x32_bf16 v[208:211], a[24:27], a[76:79], v[208:211]\n"
  "ds_read_b64_tr_b16 a[8:9], v234 offset:32\n"
  "v_mfma_f32_16x16x32_bf16 v[212:215], a[24:27], a[80:83], v[212:215]\n"
  "ds_read_b64_tr_b16 a[10:11], v234 offset:3616\n"
  "v_mfma_f32_16x16x32_bf16 v[216:219], a[24:27], a[84:87], v[216:219]\n"
  "v_mfma_f32_16x16x32_bf16 v[220:223], a[24:27], a[88:91], v[220:223]\n"
  "s_waitcnt lgkmcnt(4)\n"
  "v_mfma_f32_16x16x32_bf16 v[0:3], a[0:3], a[92:95], v[0:3]\n"
  "ds_read_b64_tr_b16 a[12:13], v234 offset:7200\n"
  "v_mfma_f32_16x16x32_bf16 v[4:7], a[0:3], a[96:99], v[4:7]\n"
  "ds_read_b64_tr_b16 a[14:15], v234 offset:10784\n"
  "v_mfma_f32_16x16x32_bf16 v[8:11], a[0:3], a[100:103], v[8:11]\n"
  "v_mfma_f32_16x16x32_bf16 v[12:15], a[0:3], a[104:107], v[12:15]\n"
  "s_waitcnt lgkmcnt(4)\n"
  "v_mfma_f32_16x16x32_bf16 v[16:19], a[4:7], a[92:95], v[16:19]\n"
  "ds_read_b64_tr_b16 a[16:17], v234 offset:64\n"
  "v_mfma_f32_16x16x32_bf16 v[20:23], a[4:7], a[96:99], v[20:23]\n"
  "ds_read_b64_tr_b16 a[18:19], v234 offset:3648\n"
  "v_mfma_f32_16x16x32_bf16 v[24:27], a[4:7], a[100:103], v[24:27]\n"
  "v_mfma_f32_16x16x32_bf16 v[28:31], a[4:7], a[104:107], v[28:31]\n"
  "s_waitcnt lgkmcnt(4)\n"
  "v_mfma_f32_16x16x32_bf16 v[32:35], a[8:11], a[92:95], v[32:35]\n"
  "ds_read_b64_tr_b16 a[20:21], v234 offset:7232\n"
  "v_mfma_f32_16x16x32_bf16 v[36:39], a[8:11], a[96:99], v[36:39]\n"
  "ds_read_b64_tr_b16 a[22:23], v234 offset:10816\n"
  "v_mfma_f32_16x16x32_bf16 v[40:43], a[8:11], a[100:103], v[40:43]\n"
  "v_mfma_f32_16x16x32_bf16 v[44:47], a[8:11], a[104:107], v[44:47]\n"
  "s_waitcnt lgkmcnt(4)\n"
  "v_mfma_f32_16x16x32_bf16 v[48:51], a[12:15], a[92:95], v[48:51]\n"
  "ds_read_b64_tr_b16 a[24:25], v234 offset:96\n"
  "v_mfma_f32_16x16x32_bf16 v[52:55], a[12:15], a[96:99], v[52:55]\n"
  "ds_read_b64_tr_b16 a[26:27], v234 offset:3680\n"
  "v_mfma_f32_16x16x32_bf16 v[56:59], a[12:15], a[100:103], v[56:59]\n"
  "v_mfma_f32_16x16x32_bf16 v[60:63], a[12:15], a[104:107], v[60:63]\n"
  "s_waitcnt lgkmcnt(4)\n"
  "v_mfma_f32_16x16x32_bf16 v[64:67], a[16:19], a[92:95], v[64:67]\n"
  "ds_read_b64_tr_b16 a[0:1], v234 offset:7264\n"
  "v_mfma_f32_16x16x32_bf16 v[68:71], a[16:19], a[96:99], v[68:71]\n"
  "ds_read_b64_tr_b16 a[2:3], v234 offset:10848\n"
  "v_mfma_f32_16x16x32_bf16 v[72:75], a[16:19], a[100:103], v[72:75]\n"
  "v_mfma_f32_16x16x32_bf16 v[76:79], a[16:19], a[104:107], v[76:79]\n"
  "s_waitcnt lgkmcnt(4)\n"
  "v_mfma_f32_16x16x32_bf16 v[80:83], a[20:23], a[92:95], v[80:83]\n"
  "ds_read_b64_tr_b16 a[4:5], v234 offset:128\n"
  "v_mfma_f32_16x16x32_bf16 v[84:87], a[20:23], a[96:99], v[84:87]\n"
  "ds_read_b64_tr_b16 a[6:7], v234 offset:3712\n"
  "v_mfma_f32_16x16x32_bf16 v[88:91], a[20:23], a[100:103], v[88:91]\n"
  "v_mfma_f32_16x16x32_bf16 v[92:95], a[20:23], a[104:107], v[92:95]\n"
  "s_waitcnt lgkmcnt(4)\n"
  "v_mfma_f32_16x16x32_bf16 v[96:99], a[24:27], a[92:95], v[96:99]\n"
  "ds_read_b64_tr_b16 a[8:9], v234 offset:7296\n"
  "v_mfma_f32_16x16x32_bf16 v[100:103], a[24:27], a[96:99], v[100:103]\n"
  "ds_read_b64_tr_b16 a[10:11], v234 offset:10880\n"
  "v_mfma_f32_16x16x32_bf16 v[104:107], a[24:27], a[100:103], v[104:107]\n"
  "v_mfma_f32_16x16x32_bf16 v[108:111], a[24:27], a[104:107], v[108:111]\n"
  "s_waitcnt lgkmcnt(4)\n"
  "v_mfma_f32_16x16x32_bf16 v[112:115], a[0:3], a[92:95], v[112:115]\n"
  "ds_read_b64_tr_b16 a[12:13], v234 offset:160\n"
  "v_mfma_f32_16x16x32_bf16 v[116:119], a[0:3], a[96:99], v[116:119]\n"
  "ds_read_b64_tr_b16 a[14:15], v234 offset:3744\n"
  "v_mfma_f32_16x16x32_bf16 v[120:123], a[0:3], a[100:103], v[120:123]\n"
  "v_mfma_f32_16x16x32_bf16 v[124:127], a[0:3], a[104:107], v[124:127]\n"
  "s_waitcnt lgkmcnt(4)\n"
  "v_mfma_f32_16x16x32_bf16 v[128:131], a[4:7], a[92:95], v[128:131]\n"
  "ds_read_b64_tr_b16 a[16:17], v234 offset:7328\n"
  "v_mfma_f32_16x16x32_bf16 v[132:135], a[4:7], a[96:99], v[132:135]\n"
  "ds_read_b64_tr_b16 a[18:19], v234 offset:10912\n"
  "v_mfma_f32_16x16x32_bf16 v[136:139], a[4:7], a[100:103], v[136:139]\n"
  "v_mfma_f32_16x16x32_bf16 v[140:143], a[4:7], a[104:107], v[140:143]\n"
  "s_waitcnt lgkmcnt(4)\n"
  "v_mfma_f32_16x16x32_bf16 v[144:147], a[8:11], a[92:95], v[144:147]\n"
  "ds_read_b64_tr_b16 a[20:21], v234 offset:192\n"
  "v_mfma_f32_16x16x32_bf16 v[148:151], a[8:11], a[96:99], v[148:151]\n"
  "ds_read_b64_tr_b16 a[22:23], v234 offset:3776\n"
  "v_mfma_f32_16x16x32_bf16 v[152:155], a[8:11], a[100:103], v[152:155]\n"
  "v_mfma_f32_16x16x32_bf16 v[156:159], a[8:11], a[104:107], v[156:159]\n"
  "s_waitcnt lgkmcnt(4)\n"
  "v_mfma_f32_16x16x32_bf16 v[160:163], a[12:15], a[92:95], v[160:163]\n"
  "ds_read_b64_tr_b16 a[24:25], v234 offset:7360\n"
  "v_mfma_f32_16x16x32_bf16 v[164:167], a[12:15], a[96:99], v[164:167]\n"
  "ds_read_b64_tr_b16 a[26:27], v234 offset:10944\n"
  "v_mfma_f32_16x16x32_bf16 v[168:171], a[12:15], a[100:103], v[168:171]\n"
  "v_mfma_f32_16x16x32_bf16 v[172:175], a[12:15], a[104:107], v[172:175]\n"
  "s_waitcnt lgkmcnt(4)\n"
  "v_mfma_f32_16x16x32_bf16 v[176:179], a[16:19], a[92:95], v[176:179]\n"
  "ds_read_b64_tr_b16 a[0:1], v234 offset:14336\n"
  "v_mfma_f32_16x16x32_bf16 v[180:183], a[16:19], a[96:99], v[180:183]\n"
  "ds_read_b64_tr_b16 a[2:3], v234 offset:17920\n"
  "v_mfma_f32_16x16x32_bf16 v[184:187], a[16:19], a[100:103], v[184:187]\n"
  "v_mfma_f32_16x16x32_bf16 v[188:191], a[16:19], a[104:107], v[188:191]\n"
  "s_waitcnt lgkmcnt(4)\n"
  "v_mfma_f32_16x16x32_bf16 v[192:195], a[20:23], a[92:95], v[192:195]\n"
  "ds_read_b64_tr_b16 a[4:5], v234 offset:21504\n"
  "v_mfma_f32_16x16x32_bf16 v[196:199], a[20:23], a[96:99], v[196:199]\n"
  "ds_read_b64_tr_b16 a[6:7], v234 offset:25088\n"
  "v_mfma_f32_16x16x32_bf16 v[200:203], a[20:23], a[100:103], v[200:203]\n"
  "v_mfma_f32_16x16x32_bf16 v[204:207], a[20:23], a[104:107], v[204:207]\n"
  "s_waitcnt lgkmcnt(4)\n"
  "v_mfma_f32_16x16x32_bf16 v[208:211], a[24:27], a[92:95], v[208:211]\n"
  "ds_read_b64_tr_b16 a[8:9], v234 offset:14368\n"
  "v_mfma_f32_16x16x32_bf16 v[212:215], a[24:27], a[96:99], v[212:215]\n"
  "ds_read_b64_tr_b16 a[10:11], v234 offset:17952\n"
  "v_mfma_f32_16x16x32_bf16 v[216:219], a[24:27], a[100:103], v[216:219]\n"
  "v_mfma_f32_16x16x32_bf16 v[220:223], a[24:27], a[104:107], v[220:223]\n"
  "s_waitcnt vmcnt(22) lgkmcnt(4)\n"
  "v_mfma_f32_16x16x32_bf16 v[0:3], a[0:3], a[108:111], v[0:3]\n"
  "ds_read_b64_tr_b16 a[12:13], v234 offset:21536\n"
  "v_mfma_f32_16x16x32_bf16 v[4:7], a[0:3], a[112:115], v[4:7]\n"
  "ds_read_b64_tr_b16 a[14:15], v234 offset:25120\n"
  "v_mfma_f32_16x16x32_bf16 v[8:11], a[0:3], a[116:119], v[8:11]\n"
  "v_mfma_f32_16x16x32_bf16 v[12:15], a[0:3], a[120:123], v[12:15]\n"
  "s_waitcnt lgkmcnt(4)\n"
  "v_mfma_f32_16x16x32_bf16 v[16:19], a[4:7], a[108:111], v[16:19]\n"
  "ds_read_b64_tr_b16 a[16:17], v234 offset:14400\n"
  "v_mfma_f32_16x16x32_bf16 v[20:23], a[4:7], a[112:115], v[20:23]\n"
  "ds_read_b64_tr_b16 a[18:19], v234 offset:17984\n"
  "v_mfma_f32_16x16x32_bf16 v[24:27], a[4:7], a[116:119], v[24:27]\n"
  "v_mfma_f32_16x16x32_bf16 v[28:31], a[4:7], a[120:123], v[28:31]\n"
  "s_waitcnt lgkmcnt(4)\n"
  "v_mfma_f32_16x16x32_bf16 v[32:35], a[8:11], a[108:111], v[32:35]\n"
  "ds_read_b64_tr_b16 a[20:21], v234 offset:21568\n"
  "v_mfma_f32_16x16x32_bf16 v[36:39], a[8:11], a[112:115], v[36:39]\n"
  "ds_read_b64_tr_b16 a[22:23], v234 offset:25152\n"
  "v_mfma_f32_16x16x32_bf16 v[40:43], a[8:11], a[116:119], v[40:43]\n"
  "v_mfma_f32_16x16x32_bf16 v[44:47], a[8:11], a[120:123], v[44:47]\n"
  "s_waitcnt lgkmcnt(4)\n"
  "v_mfma_f32_16x16x32_bf16 v[48:51], a[12:15], a[108:111], v[48:51]\n"
  "ds_read_b64_tr_b16 a[24:25], v234 offset:14432\n"
  "v_mfma_f32_16x16x32_bf16 v[52:55], a[12:15], a[112:115], v[52:55]\n"
  "ds_read_b64_tr_b16 a[26:27], v234 offset:18016\n"
  "v_mfma_f32_16x16x32_bf16 v[56:59], a[12:15], a[116:119], v[56:59]\n"
  "v_mfma_f32_16x16x32_bf16 v[60:63], a[12:15], a[120:123], v[60:63]\n"
  "s_waitcnt lgkmcnt(4)\n"
  "v_mfma_f32_16x16x32_bf16 v[64:67], a[16:19], a[108:111], v[64:67]\n"
  "ds_read_b64_tr_b16 a[0:1], v234 offset:21600\n"
  "v_mfma_f32_16x16x32_bf16 v[68:71], a[16:19], a[112:115], v[68:71]\n"
  "ds_read_b64_tr_b16 a[2:3], v234 offset:25184\n"
  "v_mfma_f32_16x16x32_bf16 v[72:75], a[16:19], a[116:119], v[72:75]\n"
  "v_mfma_f32_16x16x32_bf16 v[76:79], a[16:19], a[120:123], v[76:79]\n"
  "s_waitcnt lgkmcnt(4)\n"
  "v_mfma_f32_16x16x32_bf16 v[80:83], a[20:23], a[108:111], v[80:83]\n"
  "ds_read_b64_tr_b16 a[4:5], v234 offset:14464\n"
  "v_mfma_f32_16x16x32_bf16 v[84:87], a[20:23], a[112:115], v[84:87]\n"
  "ds_read_b64_tr_b16 a[6:7], v234 offset:18048\n"
  "v_mfma_f32_16x16x32_bf16 v[88:91], a[20:23], a[116:119], v[88:91]\n"
  "v_mfma_f32_16x16x32_bf16 v[92:95], a[20:23], a[120:123], v[92:95]\n"
  "s_waitcnt lgkmcnt(4)\n"
  "v_mfma_f32_16x16x32_bf16 v[96:99], a[24:27], a[108:111], v[96:99]\n"
  "ds_read_b64_tr_b16 a[8:9], v234 offset:21632\n"
  "v_mfma_f32_16x16x32_bf16 v[100:103], a[24:27], a[112:115], v[100:103]\n"
  "ds_read_b64_tr_b16 a[10:11], v234 offset:25216\n"
  "v_mfma_f32_16x16x32_bf16 v[104:107], a[24:27], a[116:119], v[104:107]\n"
  "v_mfma_f32_16x16x32_bf16 v[108:111], a[24:27], a[120:123], v[108:111]\n"
  "s_waitcnt lgkmcnt(4)\n"
  "v_mfma_f32_16x16x32_bf16 v[112:115], a[0:3], a[108:111], v[112:115]\n"
  "ds_read_b64_tr_b16 a[12:13], v234 offset:14496\n"
  "v_mfma_f32_16x16x32_bf16 v[116:119], a[0:3], a[112:115], v[116:119]\n"
  "ds_read_b64_tr_b16 a[14:15], v234 offset:18080\n"
  "v_mfma_f32_16x16x32_bf16 v[120:123], a[0:3], a[116:119], v[120:123]\n"
  "v_mfma_f32_16x16x32_bf16 v[124:127], a[0:3], a[120:123], v[124:127]\n"
  "s_waitcnt lgkmcnt(4)\n"
  "v_mfma_f32_16x16x32_bf16 v[128:131], a[4:7], a[108:111], v[128:131]\n"
  "ds_read_b64_tr_b16 a[16:17], v234 offset:21664\n"
  "v_mfma_f32_16x16x32_bf16 v[132:135], a[4:7], a[112:115], v[132:135]\n"
  "ds_read_b64_tr_b16 a[18:19], v234 offset:25248\n"
  "v_mfma_f32_16x16x32_bf16 v[136:139], a[4:7], a[116:119], v[136:139]\n"
  "v_mfma_f32_16x16x32_bf16 v[140:143], a[4:7], a[120:123], v[140:143]\n"
  "s_waitcnt lgkmcnt(4)\n"
  "v_mfma_f32_16x16x32_bf16 v[144:147], a[8:11], a[108:111], v[144:147]\n"
  "ds_read_b64_tr_b16 a[20:21], v234 offset:14528\n"
  "v_mfma_f32_16x16x32_bf16 v[148:151], a[8:11], a[112:115], v[148:151]\n"
  "ds_read_b64_tr_b16 a[22:23], v234 offset:18112\n"
  "v_mfma_f32_16x16x32_bf16 v[152:155], a[8:11], a[116:119], v[152:155]\n"
  "v_mfma_f32_16x16x32_bf16 v[156:159], a[8:11], a[120:123], v[156:159]\n"
  "s_waitcnt lgkmcnt(4)\n"
  "v_mfma_f32_16x16x32_bf16 v[160:163], a[12:15], a[108:111], v[160:163]\n"
  "ds_read_b64_tr_b16 a[24:25], v234 offset:21696\n"
  "v_mfma_f32_16x16x32_bf16 v[164:167], a[12:15], a[112:115], v[164:167]\n"
  "ds_read_b64_tr_b16 a[26:27], v234 offset:25280\n"
  "v_mfma_f32_16x16x32_bf16 v[168:171], a[12:15], a[116:119], v[168:171]\n"
  "v_mfma_f32_16x16x32_bf16 v[172:175], a[12:15], a[120:123], v[172:175]\n"
  "s_waitcnt vmcnt(11)\n"
  "s_barrier\n"
  "s_waitcnt lgkmcnt(4)\n"
  "v_mfma_f32_16x16x32_bf16 v[176:179], a[16:19], a[108:111], v[176:179]\n"
  "ds_read_b64_tr_b16 a[0:1], v232 offset:0\n"
  "v_mfma_f32_16x16x32_bf16 v[180:183], a[16:19], a[112:115], v[180:183]\n"
  "ds_read_b64_tr_b16 a[2:3], v232 offset:3584\n"
  "v_mfma_f32_16x16x32_bf16 v[184:187], a[16:19], a[116:119], v[184:187]\n"
  "v_mfma_f32_16x16x32_bf16 v[188:191], a[16:19], a[120:123], v[188:191]\n"
  "s_waitcnt lgkmcnt(4)\n"
  "v_mfma_f32_16x16x32_bf16 v[192:195], a[20:23], a[108:111], v[192:195]\n"
  "ds_read_b64_tr_b16 a[4:5], v232 offset:7168\n"
  "v_mfma_f32_16x16x32_bf16 v[196:199], a[20:23], a[112:115], v[196:199]\n"
  "ds_read_b64_tr_b16 a[6:7], v232 offset:10752\n"
  "v_mfma_f32_16x16x32_bf16 v[200:203], a[20:23], a[116:119], v[200:203]\n"
  "v_mfma_f32_16x16x32_bf16 v[204:207], a[20:23], a[120:123], v[204:207]\n"
  "s_waitcnt lgkmcnt(4)\n"
  "v_mfma_f32_16x16x32_bf16 v[208:211], a[24:27], a[108:111], v[208:211]\n"
  "ds_read_b64_tr_b16 a[8:9], v232 offset:32\n"
  "v_mfma_f32_16x16x32_bf16 v[212:215], a[24:27], a[112:115], v[212:215]\n"
  "ds_read_b64_tr_b16 a[10:11], v232 offset:3616\n"
  "v_mfma_f32_16x16x32_bf16 v[216:219], a[24:27], a[116:119], v[216:219]\n"
  "v_mfma_f32_16x16x32_bf16 v[220:223], a[24:27], a[120:123], v[220:223]\n"
  "s_waitcnt lgkmcnt(4)\n"
  "v_mfma_f32_16x16x32_bf16 v[0:3], a[0:3], a[124:127], v[0:3]\n"
  "ds_read_b64_tr_b16 a[12:13], v232 offset:7200\n"
  "v_mfma_f32_16x16x32_bf16 v[4:7], a[0:3], a[128:131], v[4:7]\n"
  "ds_read_b64_tr_b16 a[14:15], v232 offset:10784\n"
  "v_mfma_f32_16x16x32_bf16 v[8:11], a[0:3], a[132:135], v[8:11]\n"
  "v_mfma_f32_16x16x32_bf16 v[12:15], a[0:3], a[136:139], v[12:15]\n"
  "s_waitcnt lgkmcnt(4)\n"
  "v_mfma_f32_16x16x32_bf16 v[16:19], a[4:7], a[124:127], v[16:19]\n"
  "ds_read_b64_tr_b16 a[16:17], v232 offset:64\n"
  "v_mfma_f32_16x16x32_bf16 v[20:23], a[4:7], a[128:131], v[20:23]\n"
  "ds_read_b64_tr_b16 a[18:19], v232 offset:3648\n"
  "v_mfma_f32_16x16x32_bf16 v[24:27], a[4:7], a[132:135], v[24:27]\n"
  "v_mfma_f32_16x16x32_bf16 v[28:31], a[4:7], a[136:139], v[28:31]\n"
  "s_waitcnt lgkmcnt(4)\n"
  "v_mfma_f32_16x16x32_bf16 v[32:35], a[8:11], a[124:127], v[32:35]\n"
  "ds_read_b64_tr_b16 a[20:21], v232 offset:7232\n"
  "v_mfma_f32_16x16x32_bf16 v[36:39], a[8:11], a[128:131], v[36:39]\n"
  "ds_read_b64_tr_b16 a[22:23], v232 offset:10816\n"
  "v_mfma_f32_16x16x32_bf16 v[40:43], a[8:11], a[132:135], v[40:43]\n"
  "v_mfma_f32_16x16x32_bf16 v[44:47], a[8:11], a[136:139], v[44:47]\n"
  "s_waitcnt lgkmcnt(4)\n"
  "v_mfma_f32_16x16x32_bf16 v[48:51], a[12:15], a[124:127], v[48:51]\n"
  "ds_read_b64_tr_b16 a[24:25], v232 offset:96\n"
  "v_mfma_f32_16x16x32_bf16 v[52:55], a[12:15], a[128:131], v[52:55]\n"
  "ds_read_b64_tr_b16 a[26:27], v232 offset:3680\n"
  "v_mfma_f32_16x16x32_bf16 v[56:59], a[12:15], a[132:135], v[56:59]\n"
  "v_mfma_f32_16x16x32_bf16 v[60:63], a[12:15], a[136:139], v[60:63]\n"
  "s_waitcnt lgkmcnt(4)\n"
  "v_mfma_f32_16x16x32_bf16 v[64:67], a[16:19], a[124:127], v[64:67]\n"
  "ds_read_b64_tr_b16 a[0:1], v232 offset:7264\n"
  "v_mfma_f32_16x16x32_bf16 v[68:71], a[16:19], a[128:131], v[68:71]\n"
  "ds_read_b64_tr_b16 a[2:3], v232 offset:10848\n"
  "v_mfma_f32_16x16x32_bf16 v[72:75], a[16:19], a[132:135], v[72:75]\n"
  "v_mfma_f32_16x16x32_bf16 v[76:79], a[16:19], a[136:139], v[76:79]\n"
  "s_waitcnt lgkmcnt(4)\n"
  "v_mfma_f32_16x16x32_bf16 v[80:83], a[20:23], a[124:127], v[80:83]\n"
  "ds_read_b64_tr_b16 a[4:5], v232 offset:128\n"
  "v_mfma_f32_16x16x32_bf16 v[84:87], a[20:23], a[128:131], v[84:87]\n"
  "ds_read_b64_tr_b16 a[6:7], v232 offset:3712\n"
  "v_mfma_f32_16x16x32_bf16 v[88:91], a[20:23], a[132:135], v[88:91]\n"
  "v_mfma_f32_16x16x32_bf16 v[92:95], a[20:23], a[136:139], v[92:95]\n"
  "s_waitcnt lgkmcnt(4)\n"
  "v_mfma_f32_16x16x32_bf16 v[96:99], a[24:27], a[124:127], v[96:99]\n"
  "ds_read_b64_tr_b16 a[8:9], v232 offset:7296\n"
  "v_mfma_f32_16x16x32_bf16 v[100:103], a[24:27], a[128:131], v[100:103]\n"
  "ds_read_b64_tr_b16 a[10:11], v232 offset:10880\n"
  "v_mfma_f32_16x16x32_bf16 v[104:107], a[24:27], a[132:135], v[104:107]\n"
  "v_mfma_f32_16x16x32_bf16 v[108:111], a[24:27], a[136:139], v[108:111]\n"
  "s_waitcnt lgkmcnt(4)\n"
  "v_mfma_f32_16x16x32_bf16 v[112:115], a[0:3], a[124:127], v[112:115]\n"
  "ds_read_b64_tr_b16 a[12:13], v232 offset:160\n"
  "v_mfma_f32_16x16x32_bf16 v[116:119], a[0:3], a[128:131], v[116:119]\n"
  "ds_read_b64_tr_b16 a[14:15], v232 offset:3744\n"
  "v_mfma_f32_16x16x32_bf16 v[120:123], a[0:3], a[132:135], v[120:123]\n"
  "v_mfma_f32_16x16x32_bf16 v[124:127], a[0:3], a[136:139], v[124:127]\n"
  "s_waitcnt lgkmcnt(4)\n"
  "v_mfma_f32_16x16x32_bf16 v[128:131], a[4:7], a[124:127], v[128:131]\n"
  "ds_read_b64_tr_b16 a[16:17], v232 offset:7328\n"
  "v_mfma_f32_16x16x32_bf16 v[132:135], a[4:7], a[128:131], v[132:135]\n"
  "ds_read_b64_tr_b16 a[18:19], v232 offset:10912\n"
  "v_mfma_f32_16x16x32_bf16 v[136:139], a[4:7], a[132:135], v[136:139]\n"
  "v_mfma_f32_16x16x32_bf16 v[140:143], a[4:7], a[136:139], v[140:143]\n"
  "s_waitcnt lgkmcnt(4)\n"
  "v_mfma_f32_16x16x32_bf16 v[144:147], a[8:11], a[124:127], v[144:147]\n"
  "ds_read_b64_tr_b16 a[20:21], v232 offset:192\n"
  "v_mfma_f32_16x16x32_bf16 v[148:151], a[8:11], a[128:131], v[148:151]\n"
  "ds_read_b64_tr_b16 a[22:23], v232 offset:3776\n"
  "v_mfma_f32_16x16x32_bf16 v[152:155], a[8:11], a[132:135], v[152:155]\n"
  "v_mfma_f32_16x16x32_bf16 v[156:159], a[8:11], a[136:139], v[156:159]\n"
  "s_waitcnt lgkmcnt(4)\n"
  "v_mfma_f32_16x16x32_bf16 v[160:163], a[12:15], a[124:127], v[160:163]\n"
  "ds_read_b64_tr_b16 a[24:25], v232 offset:7360\n"
  "v_mfma_f32_16x16x32_bf16 v[164:167], a[12:15], a[128:131], v[164:167]\n"
  "ds_read_b64_tr_b16 a[26:27], v232 offset:10944\n"
  "v_mfma_f32_16x16x32_bf16 v[168:171], a[12:15], a[132:135], v[168:171]\n"
  "v_mfma_f32_16x16x32_bf16 v[172:175], a[12:15], a[136:139], v[172:175]\n"
  "s_waitcnt lgkmcnt(4)\n"
  "v_mfma_f32_16x16x32_bf16 v[176:179], a[16:19], a[124:127], v[176:179]\n"
  "ds_read_b64_tr_b16 a[0:1], v232 offset:14336\n"
  "v_mfma_f32_16x16x32_bf16 v[180:183], a[16:19], a[128:131], v[180:183]\n"
  "ds_read_b64_tr_b16 a[2:3], v232 offset:17920\n"
  "v_mfma_f32_16x16x32_bf16 v[184:187], a[16:19], a[132:135], v[184:187]\n"
  "v_mfma_f32_16x16x32_bf16 v[188:191], a[16:19], a[136:139], v[188:191]\n"
  "s_waitcnt lgkmcnt(4)\n"
  "v_mfma_f32_16x16x32_bf16 v[192:195], a[20:23], a[124:127], v[192:195]\n"
  "ds_read_b64_tr_b16 a[4:5], v232 offset:21504\n"
  "v_mfma_f32_16x16x32_bf16 v[196:199], a[20:23], a[128:131], v[196:199]\n"
  "ds_read_b64_tr_b16 a[6:7], v232 offset:25088\n"
  "v_mfma_f32_16x16x32_bf16 v[200:203], a[20:23], a[132:135], v[200:203]\n"
  "v_mfma_f32_16x16x32_bf16 v[204:207], a[20:23], a[136:139], v[204:207]\n"
  "s_waitcnt lgkmcnt(4)\n"
  "v_mfma_f32_16x16x32_bf16 v[208:211], a[24:27], a[124:127], v[208:211]\n"
  "ds_read_b64_tr_b16 a[8:9], v232 offset:14368\n"
  "v_mfma_f32_16x16x32_bf16 v[212:215], a[24:27], a[128:131], v[212:215]\n"
  "ds_read_b64_tr_b16 a[10:11], v232 offset:17952\n"
  "v_mfma_f32_16x16x32_bf16 v[216:219], a[24:27], a[132:135], v[216:219]\n"
  "v_mfma_f32_16x16x32_bf16 v[220:223], a[24:27], a[136:139], v[220:223]\n"
  "s_waitcnt vmcnt(7) lgkmcnt(4)\n"
  "v_mfma_f32_16x16x32_bf16 v[0:3], a[0:3], a[140:143], v[0:3]\n"
  "ds_read_b64_tr_b16 a[12:13], v232 offset:21536\n"
  "v_mfma_f32_16x16x32_bf16 v[4:7], a[0:3], a[144:147], v[4:7]\n"
  "ds_read_b64_tr_b16 a[14:15], v232 offset:25120\n"
  "v_mfma_f32_16x16x32_bf16 v[8:11], a[0:3], a[148:151], v[8:11]\n"
  "v_mfma_f32_16x16x32_bf16 v[12:15], a[0:3], a[152:155], v[12:15]\n"
  "s_waitcnt lgkmcnt(4)\n"
  "v_mfma_f32_16x16x32_bf16 v[16:19], a[4:7], a[140:143], v[16:19]\n"
  "ds_read_b64_tr_b16 a[16:17], v232 offset:14400\n"
  "v_mfma_f32_16x16x32_bf16 v[20:23], a[4:7], a[144:147], v[20:23]\n"
  "ds_read_b64_tr_b16 a[18:19], v232 offset:17984\n"
  "v_mfma_f32_16x16x32_bf16 v[24:27], a[4:7], a[148:151], v[24:27]\n"
  "v_mfma_f32_16x16x32_bf16 v[28:31], a[4:7], a[152:155], v[28:31]\n"
  "s_waitcnt lgkmcnt(4)\n"
  "v_mfma_f32_16x16x32_bf16 v[32:35], a[8:11], a[140:143], v[32:35]\n"
  "ds_read_b64_tr_b16 a[20:21], v232 offset:21568\n"
  "v_mfma_f32_16x16x32_bf16 v[36:39], a[8:11], a[144:147], v[36:39]\n"
  "ds_read_b64_tr_b16 a[22:23], v232 offset:25152\n"
  "v_mfma_f32_16x16x32_bf16 v[40:43], a[8:11], a[148:151], v[40:43]\n"
  "v_mfma_f32_16x16x32_bf16 v[44:47], a[8:11], a[152:155], v[44:47]\n"
  "s_waitcnt lgkmcnt(4)\n"
  "v_mfma_f32_16x16x32_bf16 v[48:51], a[12:15], a[140:143], v[48:51]\n"
  "ds_read_b64_tr_b16 a[24:25], v232 offset:14432\n"
  "v_mfma_f32_16x16x32_bf16 v[52:55], a[12:15], a[144:147], v[52:55]\n"
  "ds_read_b64_tr_b16 a[26:27], v232 offset:18016\n"
  "v_mfma_f32_16x16x32_bf16 v[56:59], a[12:15], a[148:151], v[56:59]\n"
  "v_mfma_f32_16x16x32_bf16 v[60:63], a[12:15], a[152:155], v[60:63]\n"
  "s_waitcnt lgkmcnt(4)\n"
  "v_mfma_f32_16x16x32_bf16 v[64:67], a[16:19], a[140:143], v[64:67]\n"
  "ds_read_b64_tr_b16 a[0:1], v232 offset:21600\n"
  "v_mfma_f32_16x16x32_bf16 v[68:71], a[16:19], a[144:147], v[68:71]\n"
  "ds_read_b64_tr_b16 a[2:3], v232 offset:25184\n"
  "v_mfma_f32_16x16x32_bf16 v[72:75], a[16:19], a[148:151], v[72:75]\n"
  "v_mfma_f32_16x16x32_bf16 v[76:79], a[16:19], a[152:155], v[76:79]\n"
  "s_waitcnt lgkmcnt(4)\n"
  "v_mfma_f32_16x16x32_bf16 v[80:83], a[20:23], a[140:143], v[80:83]\n"
  "ds_read_b64_tr_b16 a[4:5], v232 offset:14464\n"
  "v_mfma_f32_16x16x32_bf16 v[84:87], a[20:23], a[144:147], v[84:87]\n"
  "ds_read_b64_tr_b16 a[6:7], v232 offset:18048\n"
  "v_mfma_f32_16x16x32_bf16 v[88:91], a[20:23], a[148:151], v[88:91]\n"
  "v_mfma_f32_16x16x32_bf16 v[92:95], a[20:23], a[152:155], v[92:95]\n"
  "s_waitcnt lgkmcnt(4)\n"
  "v_mfma_f32_16x16x32_bf16 v[96:99], a[24:27], a[140:143], v[96:99]\n"
  "ds_read_b64_tr_b16 a[8:9], v232 offset:21632\n"
  "v_mfma_f32_16x16x32_bf16 v[100:103], a[24:27], a[144:147], v[100:103]\n"
  "ds_read_b64_tr_b16 a[10:11], v232 offset:25216\n"
  "v_mfma_f32_16x16x32_bf16 v[104:107], a[24:27], a[148:151], v[104:107]\n"
  "v_mfma_f32_16x16x32_bf16 v[108:111], a[24:27], a[152:155], v[108:111]\n"
  "s_waitcnt lgkmcnt(4)\n"
  "v_mfma_f32_16x16x32_bf16 v[112:115], a[0:3], a[140:143], v[112:115]\n"
  "ds_read_b64_tr_b16 a[12:13], v232 offset:14496\n"
  "v_mfma_f32_16x16x32_bf16 v[116:119], a[0:3], a[144:147], v[116:119]\n"
  "ds_read_b64_tr_b16 a[14:15], v232 offset:18080\n"
  "v_mfma_f32_16x16x32_bf16 v[120:123], a[0:3], a[148:151], v[120:123]\n"
  "v_mfma_f32_16x16x32_bf16 v[124:127], a[0:3], a[152:155], v[124:127]\n"
  "s_waitcnt lgkmcnt(4)\n"
  "v_mfma_f32_16x16x32_bf16 v[128:131], a[4:7], a[140:143], v[128:131]\n"
  "ds_read_b64_tr_b16 a[16:17], v232 offset:21664\n"
  "v_mfma_f32_16x16x32_bf16 v[132:135], a[4:7], a[144:147], v[132:135]\n"
  "ds_read_b64_tr_b16 a[18:19], v232 offset:25248\n"
  "v_mfma_f32_16x16x32_bf16 v[136:139], a[4:7], a[148:151], v[136:139]\n"
  "v_mfma_f32_16x16x32_bf16 v[140:143], a[4:7], a[152:155], v[140:143]\n"
  "s_waitcnt lgkmcnt(4)\n"
  "v_mfma_f32_16x16x32_bf16 v[144:147], a[8:11], a[140:143], v[144:147]\n"
  "ds_read_b64_tr_b16 a[20:21], v232 offset:14528\n"
  "v_mfma_f32_16x16x32_bf16 v[148:151], a[8:11], a[144:147], v[148:151]\n"
  "ds_read_b64_tr_b16 a[22:23], v232 offset:18112\n"
  "v_mfma_f32_16x16x32_bf16 v[152:155], a[8:11], a[148:151], v[152:155]\n"
  "v_mfma_f32_16x16x32_bf16 v[156:159], a[8:11], a[152:155], v[156:159]\n"
  "s_waitcnt lgkmcnt(4)\n"
  "v_mfma_f32_16x16x32_bf16 v[160:163], a[12:15], a[140:143], v[160:163]\n"
  "ds_read_b64_tr_b16 a[24:25], v232 offset:21696\n"
  "v_mfma_f32_16x16x32_bf16 v[164:167], a[12:15], a[144:147], v[164:167]\n"
  "ds_read_b64_tr_b16 a[26:27], v232 offset:25280\n"
  "v_mfma_f32_16x16x32_bf16 v[168:171], a[12:15], a[148:151], v[168:171]\n"
  "v_mfma_f32_16x16x32_bf16 v[172:175], a[12:15], a[152:155], v[172:175]\n"
  "s_waitcnt lgkmcnt(4)\n"
  "v_mfma_f32_16x16x32_bf16 v[176:179], a[16:19], a[140:143], v[176:179]\n"
  "v_mfma_f32_16x16x32_bf16 v[180:183], a[16:19], a[144:147], v[180:183]\n"
  "v_mfma_f32_16x16x32_bf16 v[184:187], a[16:19], a[148:151], v[184:187]\n"
  "v_mfma_f32_16x16x32_bf16 v[188:191], a[16:19], a[152:155], v[188:191]\n"
  "s_waitcnt lgkmcnt(2)\n"
  "v_mfma_f32_16x16x32_bf16 v[192:195], a[20:23], a[140:143], v[192:195]\n"
  "v_mfma_f32_16x16x32_bf16 v[196:199], a[20:23], a[144:147], v[196:199]\n"
  "v_mfma_f32_16x16x32_bf16 v[200:203], a[20:23], a[148:151], v[200:203]\n"
  "v_mfma_f32_16x16x32_bf16 v[204:207], a[20:23], a[152:155], v[204:207]\n"
  "s_waitcnt lgkmcnt(0)\n"
  "v_mfma_f32_16x16x32_bf16 v[208:211], a[24:27], a[140:143], v[208:211]\n"
  "v_mfma_f32_16x16x32_bf16 v[212:215], a[24:27], a[144:147], v[212:215]\n"
  "v_mfma_f32_16x16x32_bf16 v[216:219], a[24:27], a[148:151], v[216:219]\n"
  "v_mfma_f32_16x16x32_bf16 v[220:223], a[24:27], a[152:155], v[220:223]\n"
  "k3_loop_end:\n"
  "s_barrier\n"
  "s_nop 7\n"
  "v_mov_b32 v236, 0xbdd2d3e8\n"
  "v_mov_b32 v237, 0xbdd2d3e8\n"
  "v_mov_b32 v238, 0xc0135761\n"
  "v_mov_b32 v239, 0xc0135761\n"
  "v_mov_b32 v240, 0x3f800000\n"
  "v_mov_b32 v241, 0x3f800000\n"
  "v_pk_mul_f32 v[242:243], v[0:1], v[0:1]\n"
  "v_pk_mul_f32 v[244:245], v[2:3], v[2:3]\n"
  "v_pk_mul_f32 v[246:247], v[32:33], v[32:33]\n"
  "v_pk_mul_f32 v[248:249], v[34:35], v[34:35]\n"
  "v_pk_fma_f32 v[242:243], v[242:243], v[236:237], v[238:239]\n"
  "v_pk_fma_f32 v[244:245], v[244:245], v[236:237], v[238:239]\n"
  "v_pk_fma_f32 v[246:247], v[246:247], v[236:237], v[238:239]\n"
  "v_pk_fma_f32 v[248:249], v[248:249], v[236:237], v[238:239]\n"
  "v_pk_mul_f32 v[242:243], v[242:243], v[0:1]\n"
  "v_pk_mul_f32 v[244:245], v[244:245], v[2:3]\n"
  "v_pk_mul_f32 v[246:247], v[246:247], v[32:33]\n"
  "v_pk_mul_f32 v[248:249], v[248:249], v[34:35]\n"
  "v_exp_f32 v242, v242\n"
  "v_exp_f32 v243, v243\n"
  "v_exp_f32 v244, v244\n"
  "v_exp_f32 v245, v245\n"
  "v_exp_f32 v246, v246\n"
  "v_exp_f32 v247, v247\n"
  "v_exp_f32 v248, v248\n"
  "v_exp_f32 v249, v249\n"
  "v_pk_mul_f32 v[0:1], v[0:1], v[16:17]\n"
  "v_pk_mul_f32 v[2:3], v[2:3], v[18:19]\n"
  "v_pk_mul_f32 v[32:33], v[32:33], v[48:49]\n"
  "v_pk_mul_f32 v[34:35], v[34:35], v[50:51]\n"
  "v_pk_add_f32 v[242:243], v[242:243], v[240:241]\n"
  "v_pk_add_f32 v[244:245], v[244:245], v[240:241]\n"
  "v_pk_add_f32 v[246:247], v[246:247], v[240:241]\n"
  "v_pk_add_f32 v[248:249], v[248:249], v[240:241]\n"
  "v_rcp_f32 v242, v242\n"
  "v_rcp_f32 v243, v243\n"
  "v_rcp_f32 v244, v244\n"
  "v_rcp_f32 v245, v245\n"
  "v_rcp_f32 v246, v246\n"
  "v_rcp_f32 v247, v247\n"
  "v_rcp_f32 v248, v248\n"
  "v_rcp_f32 v249, v249\n"
  "s_nop 0\n"
  "v_pk_mul_f32 v[242:243], v[242:243], v[0:1]\n"
  "v_pk_mul_f32 v[244:245], v[244:245], v[2:3]\n"
  "v_pk_mul_f32 v[246:247], v[246:247], v[32:33]\n"
  "v_pk_mul_f32 v[248:249], v[248:249], v[34:35]\n"
  "v_cvt_pk_bf16_f32 v16, v242, v243\n"
  "v_cvt_pk_bf16_f32 v17, v244, v245\n"
  "v_cvt_pk_bf16_f32 v18, v246, v247\n"
  "v_cvt_pk_bf16_f32 v19, v248, v249\n"
  "buffer_store_dwordx4 v[16:19], v231, s[16:19], s25 offen offset:0 sc1\n"
  "v_pk_mul_f32 v[242:243], v[4:5], v[4:5]\n"
  "v_pk_mul_f32 v[244:245], v[6:7], v[6:7]\n"
  "v_pk_mul_f32 v[246:247], v[36:37], v[36:37]\n"
  "v_pk_mul_f32 v[248:249], v[38:39], v[38:39]\n"
  "v_pk_fma_f32 v[242:243], v[242:243], v[236:237], v[238:239]\n"
  "v_pk_fma_f32 v[244:245], v[244:245], v[236:237], v[238:239]\n"
  "v_pk_fma_f32 v[246:247], v[246:247], v[236:237], v[238:239]\n"
  "v_pk_fma_f32 v[248:249], v[248:249], v[236:237], v[238:239]\n"
  "v_pk_mul_f32 v[242:243], v[242:243], v[4:5]\n"
  "v_pk_mul_f32 v[244:245], v[244:245], v[6:7]\n"
  "v_pk_mul_f32 v[246:247], v[246:247], v[36:37]\n"
  "v_pk_mul_f32 v[248:249], v[248:249], v[38:39]\n"
  "v_exp_f32 v242, v242\n"
  "v_exp_f32 v243, v243\n"
  "v_exp_f32 v244, v244\n"
  "v_exp_f32 v245, v245\n"
  "v_exp_f32 v246, v246\n"
  "v_exp_f32 v247, v247\n"
  "v_exp_f32 v248, v248\n"
  "v_exp_f32 v249, v249\n"
  "v_pk_mul_f32 v[4:5], v[4:5], v[20:21]\n"
  "v_pk_mul_f32 v[6:7], v[6:7], v[22:23]\n"
  "v_pk_mul_f32 v[36:37], v[36:37], v[52:53]\n"
  "v_pk_mul_f32 v[38:39], v[38:39], v[54:55]\n"
  "v_pk_add_f32 v[242:243], v[242:243], v[240:241]\n"
  "v_pk_add_f32 v[244:245], v[244:245], v[240:241]\n"
  "v_pk_add_f32 v[246:247], v[246:247], v[240:241]\n"
  "v_pk_add_f32 v[248:249], v[248:249], v[240:241]\n"
  "v_rcp_f32 v242, v242\n"
  "v_rcp_f32 v243, v243\n"
  "v_rcp_f32 v244, v244\n"
  "v_rcp_f32 v245, v245\n"
  "v_rcp_f32 v246, v246\n"
  "v_rcp_f32 v247, v247\n"
  "v_rcp_f32 v248, v248\n"
  "v_rcp_f32 v249, v249\n"
  "s_nop 0\n"
  "v_pk_mul_f32 v[242:243], v[242:243], v[4:5]\n"
  "v_pk_mul_f32 v[244:245], v[244:245], v[6:7]\n"
  "v_pk_mul_f32 v[246:247], v[246:247], v[36:37]\n"
  "v_pk_mul_f32 v[248:249], v[248:249], v[38:39]\n"
  "v_cvt_pk_bf16_f32 v20, v242, v243\n"
  "v_cvt_pk_bf16_f32 v21, v244, v245\n"
  "v_cvt_pk_bf16_f32 v22, v246, v247\n"
  "v_cvt_pk_bf16_f32 v23, v248, v249\n"
  "buffer_store_dwordx4 v[20:23], v231, s[16:19], s25 offen offset:1024 sc1\n"
  "v_pk_mul_f32 v[242:243], v[8:9], v[8:9]\n"
  "v_pk_mul_f32 v[244:245], v[10:11], v[10:11]\n"
  "v_pk_mul_f32 v[246:247], v[40:41], v[40:41]\n"
  "v_pk_mul_f32 v[248:249], v[42:43], v[42:43]\n"
  "v_pk_fma_f32 v[242:243], v[242:243], v[236:237], v[238:239]\n"
  "v_pk_fma_f32 v[244:245], v[244:245], v[236:237], v[238:239]\n"
  "v_pk_fma_f32 v[246:247], v[246:247], v[236:237], v[238:239]\n"
  "v_pk_fma_f32 v[248:249], v[248:249], v[236:237], v[238:239]\n"
  "v_pk_mul_f32 v[242:243], v[242:243], v[8:9]\n"
  "v_pk_mul_f32 v[244:245], v[244:245], v[10:11]\n"
  "v_pk_mul_f32 v[246:247], v[246:247], v[40:41]\n"
  "v_pk_mul_f32 v[248:249], v[248:249], v[42:43]\n"
  "v_exp_f32 v242, v242\n"
  "v_exp_f32 v243, v243\n"
  "v_exp_f32 v244, v244\n"
  "v_exp_f32 v245, v245\n"
  "v_exp_f32 v246, v246\n"
  "v_exp_f32 v247, v247\n"
  "v_exp_f32 v248, v248\n"
  "v_exp_f32 v249, v249\n"
  "v_pk_mul_f32 v[8:9], v[8:9], v[24:25]\n"
  "v_pk_mul_f32 v[10:11], v[10:11], v[26:27]\n"
  "v_pk_mul_f32 v[40:41], v[40:41], v[56:57]\n"
  "v_pk_mul_f32 v[42:43], v[42:43], v[58:59]\n"
  "v_pk_add_f32 v[242:243], v[242:243], v[240:241]\n"
  "v_pk_add_f32 v[244:245], v[244:245], v[240:241]\n"
  "v_pk_add_f32 v[246:247], v[246:247], v[240:241]\n"
  "v_pk_add_f32 v[248:249], v[248:249], v[240:241]\n"
  "v_rcp_f32 v242, v242\n"
  "v_rcp_f32 v243, v243\n"
  "v_rcp_f32 v244, v244\n"
  "v_rcp_f32 v245, v245\n"
  "v_rcp_f32 v246, v246\n"
  "v_rcp_f32 v247, v247\n"
  "v_rcp_f32 v248, v248\n"
  "v_rcp_f32 v249, v249\n"
  "s_nop 0\n"
  "v_pk_mul_f32 v[242:243], v[242:243], v[8:9]\n"
  "v_pk_mul_f32 v[244:245], v[244:245], v[10:11]\n"
  "v_pk_mul_f32 v[246:247], v[246:247], v[40:41]\n"
  "v_pk_mul_f32 v[248:249], v[248:249], v[42:43]\n"
  "v_cvt_pk_bf16_f32 v24, v242, v243\n"
  "v_cvt_pk_bf16_f32 v25, v244, v245\n"
  "v_cvt_pk_bf16_f32 v26, v246, v247\n"
  "v_cvt_pk_bf16_f32 v27, v248, v249\n"
  "buffer_store_dwordx4 v[24:27], v231, s[16:19], s25 offen offset:2048 sc1\n"
  "v_pk_mul_f32 v[242:243], v[12:13], v[12:13]\n"
  "v_pk_mul_f32 v[244:245], v[14:15], v[14:15]\n"
  "v_pk_mul_f32 v[246:247], v[44:45], v[44:45]\n"
  "v_pk_mul_f32 v[248:249], v[46:47], v[46:47]\n"
  "v_pk_fma_f32 v[242:243], v[242:243], v[236:237], v[238:239]\n"
  "v_pk_fma_f32 v[244:245], v[244:245], v[236:237], v[238:239]\n"
  "v_pk_fma_f32 v[246:247], v[246:247], v[236:237], v[238:239]\n"
  "v_pk_fma_f32 v[248:249], v[248:249], v[236:237], v[238:239]\n"
  "v_pk_mul_f32 v[242:243], v[242:243], v[12:13]\n"
  "v_pk_mul_f32 v[244:245], v[244:245], v[14:15]\n"
  "v_pk_mul_f32 v[246:247], v[246:247], v[44:45]\n"
  "v_pk_mul_f32 v[248:249], v[248:249], v[46:47]\n"
  "v_exp_f32 v242, v242\n"
  "v_exp_f32 v243, v243\n"
  "v_exp_f32 v244, v244\n"
  "v_exp_f32 v245, v245\n"
  "v_exp_f32 v246, v246\n"
  "v_exp_f32 v247, v247\n"
  "v_exp_f32 v248, v248\n"
  "v_exp_f32 v249, v249\n"
  "v_pk_mul_f32 v[12:13], v[12:13], v[28:29]\n"
  "v_pk_mul_f32 v[14:15], v[14:15], v[30:31]\n"
  "v_pk_mul_f32 v[44:45], v[44:45], v[60:61]\n"
  "v_pk_mul_f32 v[46:47], v[46:47], v[62:63]\n"
  "v_pk_add_f32 v[242:243], v[242:243], v[240:241]\n"
  "v_pk_add_f32 v[244:245], v[244:245], v[240:241]\n"
  "v_pk_add_f32 v[246:247], v[246:247], v[240:241]\n"
  "v_pk_add_f32 v[248:249], v[248:249], v[240:241]\n"
  "v_rcp_f32 v242, v242\n"
  "v_rcp_f32 v243, v243\n"
  "v_rcp_f32 v244, v244\n"
  "v_rcp_f32 v245, v245\n"
  "v_rcp_f32 v246, v246\n"
  "v_rcp_f32 v247, v247\n"
  "v_rcp_f32 v248, v248\n"
  "v_rcp_f32 v249, v249\n"
  "s_nop 0\n"
  "v_pk_mul_f32 v[242:243], v[242:243], v[12:13]\n"
  "v_pk_mul_f32 v[244:245], v[244:245], v[14:15]\n"
  "v_pk_mul_f32 v[246:247], v[246:247], v[44:45]\n"
  "v_pk_mul_f32 v[248:249], v[248:249], v[46:47]\n"
  "v_cvt_pk_bf16_f32 v28, v242, v243\n"
  "v_cvt_pk_bf16_f32 v29, v244, v245\n"
  "v_cvt_pk_bf16_f32 v30, v246, v247\n"
  "v_cvt_pk_bf16_f32 v31, v248, v249\n"
  "buffer_store_dwordx4 v[28:31], v231, s[16:19], s25 offen offset:3072 sc1\n"
  "v_pk_mul_f32 v[242:243], v[64:65], v[64:65]\n"
  "v_pk_mul_f32 v[244:245], v[66:67], v[66:67]\n"
  "v_pk_mul_f32 v[246:247], v[96:97], v[96:97]\n"
  "v_pk_mul_f32 v[248:249], v[98:99], v[98:99]\n"
  "v_pk_fma_f32 v[242:243], v[242:243], v[236:237], v[238:239]\n"
  "v_pk_fma_f32 v[244:245], v[244:245], v[236:237], v[238:239]\n"
  "v_pk_fma_f32 v[246:247], v[246:247], v[236:237], v[238:239]\n"
  "v_pk_fma_f32 v[248:249], v[248:249], v[236:237], v[238:239]\n"
  "v_pk_mul_f32 v[242:243], v[242:243], v[64:65]\n"
  "v_pk_mul_f32 v[244:245], v[244:245], v[66:67]\n"
  "v_pk_mul_f32 v[246:247], v[246:247], v[96:97]\n"
  "v_pk_mul_f32 v[248:249], v[248:249], v[98:99]\n"
  "v_exp_f32 v242, v242\n"
  "v_exp_f32 v243, v243\n"
  "v_exp_f32 v244, v244\n"
  "v_exp_f32 v245, v245\n"
  "v_exp_f32 v246, v246\n"
  "v_exp_f32 v247, v247\n"
  "v_exp_f32 v248, v248\n"
  "v_exp_f32 v249, v249\n"
  "v_pk_mul_f32 v[64:65], v[64:65], v[80:81]\n"
  "v_pk_mul_f32 v[66:67], v[66:67], v[82:83]\n"
  "v_pk_mul_f32 v[96:97], v[96:97], v[112:113]\n"
  "v_pk_mul_f32 v[98:99], v[98:99], v[114:115]\n"
  "v_pk_add_f32 v[242:243], v[242:243], v[240:241]\n"
  "v_pk_add_f32 v[244:245], v[244:245], v[240:241]\n"
  "v_pk_add_f32 v[246:247], v[246:247], v[240:241]\n"
  "v_pk_add_f32 v[248:249], v[248:249], v[240:241]\n"
  "v_rcp_f32 v242, v242\n"
  "v_rcp_f32 v243, v243\n"
  "v_rcp_f32 v244, v244\n"
  "v_rcp_f32 v245, v245\n"
  "v_rcp_f32 v246, v246\n"
  "v_rcp_f32 v247, v247\n"
  "v_rcp_f32 v248, v248\n"
  "v_rcp_f32 v249, v249\n"
  "s_nop 0\n"
  "v_pk_mul_f32 v[242:243], v[242:243], v[64:65]\n"
  "v_pk_mul_f32 v[244:245], v[244:245], v[66:67]\n"
  "v_pk_mul_f32 v[246:247], v[246:247], v[96:97]\n"
  "v_pk_mul_f32 v[248:249], v[248:249], v[98:99]\n"
  "v_cvt_pk_bf16_f32 v80, v242, v243\n"
  "v_cvt_pk_bf16_f32 v81, v244, v245\n"
  "v_cvt_pk_bf16_f32 v82, v246, v247\n"
  "v_cvt_pk_bf16_f32 v83, v248, v249\n"
  "s_add_u32 s25, s25, 4096\n"
  "buffer_store_dwordx4 v[80:83], v231, s[16:19], s25 offen offset:0 sc1\n"
  "v_pk_mul_f32 v[242:243], v[68:69], v[68:69]\n"
  "v_pk_mul_f32 v[244:245], v[70:71], v[70:71]\n"
  "v_pk_mul_f32 v[246:247], v[100:101], v[100:101]\n"
  "v_pk_mul_f32 v[248:249], v[102:103], v[102:103]\n"
  "v_pk_fma_f32 v[242:243], v[242:243], v[236:237], v[238:239]\n"
  "v_pk_fma_f32 v[244:245], v[244:245], v[236:237], v[238:239]\n"
  "v_pk_fma_f32 v[246:247], v[246:247], v[236:237], v[238:239]\n"
  "v_pk_fma_f32 v[248:249], v[248:249], v[236:237], v[238:239]\n"
  "v_pk_mul_f32 v[242:243], v[242:243], v[68:69]\n"
  "v_pk_mul_f32 v[244:245], v[244:245], v[70:71]\n"
  "v_pk_mul_f32 v[246:247], v[246:247], v[100:101]\n"
  "v_pk_mul_f32 v[248:249], v[248:249], v[102:103]\n"
  "v_exp_f32 v242, v242\n"
  "v_exp_f32 v243, v243\n"
  "v_exp_f32 v244, v244\n"
  "v_exp_f32 v245, v245\n"
  "v_exp_f32 v246, v246\n"
  "v_exp_f32 v247, v247\n"
  "v_exp_f32 v248, v248\n"
  "v_exp_f32 v249, v249\n"
  "v_pk_mul_f32 v[68:69], v[68:69], v[84:85]\n"
  "v_pk_mul_f32 v[70:71], v[70:71], v[86:87]\n"
  "v_pk_mul_f32 v[100:101], v[100:101], v[116:117]\n"
  "v_pk_mul_f32 v[102:103], v[102:103], v[118:119]\n"
  "v_pk_add_f32 v[242:243], v[242:243], v[240:241]\n"
  "v_pk_add_f32 v[244:245], v[244:245], v[240:241]\n"
  "v_pk_add_f32 v[246:247], v[246:247], v[240:241]\n"
  "v_pk_add_f32 v[248:249], v[248:249], v[240:241]\n"
  "v_rcp_f32 v242, v242\n"
  "v_rcp_f32 v243, v243\n"
  "v_rcp_f32 v244, v244\n"
  "v_rcp_f32 v245, v245\n"
  "v_rcp_f32 v246, v246\n"
  "v_rcp_f32 v247, v247\n"
  "v_rcp_f32 v248, v248\n"
  "v_rcp_f32 v249, v249\n"
  "s_nop 0\n"
  "v_pk_mul_f32 v[242:243], v[242:243], v[68:69]\n"
  "v_pk_mul_f32 v[244:245], v[244:245], v[70:71]\n"
  "v_pk_mul_f32 v[246:247], v[246:247], v[100:101]\n"
  "v_pk_mul_f32 v[248:249], v[248:249], v[102:103]\n"
  "v_cvt_pk_bf16_f32 v84, v242, v243\n"
  "v_cvt_pk_bf16_f32 v85, v244, v245\n"
  "v_cvt_pk_bf16_f32 v86, v246, v247\n"
  "v_cvt_pk_bf16_f32 v87, v248, v249\n"
  "buffer_store_dwordx4 v[84:87], v231, s[16:19], s25 offen offset:1024 sc1\n"
  "v_pk_mul_f32 v[242:243], v[72:73], v[72:73]\n"
  "v_pk_mul_f32 v[244:245], v[74:75], v[74:75]\n"
  "v_pk_mul_f32 v[246:247], v[104:105], v[104:105]\n"
  "v_pk_mul_f32 v[248:249], v[106:107], v[106:107]\n"
  "v_pk_fma_f32 v[242:243], v[242:243], v[236:237], v[238:239]\n"
  "v_pk_fma_f32 v[244:245], v[244:245], v[236:237], v[238:239]\n"
  "v_pk_fma_f32 v[246:247], v[246:247], v[236:237], v[238:239]\n"
  "v_pk_fma_f32 v[248:249], v[248:249], v[236:237], v[238:239]\n"
  "v_pk_mul_f32 v[242:243], v[242:243], v[72:73]\n"
  "v_pk_mul_f32 v[244:245], v[244:245], v[74:75]\n"
  "v_pk_mul_f32 v[246:247], v[246:247], v[104:105]\n"
  "v_pk_mul_f32 v[248:249], v[248:249], v[106:107]\n"
  "v_exp_f32 v242, v242\n"
  "v_exp_f32 v243, v243\n"
  "v_exp_f32 v244, v244\n"
  "v_exp_f32 v245, v245\n"
  "v_exp_f32 v246, v246\n"
  "v_exp_f32 v247, v247\n"
  "v_exp_f32 v248, v248\n"
  "v_exp_f32 v249, v249\n"
  "v_pk_mul_f32 v[72:73], v[72:73], v[88:89]\n"
  "v_pk_mul_f32 v[74:75], v[74:75], v[90:91]\n"
  "v_pk_mul_f32 v[104:105], v[104:105], v[120:121]\n"
  "v_pk_mul_f32 v[106:107], v[106:107], v[122:123]\n"
  "v_pk_add_f32 v[242:243], v[242:243], v[240:241]\n"
  "v_pk_add_f32 v[244:245], v[244:245], v[240:241]\n"
  "v_pk_add_f32 v[246:247], v[246:247], v[240:241]\n"
  "v_pk_add_f32 v[248:249], v[248:249], v[240:241]\n"
  "v_rcp_f32 v242, v242\n"
  "v_rcp_f32 v243, v243\n"
  "v_rcp_f32 v244, v244\n"
  "v_rcp_f32 v245, v245\n"
  "v_rcp_f32 v246, v246\n"
  "v_rcp_f32 v247, v247\n"
  "v_rcp_f32 v248, v248\n"
  "v_rcp_f32 v249, v249\n"
  "s_nop 0\n"
  "v_pk_mul_f32 v[242:243], v[242:243], v[72:73]\n"
  "v_pk_mul_f32 v[244:245], v[244:245], v[74:75]\n"
  "v_pk_mul_f32 v[246:247], v[246:247], v[104:105]\n"
  "v_pk_mul_f32 v[248:249], v[248:249], v[106:107]\n"
  "v_cvt_pk_bf16_f32 v88, v242, v243\n"
  "v_cvt_pk_bf16_f32 v89, v244, v245\n"
  "v_cvt_pk_bf16_f32 v90, v246, v247\n"
  "v_cvt_pk_bf16_f32 v91, v248, v249\n"
  "buffer_store_dwordx4 v[88:91], v231, s[16:19], s25 offen offset:2048 sc1\n"
  "s_waitcnt vmcnt(3)\n"
  "s_mov_b64 exec, 15\n"
  "global_store_dword v253, v240, s[70:71] sc1\n"
  "s_mov_b64 exec, -1\n"
  "v_pk_mul_f32 v[242:243], v[76:77], v[76:77]\n"
  "v_pk_mul_f32 v[244:245], v[78:79], v[78:79]\n"
  "v_pk_mul_f32 v[246:247], v[108:109], v[108:109]\n"
  "v_pk_mul_f32 v[248:249], v[110:111], v[110:111]\n"
  "v_pk_fma_f32 v[242:243], v[242:243], v[236:237], v[238:239]\n"
  "v_pk_fma_f32 v[244:245], v[244:245], v[236:237], v[238:239]\n"
  "v_pk_fma_f32 v[246:247], v[246:247], v[236:237], v[238:239]\n"
  "v_pk_fma_f32 v[248:249], v[248:249], v[236:237], v[238:239]\n"
  "v_pk_mul_f32 v[242:243], v[242:243], v[76:77]\n"
  "v_pk_mul_f32 v[244:245], v[244:245], v[78:79]\n"
  "v_pk_mul_f32 v[246:247], v[246:247], v[108:109]\n"
  "v_pk_mul_f32 v[248:249], v[248:249], v[110:111]\n"
  "v_exp_f32 v242, v242\n"
  "v_exp_f32 v243, v243\n"
  "v_exp_f32 v244, v244\n"
  "v_exp_f32 v245, v245\n"
  "v_exp_f32 v246, v246\n"
  "v_exp_f32 v247, v247\n"
  "v_exp_f32 v248, v248\n"
  "v_exp_f32 v249, v249\n"
  "v_pk_mul_f32 v[76:77], v[76:77], v[92:93]\n"
  "v_pk_mul_f32 v[78:79], v[78:79], v[94:95]\n"
  "v_pk_mul_f32 v[108:109], v[108:109], v[124:125]\n"
  "v_pk_mul_f32 v[110:111], v[110:111], v[126:127]\n"
  "v_pk_add_f32 v[242:243], v[242:243], v[240:241]\n"
  "v_pk_add_f32 v[244:245], v[244:245], v[240:241]\n"
  "v_pk_add_f32 v[246:247], v[246:247], v[240:241]\n"
  "v_pk_add_f32 v[248:249], v[248:249], v[240:241]\n"
  "v_rcp_f32 v242, v242\n"
  "v_rcp_f32 v243, v243\n"
  "v_rcp_f32 v244, v244\n"
  "v_rcp_f32 v245, v245\n"
  "v_rcp_f32 v246, v246\n"
  "v_rcp_f32 v247, v247\n"
  "v_rcp_f32 v248, v248\n"
  "v_rcp_f32 v249, v249\n"
  "s_nop 0\n"
  "v_pk_mul_f32 v[242:243], v[242:243], v[76:77]\n"
  "v_pk_mul_f32 v[244:245], v[244:245], v[78:79]\n"
  "v_pk_mul_f32 v[246:247], v[246:247], v[108:109]\n"
  "v_pk_mul_f32 v[248:249], v[248:249], v[110:111]\n"
  "v_cvt_pk_bf16_f32 v92, v242, v243\n"
  "v_cvt_pk_bf16_f32 v93, v244, v245\n"
  "v_cvt_pk_bf16_f32 v94, v246, v247\n"
  "v_cvt_pk_bf16_f32 v95, v248, v249\n"
  "buffer_store_dwordx4 v[92:95], v231, s[16:19], s25 offen offset:3072 sc1\n"
  "v_pk_mul_f32 v[242:243], v[128:129], v[128:129]\n"
  "v_pk_mul_f32 v[244:245], v[130:131], v[130:131]\n"
  "v_pk_mul_f32 v[246:247], v[160:161], v[160:161]\n"
  "v_pk_mul_f32 v[248:249], v[162:163], v[162:163]\n"
  "v_pk_fma_f32 v[242:243], v[242:243], v[236:237], v[238:239]\n"
  "v_pk_fma_f32 v[244:245], v[244:245], v[236:237], v[238:239]\n"
  "v_pk_fma_f32 v[246:247], v[246:247], v[236:237], v[238:239]\n"
  "v_pk_fma_f32 v[248:249], v[248:249], v[236:237], v[238:239]\n"
  "v_pk_mul_f32 v[242:243], v[242:243], v[128:129]\n"
  "v_pk_mul_f32 v[244:245], v[244:245], v[130:131]\n"
  "v_pk_mul_f32 v[246:247], v[246:247], v[160:161]\n"
  "v_pk_mul_f32 v[248:249], v[248:249], v[162:163]\n"
  "v_exp_f32 v242, v242\n"
  "s_add_u32 s20, s61, 0x0\n"
  "s_add_u32 m0, s60, 0\n"
  "v_exp_f32 v243, v243\n"
  "s_nop 0\n"
  "buffer_load_dwordx4 v251, s[52:55], s20 offen lds\n"
  "v_exp_f32 v244, v244\n"
  "s_add_u32 s20, s61, 0x40000\n"
  "s_add_u32 m0, s60, 4096\n"
  "v_exp_f32 v245, v245\n"
  "s_nop 0\n"
  "buffer_load_dwordx4 v251, s[52:55], s20 offen lds\n"
  "v_exp_f32 v246, v246\n"
  "s_add_u32 s20, s61, 0x70000\n"
  "s_add_u32 m0, s60, 8192\n"
  "v_exp_f32 v247, v247\n"
  "s_nop 0\n"
  "buffer_load_dwordx4 v251, s[52:55], s20 offen lds\n"
  "v_exp_f32 v248, v248\n"
  "s_add_u32 s20, s61, 0xb0000\n"
  "s_add_u32 m0, s60, 12288\n"
  "v_exp_f32 v249, v249\n"
  "s_nop 0\n"
  "buffer_load_dwordx4 v251, s[52:55], s20 offen lds\n"
  "v_pk_mul_f32 v[128:129], v[128:129], v[144:145]\n"
  "v_pk_mul_f32 v[130:131], v[130:131], v[146:147]\n"
  "v_pk_mul_f32 v[160:161], v[160:161], v[176:177]\n"
  "v_pk_mul_f32 v[162:163], v[162:163], v[178:179]\n"
  "v_pk_add_f32 v[242:243], v[242:243], v[240:241]\n"
  "v_pk_add_f32 v[244:245], v[244:245], v[240:241]\n"
  "v_pk_add_f32 v[246:247], v[246:247], v[240:241]\n"
  "v_pk_add_f32 v[248:249], v[248:249], v[240:241]\n"
  "v_rcp_f32 v242, v242\n"
  "s_sub_u32 s20, 0xe0000, s61\n"
  "s_add_u32 m0, s60, 16384\n"
  "v_rcp_f32 v243, v243\n"
  "s_nop 0\n"
  "buffer_load_dwordx4 v251, s[52:55], s20 offen lds\n"
  "v_rcp_f32 v244, v244\n"
  "s_sub_u32 s20, 0x120000, s61\n"
  "s_add_u32 m0, s60, 20480\n"
  "v_rcp_f32 v245, v245\n"
  "s_nop 0\n"
  "buffer_load_dwordx4 v251, s[52:55], s20 offen lds\n"
  "v_rcp_f32 v246, v246\n"
  "s_sub_u32 s20, 0x150000, s61\n"
  "s_add_u32 m0, s60, 24576\n"
  "v_rcp_f32 v247, v247\n"
  "s_nop 0\n"
  "buffer_load_dwordx4 v251, s[52:55], s20 offen lds\n"
  "v_rcp_f32 v248, v248\n"
  "s_sub_u32 s20, 0x190000, s61\n"
  "s_add_u32 m0, s60, 28672\n"
  "v_rcp_f32 v249, v249\n"
  "s_nop 0\n"
  "buffer_load_dwordx4 v251, s[52:55], s20 offen lds\n"
  "s_nop 0\n"
  "v_pk_mul_f32 v[242:243], v[242:243], v[128:129]\n"
  "v_pk_mul_f32 v[244:245], v[244:245], v[130:131]\n"
  "v_pk_mul_f32 v[246:247], v[246:247], v[160:161]\n"
  "v_pk_mul_f32 v[248:249], v[248:249], v[162:163]\n"
  "v_cvt_pk_bf16_f32 v144, v242, v243\n"
  "v_cvt_pk_bf16_f32 v145, v244, v245\n"
  "v_cvt_pk_bf16_f32 v146, v246, v247\n"
  "v_cvt_pk_bf16_f32 v147, v248, v249\n"
  "v_pk_mul_f32 v[242:243], v[132:133], v[132:133]\n"
  "v_pk_mul_f32 v[244:245], v[134:135], v[134:135]\n"
  "v_pk_mul_f32 v[246:247], v[164:165], v[164:165]\n"
  "v_pk_mul_f32 v[248:249], v[166:167], v[166:167]\n"
  "v_pk_fma_f32 v[242:243], v[242:243], v[236:237], v[238:239]\n"
  "v_pk_fma_f32 v[244:245], v[244:245], v[236:237], v[238:239]\n"
  "v_pk_fma_f32 v[246:247], v[246:247], v[236:237], v[238:239]\n"
  "v_pk_fma_f32 v[248:249], v[248:249], v[236:237], v[238:239]\n"
  "v_pk_mul_f32 v[242:243], v[242:243], v[132:133]\n"
  "v_pk_mul_f32 v[244:245], v[244:245], v[134:135]\n"
  "v_pk_mul_f32 v[246:247], v[246:247], v[164:165]\n"
  "v_pk_mul_f32 v[248:249], v[248:249], v[166:167]\n"
  "v_exp_f32 v242, v242\n"
  "s_add_u32 s20, s61, 0x1c0000\n"
  "s_add_u32 m0, s60, 32768\n"
  "v_exp_f32 v243, v243\n"
  "s_nop 0\n"
  "buffer_load_dwordx4 v251, s[52:55], s20 offen lds\n"
  "v_exp_f32 v244, v244\n"
  "s_add_u32 s20, s61, 0x200000\n"
  "s_add_u32 m0, s60, 36864\n"
  "v_exp_f32 v245, v245\n"
  "s_nop 0\n"
  "buffer_load_dwordx4 v251, s[52:55], s20 offen lds\n"
  "v_exp_f32 v246, v246\n"
  "s_add_u32 s20, s61, 0x230000\n"
  "s_add_u32 m0, s60, 40960\n"
  "v_exp_f32 v247, v247\n"
  "s_nop 0\n"
  "buffer_load_dwordx4 v251, s[52:55], s20 offen lds\n"
  "v_exp_f32 v248, v248\n"
  "s_add_u32 s20, s61, 0x270000\n"
  "s_add_u32 m0, s60, 45056\n"
  "v_exp_f32 v249, v249\n"
  "s_nop 0\n"
  "buffer_load_dwordx4 v251, s[52:55], s20 offen lds\n"
  "v_pk_mul_f32 v[132:133], v[132:133], v[148:149]\n"
  "v_pk_mul_f32 v[134:135], v[134:135], v[150:151]\n"
  "v_pk_mul_f32 v[164:165], v[164:165], v[180:181]\n"
  "v_pk_mul_f32 v[166:167], v[166:167], v[182:183]\n"
  "v_pk_add_f32 v[242:243], v[242:243], v[240:241]\n"
  "v_pk_add_f32 v[244:245], v[244:245], v[240:241]\n"
  "v_pk_add_f32 v[246:247], v[246:247], v[240:241]\n"
  "v_pk_add_f32 v[248:249], v[248:249], v[240:241]\n"
  "v_rcp_f32 v242, v242\n"
  "s_sub_u32 s20, 0x2a0000, s61\n"
  "s_add_u32 m0, s60, 49152\n"
  "v_rcp_f32 v243, v243\n"
  "s_nop 0\n"
  "buffer_load_dwordx4 v251, s[52:55], s20 offen lds\n"
  "v_rcp_f32 v244, v244\n"
  "s_sub_u32 s20, 0x2e0000, s61\n"
  "s_add_u32 m0, s60, 53248\n"
  "v_rcp_f32 v245, v245\n"
  "s_nop 0\n"
  "buffer_load_dwordx4 v251, s[52:55], s20 offen lds\n"
  "v_rcp_f32 v246, v246\n"
  "s_sub_u32 s20, 0x310000, s61\n"
  "s_add_u32 m0, s60, 57344\n"
  "v_rcp_f32 v247, v247\n"
  "s_nop 0\n"
  "buffer_load_dwordx4 v251, s[52:55], s20 offen lds\n"
  "v_rcp_f32 v248, v248\n"
  "s_sub_u32 s20, 0x350000, s61\n"
  "s_add_u32 m0, s60, 61440\n"
  "v_rcp_f32 v249, v249\n"
  "s_nop 0\n"
  "buffer_load_dwordx4 v251, s[52:55], s20 offen lds\n"
  "s_nop 0\n"
  "v_pk_mul_f32 v[242:243], v[242:243], v[132:133]\n"
  "v_pk_mul_f32 v[244:245], v[244:245], v[134:135]\n"
  "v_pk_mul_f32 v[246:247], v[246:247], v[164:165]\n"
  "v_pk_mul_f32 v[248:249], v[248:249], v[166:167]\n"
  "v_cvt_pk_bf16_f32 v148, v242, v243\n"
  "v_cvt_pk_bf16_f32 v149, v244, v245\n"
  "v_cvt_pk_bf16_f32 v150, v246, v247\n"
  "v_cvt_pk_bf16_f32 v151, v248, v249\n"
  "v_pk_mul_f32 v[242:243], v[136:137], v[136:137]\n"
  "v_pk_mul_f32 v[244:245], v[138:139], v[138:139]\n"
  "v_pk_mul_f32 v[246:247], v[168:169], v[168:169]\n"
  "v_pk_mul_f32 v[248:249], v[170:171], v[170:171]\n"
  "v_pk_fma_f32 v[242:243], v[242:243], v[236:237], v[238:239]\n"
  "v_pk_fma_f32 v[244:245], v[244:245], v[236:237], v[238:239]\n"
  "v_pk_fma_f32 v[246:247], v[246:247], v[236:237], v[238:239]\n"
  "v_pk_fma_f32 v[248:249], v[248:249], v[236:237], v[238:239]\n"
  "v_pk_mul_f32 v[242:243], v[242:243], v[136:137]\n"
  "v_pk_mul_f32 v[244:245], v[244:245], v[138:139]\n"
  "v_pk_mul_f32 v[246:247], v[246:247], v[168:169]\n"
  "v_pk_mul_f32 v[248:249], v[248:249], v[170:171]\n"
  "v_exp_f32 v242, v242\n"
  "s_add_u32 s20, s61, 0x380000\n"
  "s_add_u32 m0, s60, 65536\n"
  "v_exp_f32 v243, v243\n"
  "s_nop 0\n"
  "buffer_load_dwordx4 v251, s[52:55], s20 offen lds\n"
  "v_exp_f32 v244, v244\n"
  "s_add_u32 s20, s61, 0x3c0000\n"
  "s_add_u32 m0, s60, 69632\n"
  "v_exp_f32 v245, v245\n"
  "s_nop 0\n"
  "buffer_load_dwordx4 v251, s[52:55], s20 offen lds\n"
  "v_exp_f32 v246, v246\n"
  "s_add_u32 s20, s61, 0x3f0000\n"
  "s_add_u32 m0, s60, 73728\n"
  "v_exp_f32 v247, v247\n"
  "s_nop 0\n"
  "buffer_load_dwordx4 v251, s[52:55], s20 offen lds\n"
  "v_exp_f32 v248, v248\n"
  "s_add_u32 s20, s61, 0x430000\n"
  "s_add_u32 m0, s60, 77824\n"
  "v_exp_f32 v249, v249\n"
  "s_nop 0\n"
  "buffer_load_dwordx4 v251, s[52:55], s20 offen lds\n"
  "v_pk_mul_f32 v[136:137], v[136:137], v[152:153]\n"
  "v_pk_mul_f32 v[138:139], v[138:139], v[154:155]\n"
  "v_pk_mul_f32 v[168:169], v[168:169], v[184:185]\n"
  "v_pk_mul_f32 v[170:171], v[170:171], v[186:187]\n"
  "v_pk_add_f32 v[242:243], v[242:243], v[240:241]\n"
  "v_pk_add_f32 v[244:245], v[244:245], v[240:241]\n"
  "v_pk_add_f32 v[246:247], v[246:247], v[240:241]\n"
  "v_pk_add_f32 v[248:249], v[248:249], v[240:241]\n"
  "v_rcp_f32 v242, v242\n"
  "s_sub_u32 s20, 0x460000, s61\n"
  "s_add_u32 m0, s60, 81920\n"
  "v_rcp_f32 v243, v243\n"
  "s_nop 0\n"
  "buffer_load_dwordx4 v251, s[52:55], s20 offen lds\n"
  "v_rcp_f32 v244, v244\n"
  "s_sub_u32 s20, 0x4a0000, s61\n"
  "s_add_u32 m0, s60, 86016\n"
  "v_rcp_f32 v245, v245\n"
  "s_nop 0\n"
  "buffer_load_dwordx4 v251, s[52:55], s20 offen lds\n"
  "v_rcp_f32 v246, v246\n"
  "s_sub_u32 s20, 0x4d0000, s61\n"
  "s_add_u32 m0, s60, 90112\n"
  "v_rcp_f32 v247, v247\n"
  "s_nop 0\n"
  "buffer_load_dwordx4 v251, s[52:55], s20 offen lds\n"
  "v_rcp_f32 v248, v248\n"
  "s_sub_u32 s20, 0x510000, s61\n"
  "s_add_u32 m0, s60, 94208\n"
  "v_rcp_f32 v249, v249\n"
  "s_nop 0\n"
  "buffer_load_dwordx4 v251, s[52:55], s20 offen lds\n"
  "s_nop 0\n"
  "v_pk_mul_f32 v[242:243], v[242:243], v[136:137]\n"
  "v_pk_mul_f32 v[244:245], v[244:245], v[138:139]\n"
  "v_pk_mul_f32 v[246:247], v[246:247], v[168:169]\n"
  "v_pk_mul_f32 v[248:249], v[248:249], v[170:171]\n"
  "v_cvt_pk_bf16_f32 v152, v242, v243\n"
  "v_cvt_pk_bf16_f32 v153, v244, v245\n"
  "v_cvt_pk_bf16_f32 v154, v246, v247\n"
  "v_cvt_pk_bf16_f32 v155, v248, v249\n"
  "global_load_dword v255, v254, s[74:75] sc1\n"
  "v_pk_mul_f32 v[242:243], v[140:141], v[140:141]\n"
  "v_pk_mul_f32 v[244:245], v[142:143], v[142:143]\n"
  "v_pk_mul_f32 v[246:247], v[172:173], v[172:173]\n"
  "v_pk_mul_f32 v[248:249], v[174:175], v[174:175]\n"
  "v_pk_fma_f32 v[242:243], v[242:243], v[236:237], v[238:239]\n"
  "v_pk_fma_f32 v[244:245], v[244:245], v[236:237], v[238:239]\n"
  "v_pk_fma_f32 v[246:247], v[246:247], v[236:237], v[238:239]\n"
  "v_pk_fma_f32 v[248:249], v[248:249], v[236:237], v[238:239]\n"
  "v_pk_mul_f32 v[242:243], v[242:243], v[140:141]\n"
  "v_pk_mul_f32 v[244:245], v[244:245], v[142:143]\n"
  "v_pk_mul_f32 v[246:247], v[246:247], v[172:173]\n"
  "v_pk_mul_f32 v[248:249], v[248:249], v[174:175]\n"
  "v_exp_f32 v242, v242\n"
  "v_exp_f32 v243, v243\n"
  "v_exp_f32 v244, v244\n"
  "v_exp_f32 v245, v245\n"
  "v_exp_f32 v246, v246\n"
  "v_exp_f32 v247, v247\n"
  "v_exp_f32 v248, v248\n"
  "v_exp_f32 v249, v249\n"
  "v_pk_mul_f32 v[140:141], v[140:141], v[156:157]\n"
  "v_pk_mul_f32 v[142:143], v[142:143], v[158:159]\n"
  "v_pk_mul_f32 v[172:173], v[172:173], v[188:189]\n"
  "v_pk_mul_f32 v[174:175], v[174:175], v[190:191]\n"
  "v_pk_add_f32 v[242:243], v[242:243], v[240:241]\n"
  "v_pk_add_f32 v[244:245], v[244:245], v[240:241]\n"
  "v_pk_add_f32 v[246:247], v[246:247], v[240:241]\n"
  "v_pk_add_f32 v[248:249], v[248:249], v[240:241]\n"
  "v_rcp_f32 v242, v242\n"
  "v_rcp_f32 v243, v243\n"
  "v_rcp_f32 v244, v244\n"
  "v_rcp_f32 v245, v245\n"
  "v_rcp_f32 v246, v246\n"
  "v_rcp_f32 v247, v247\n"
  "v_rcp_f32 v248, v248\n"
  "v_rcp_f32 v249, v249\n"
  "s_nop 0\n"
  "v_pk_mul_f32 v[242:243], v[242:243], v[140:141]\n"
  "v_pk_mul_f32 v[244:245], v[244:245], v[142:143]\n"
  "v_pk_mul_f32 v[246:247], v[246:247], v[172:173]\n"
  "v_pk_mul_f32 v[248:249], v[248:249], v[174:175]\n"
  "v_cvt_pk_bf16_f32 v156, v242, v243\n"
  "v_cvt_pk_bf16_f32 v157, v244, v245\n"
  "v_cvt_pk_bf16_f32 v158, v246, v247\n"
  "v_cvt_pk_bf16_f32 v159, v248, v249\n"
  "v_pk_mul_f32 v[242:243], v[192:193], v[192:193]\n"
  "v_pk_mul_f32 v[244:245], v[194:195], v[194:195]\n"
  "v_pk_mul_f32 v[246:247], v[196:197], v[196:197]\n"
  "v_pk_mul_f32 v[248:249], v[198:199], v[198:199]\n"
  "v_pk_fma_f32 v[242:243], v[242:243], v[236:237], v[238:239]\n"
  "v_pk_fma_f32 v[244:245], v[244:245], v[236:237], v[238:239]\n"
  "v_pk_fma_f32 v[246:247], v[246:247], v[236:237], v[238:239]\n"
  "v_pk_fma_f32 v[248:249], v[248:249], v[236:237], v[238:239]\n"
  "v_pk_mul_f32 v[242:243], v[242:243], v[192:193]\n"
  "v_pk_mul_f32 v[244:245], v[244:245], v[194:195]\n"
  "v_pk_mul_f32 v[246:247], v[246:247], v[196:197]\n"
  "v_pk_mul_f32 v[248:249], v[248:249], v[198:199]\n"
  "v_exp_f32 v242, v242\n"
  "v_exp_f32 v243, v243\n"
  "v_exp_f32 v244, v244\n"
  "v_exp_f32 v245, v245\n"
  "v_exp_f32 v246, v246\n"
  "v_exp_f32 v247, v247\n"
  "v_exp_f32 v248, v248\n"
  "v_exp_f32 v249, v249\n"
  "v_pk_mul_f32 v[192:193], v[192:193], v[208:209]\n"
  "v_pk_mul_f32 v[194:195], v[194:195], v[210:211]\n"
  "v_pk_mul_f32 v[196:197], v[196:197], v[212:213]\n"
  "v_pk_mul_f32 v[198:199], v[198:199], v[214:215]\n"
  "v_pk_add_f32 v[242:243], v[242:243], v[240:241]\n"
  "v_pk_add_f32 v[244:245], v[244:245], v[240:241]\n"
  "v_pk_add_f32 v[246:247], v[246:247], v[240:241]\n"
  "v_pk_add_f32 v[248:249], v[248:249], v[240:241]\n"
  "v_rcp_f32 v242, v242\n"
  "v_rcp_f32 v243, v243\n"
  "v_rcp_f32 v244, v244\n"
  "v_rcp_f32 v245, v245\n"
  "v_rcp_f32 v246, v246\n"
  "v_rcp_f32 v247, v247\n"
  "v_rcp_f32 v248, v248\n"
  "v_rcp_f32 v249, v249\n"
  "s_nop 0\n"
  "v_pk_mul_f32 v[242:243], v[242:243], v[192:193]\n"
  "v_pk_mul_f32 v[244:245], v[244:245], v[194:195]\n"
  "v_pk_mul_f32 v[246:247], v[246:247], v[196:197]\n"
  "v_pk_mul_f32 v[248:249], v[248:249], v[198:199]\n"
  "v_cvt_pk_bf16_f32 v208, v242, v243\n"
  "v_cvt_pk_bf16_f32 v209, v244, v245\n"
  "v_cvt_pk_bf16_f32 v212, v246, v247\n"
  "v_cvt_pk_bf16_f32 v213, v248, v249\n"
  "v_pk_mul_f32 v[242:243], v[200:201], v[200:201]\n"
  "v_pk_mul_f32 v[244:245], v[202:203], v[202:203]\n"
  "v_pk_mul_f32 v[246:247], v[204:205], v[204:205]\n"
  "v_pk_mul_f32 v[248:249], v[206:207], v[206:207]\n"
  "v_pk_fma_f32 v[242:243], v[242:243], v[236:237], v[238:239]\n"
  "v_pk_fma_f32 v[244:245], v[244:245], v[236:237], v[238:239]\n"
  "v_pk_fma_f32 v[246:247], v[246:247], v[236:237], v[238:239]\n"
  "v_pk_fma_f32 v[248:249], v[248:249], v[236:237], v[238:239]\n"
  "v_pk_mul_f32 v[242:243], v[242:243], v[200:201]\n"
  "v_pk_mul_f32 v[244:245], v[244:245], v[202:203]\n"
  "v_pk_mul_f32 v[246:247], v[246:247], v[204:205]\n"
  "v_pk_mul_f32 v[248:249], v[248:249], v[206:207]\n"
  "v_exp_f32 v242, v242\n"
  "v_exp_f32 v243, v243\n"
  "v_exp_f32 v244, v244\n"
  "v_exp_f32 v245, v245\n"
  "v_exp_f32 v246, v246\n"
  "v_exp_f32 v247, v247\n"
  "v_exp_f32 v248, v248\n"
  "v_exp_f32 v249, v249\n"
  "v_pk_mul_f32 v[200:201], v[200:201], v[216:217]\n"
  "v_pk_mul_f32 v[202:203], v[202:203], v[218:219]\n"
  "v_pk_mul_f32 v[204:205], v[204:205], v[220:221]\n"
  "v_pk_mul_f32 v[206:207], v[206:207], v[222:223]\n"
  "v_pk_add_f32 v[242:243], v[242:243], v[240:241]\n"
  "v_pk_add_f32 v[244:245], v[244:245], v[240:241]\n"
  "v_pk_add_f32 v[246:247], v[246:247], v[240:241]\n"
  "v_pk_add_f32 v[248:249], v[248:249], v[240:241]\n"
  "v_rcp_f32 v242, v242\n"
  "v_rcp_f32 v243, v243\n"
  "v_rcp_f32 v244, v244\n"
  "v_rcp_f32 v245, v245\n"
  "v_rcp_f32 v246, v246\n"
  "v_rcp_f32 v247, v247\n"
  "v_rcp_f32 v248, v248\n"
  "v_rcp_f32 v249, v249\n"
  "s_nop 0\n"
  "v_pk_mul_f32 v[242:243], v[242:243], v[200:201]\n"
  "v_pk_mul_f32 v[244:245], v[244:245], v[202:203]\n"
  "v_pk_mul_f32 v[246:247], v[246:247], v[204:205]\n"
  "v_pk_mul_f32 v[248:249], v[248:249], v[206:207]\n"
  "v_cvt_pk_bf16_f32 v216, v242, v243\n"
  "v_cvt_pk_bf16_f32 v217, v244, v245\n"
  "v_cvt_pk_bf16_f32 v220, v246, v247\n"
  "v_cvt_pk_bf16_f32 v221, v248, v249\n"
  "s_waitcnt vmcnt(0)\n"
  : "+{s25}"(ooff_),"+{s27}"(pfo_)
    ,"={v[144:147]}"(hd0),"={v[148:151]}"(hd1),"={v[152:155]}"(hd2),"={v[156:159]}"(hd3),
     "={v[208:209]}"(he0),"={v[212:213]}"(he1),"={v[216:217]}"(he2),"={v[220:221]}"(he3),"={v255}"(pv)
  : "{v224}"(doff[0]),"{v225}"(doff[1]),"{v226}"(doff[2]),"{v227}"(doff[3]),"{v228}"(doff[4]),"{v229}"(doff[5]),"{v230}"(doff[6]),
     "{v231}"(l16),"{v232}"(ldl),"{v233}"(ldl+57344u),"{v234}"(ldl+114688u),"{v235}"(vhalf),"{v237}"((lane&7)*4u),
     "{s[8:11]}"(srdW),"{s[12:15]}"(srdT),"{s[16:19]}"(srdO),"{s[56:57]}"(fpoll),
     "{s[52:55]}"(srdP),"{s23}"(dreg),"{s26}"(hoff)
     ,"{s[64:67]}"(srdC2),"{s68}"(((idx*4+w)&31u)*1024u)
     ,"{v251}"(doff4),"{s60}"(ldsb+w*1024u),"{s61}"(rot4*(14u*65536u)),"{v253}"(lane*128u),"{v254}"((lane&31)*4u),
      "{s[70:71]}"(hfl+T*128+fb),"{s[72:73]}"(hfl+4096+T*128+fb),"{s[74:75]}"(hfl+(T4*4+(nb4&3))*32)
  : "v0","v1","v2","v3","v4","v5","v6","v7","v8","v9","v10","v11","v12","v13","v14","v15","v16","v17","v18","v19","v20","v21","v22","v23","v24","v25","v26","v27","v28","v29","v30","v31","v32","v33","v34","v35","v36","v37","v38","v39","v40","v41","v42","v43","v44","v45","v46","v47","v48","v49","v50","v51","v52","v53","v54","v55","v56","v57","v58","v59","v60","v61","v62","v63","v64","v65","v66","v67","v68","v69","v70","v71","v72","v73","v74","v75","v76","v77","v78","v79","v80","v81","v82","v83","v84","v85","v86","v87","v88","v89","v90","v91","v92","v93","v94","v95","v96","v97","v98","v99","v100","v101","v102","v103","v104","v105","v106","v107","v108","v109","v110","v111","v112","v113","v114","v115","v116","v117","v118","v119","v120","v121","v122","v123","v124","v125","v126","v127","v128","v129","v130","v131","v132","v133","v134","v135","v136","v137","v138","v139","v140","v141","v142","v143","v160","v161","v162","v163","v164","v165","v166","v167","v168","v169","v170","v171","v172","v173","v174","v175","v176","v177","v178","v179","v180","v181","v182","v183","v184","v185","v186","v187","v188","v189","v190","v191","v192","v193","v194","v195","v196","v197","v198","v199","v200","v201","v202","v203","v204","v205","v206","v207","v210","v211","v214","v215","v218","v219","v222","v223","v236","v238","v239","v240","v241","v242","v243","v244","v245","v246","v247","v248","v249","v250","a0","a1","a2","a3","a4","a5","a6","a7","a8","a9","a10","a11","a12","a13","a14","a15","a16","a17","a18","a19","a20","a21","a22","a23","a24","a25","a26","a27","a28","a29","a30","a31","a32","a33","a34","a35","a36","a37","a38","a39","a40","a41","a42","a43","a44","a45","a46","a47","a48","a49","a50","a51","a52","a53","a54","a55","a56","a57","a58","a59","a60","a61","a62","a63","a64","a65","a66","a67","a68","a69","a70","a71","a72","a73","a74","a75","a76","a77","a78","a79","a80","a81","a82","a83","a84","a85","a86","a87","a88","a89","a90","a91","a92","a93","a94","a95","a96","a97","a98","a99","a100","a101","a102","a103","a104","a105","a106","a107","a108","a109","a110","a111","a112","a113","a114","a115","a116","a117","a118","a119","a120","a121","a122","a123","a124","a125","a126","a127","a128","a129","a130","a131","a132","a133","a134","a135","a136","a137","a138","a139","a140","a141","a142","a143","a144","a145","a146","a147","a148","a149","a150","a151","a152","a153","a154","a155","a156","a157","a158","a159","s20","s21","m0","scc","vcc","memory");
  {
    unsigned ln; asm volatile("v_mbcnt_lo_u32_b32 %0, -1, 0\n v_mbcnt_hi_u32_b32 %0, -1, %0":"=v"(ln));
    const unsigned g4=ln>>4;
    const unsigned r8=ln>>3, c8=ln&7;
    const unsigned df4=(w*8+r8)*2048u+nb4*128u+32u*((c8>>1)^((r8>>1)&3))+16u*(c8&1);
    const unsigned q4=(ln&15)>>2, pp4=ln&3, row=4*g4+q4;
    const unsigned rb=ldsb+row*128u+8u*pp4, sw=(row>>1)&3;
    const unsigned rd0=rb+32u*(0^sw), rd1=rb+32u*(1^sw), rd2=rb+32u*(2^sw), rd3=rb+32u*(3^sw);
    const unsigned oo4=(ln&15)*2048u+(K4_W4?(g4&1)*32u+(g4>>1)*16u:g4*8u), l16=ln*16u, pof=(ln&31)*4u;
    u32x4 srdW4=mksrd(wd,3584u*2048u);
    u32x4 srdH4=mksrd((const char*)Ht+(size_t)T4*(112u*4096u)+ih*2048u,112u*4096u-ih*2048u);
    u32x4 srdO4=mksrd(out,2048u*2048u);
    u32x4 srdX4=mksrd((const char*)x1t+(size_t)T4*131072u+nb4*8192u+ih*2048u,8192u-ih*2048u);
    u32x4 srdG4=mksrd(modp+5120+nb4*64,(6144u+64u)*4u);
    u32x4 srdO3=mksrd((const char*)Ht+(size_t)T*(112u*4096u),112u*4096u);
    const unsigned os0=(T4*64u+ih*32u)*2048u+nb4*128u;
    const unsigned* fpl=hfl+(T4*4+(nb4&3))*32;
    if(__builtin_amdgcn_ballot_w64(pv!=0x3f800000u)!=0ull){
      unsigned n=0, v;
      do{
        __builtin_amdgcn_s_sleep(K4_PSLEEP);
        asm volatile("global_load_dword %0, %1, %2 sc1\n s_waitcnt vmcnt(0)":"=v"(v):"v"(pof),"s"(fpl):"memory");
        if(__builtin_amdgcn_ballot_w64(v!=0x3f800000u)==0ull) break;
      }while(++n<0x40000u);
    }
    asm volatile(
  "s_nop 4\n"
  "s_mul_i32 s54, s59, 0xe000\n"
  "s_mul_i32 s57, s59, 0xe0000\n"
  "s_mul_i32 s55, s59, 0xa000\n"
  "s_mul_i32 s65, s59, 0xa0000\n"
  "s_mul_i32 s56, s59, 0x8000\n"
  "s_mul_i32 s66, s59, 0x80000\n"
  "s_add_u32 s21, s54, 0x0\n"
  "buffer_load_dwordx4 a[0:3], v73, s[12:15], s21 offen offset:0 sc1\n"
  "buffer_load_dwordx4 a[4:7], v73, s[12:15], s21 offen offset:1024 sc1\n"
  "s_add_u32 s21, s54, 0x4000\n"
  "buffer_load_dwordx4 a[8:11], v73, s[12:15], s21 offen offset:0 sc1\n"
  "buffer_load_dwordx4 a[12:15], v73, s[12:15], s21 offen offset:1024 sc1\n"
  "s_add_u32 s21, s54, 0x7000\n"
  "buffer_load_dwordx4 a[16:19], v73, s[12:15], s21 offen offset:0 sc1\n"
  "buffer_load_dwordx4 a[20:23], v73, s[12:15], s21 offen offset:1024 sc1\n"
  "s_add_u32 s21, s54, 0xb000\n"
  "buffer_load_dwordx4 a[24:27], v73, s[12:15], s21 offen offset:0 sc1\n"
  "buffer_load_dwordx4 a[28:31], v73, s[12:15], s21 offen offset:1024 sc1\n"
  "s_waitcnt vmcnt(6)\n"
  "s_barrier\n"
  "ds_read_b64_tr_b16 a[160:161], v74 offset:0\n"
  "ds_read_b64_tr_b16 a[162:163], v74 offset:2048\n"
  "ds_read_b64_tr_b16 a[164:165], v75 offset:0\n"
  "ds_read_b64_tr_b16 a[166:167], v75 offset:2048\n"
  "ds_read_b64_tr_b16 a[168:169], v76 offset:0\n"
  "ds_read_b64_tr_b16 a[170:171], v76 offset:2048\n"
  "ds_read_b64_tr_b16 a[172:173], v77 offset:0\n"
  "ds_read_b64_tr_b16 a[174:175], v77 offset:2048\n"
  "s_waitcnt lgkmcnt(6)\n"
  "v_mfma_f32_16x16x32_bf16 v[0:3], a[160:163], a[0:3], 0\n"
  "ds_read_b64_tr_b16 a[176:177], v74 offset:4096\n"
  "ds_read_b64_tr_b16 a[178:179], v74 offset:6144\n"
  "v_mfma_f32_16x16x32_bf16 v[16:19], a[160:163], a[4:7], 0\n"
  "s_sub_u32 s21, 0xe000, s54\n"
  "buffer_load_dwordx4 a[32:35], v73, s[12:15], s21 offen offset:0 sc1\n"
  "s_waitcnt lgkmcnt(6)\n"
  "v_mfma_f32_16x16x32_bf16 v[4:7], a[164:167], a[0:3], 0\n"
  "ds_read_b64_tr_b16 a[180:181], v75 offset:4096\n"
  "ds_read_b64_tr_b16 a[182:183], v75 offset:6144\n"
  "v_mfma_f32_16x16x32_bf16 v[20:23], a[164:167], a[4:7], 0\n"
  "buffer_load_dwordx4 a[36:39], v73, s[12:15], s21 offen offset:1024 sc1\n"
  "s_sub_u32 s21, 0x12000, s54\n"
  "buffer_load_dwordx4 a[40:43], v73, s[12:15], s21 offen offset:0 sc1\n"
  "s_waitcnt lgkmcnt(6)\n"
  "v_mfma_f32_16x16x32_bf16 v[8:11], a[168:171], a[0:3], 0\n"
  "ds_read_b64_tr_b16 a[184:185], v76 offset:4096\n"
  "ds_read_b64_tr_b16 a[186:187], v76 offset:6144\n"
  "v_mfma_f32_16x16x32_bf16 v[24:27], a[168:171], a[4:7], 0\n"
  "buffer_load_dwordx4 a[44:47], v73, s[12:15], s21 offen offset:1024 sc1\n"
  "s_waitcnt lgkmcnt(6)\n"
  "v_mfma_f32_16x16x32_bf16 v[12:15], a[172:175], a[0:3], 0\n"
  "ds_read_b64_tr_b16 a[188:189], v77 offset:4096\n"
  "ds_read_b64_tr_b16 a[190:191], v77 offset:6144\n"
  "v_mfma_f32_16x16x32_bf16 v[28:31], a[172:175], a[4:7], 0\n"
  "s_sub_u32 s21, 0x15000, s54\n"
  "buffer_load_dwordx4 a[48:51], v73, s[12:15], s21 offen offset:0 sc1\n"
  "buffer_load_dwordx4 a[52:55], v73, s[12:15], s21 offen offset:1024 sc1\n"
  "s_waitcnt vmcnt(10) lgkmcnt(6)\n"
  "v_mfma_f32_16x16x32_bf16 v[0:3], a[176:179], a[8:11], v[0:3]\n"
  "ds_read_b64_tr_b16 a[160:161], v74 offset:8192\n"
  "ds_read_b64_tr_b16 a[162:163], v74 offset:10240\n"
  "v_mfma_f32_16x16x32_bf16 v[16:19], a[176:179], a[12:15], v[16:19]\n"
  "s_sub_u32 s21, 0x19000, s54\n"
  "buffer_load_dwordx4 a[56:59], v73, s[12:15], s21 offen offset:0 sc1\n"
  "s_waitcnt lgkmcnt(6)\n"
  "v_mfma_f32_16x16x32_bf16 v[4:7], a[180:183], a[8:11], v[4:7]\n"
  "ds_read_b64_tr_b16 a[164:165], v75 offset:8192\n"
  "ds_read_b64_tr_b16 a[166:167], v75 offset:10240\n"
  "v_mfma_f32_16x16x32_bf16 v[20:23], a[180:183], a[12:15], v[20:23]\n"
  "buffer_load_dwordx4 a[60:63], v73, s[12:15], s21 offen offset:1024 sc1\n"
  "s_add_u32 s21, s54, 0x1c000\n"
  "buffer_load_dwordx4 a[64:67], v73, s[12:15], s21 offen offset:0 sc1\n"
  "s_waitcnt lgkmcnt(6)\n"
  "v_mfma_f32_16x16x32_bf16 v[8:11], a[184:187], a[8:11], v[8:11]\n"
  "ds_read_b64_tr_b16 a[168:169], v76 offset:8192\n"
  "ds_read_b64_tr_b16 a[170:171], v76 offset:10240\n"
  "v_mfma_f32_16x16x32_bf16 v[24:27], a[184:187], a[12:15], v[24:27]\n"
  "buffer_load_dwordx4 a[68:71], v73, s[12:15], s21 offen offset:1024 sc1\n"
  "s_waitcnt lgkmcnt(6)\n"
  "v_mfma_f32_16x16x32_bf16 v[12:15], a[188:191], a[8:11], v[12:15]\n"
  "ds_read_b64_tr_b16 a[172:173], v77 offset:8192\n"
  "ds_read_b64_tr_b16 a[174:175], v77 offset:10240\n"
  "v_mfma_f32_16x16x32_bf16 v[28:31], a[188:191], a[12:15], v[28:31]\n"
  "s_add_u32 s21, s54, 0x20000\n"
  "buffer_load_dwordx4 a[72:75], v73, s[12:15], s21 offen offset:0 sc1\n"
  "buffer_load_dwordx4 a[76:79], v73, s[12:15], s21 offen offset:1024 sc1\n"
  "s_waitcnt vmcnt(14) lgkmcnt(6)\n"
  "v_mfma_f32_16x16x32_bf16 v[0:3], a[160:163], a[16:19], v[0:3]\n"
  "ds_read_b64_tr_b16 a[176:177], v74 offset:12288\n"
  "ds_read_b64_tr_b16 a[178:179], v74 offset:14336\n"
  "v_mfma_f32_16x16x32_bf16 v[16:19], a[160:163], a[20:23], v[16:19]\n"
  "s_add_u32 s21, s54, 0x23000\n"
  "buffer_load_dwordx4 a[80:83], v73, s[12:15], s21 offen offset:0 sc1\n"
  "s_waitcnt lgkmcnt(6)\n"
  "v_mfma_f32_16x16x32_bf16 v[4:7], a[164:167], a[16:19], v[4:7]\n"
  "ds_read_b64_tr_b16 a[180:181], v75 offset:12288\n"
  "ds_read_b64_tr_b16 a[182:183], v75 offset:14336\n"
  "v_mfma_f32_16x16x32_bf16 v[20:23], a[164:167], a[20:23], v[20:23]\n"
  "buffer_load_dwordx4 a[84:87], v73, s[12:15], s21 offen offset:1024 sc1\n"
  "s_add_u32 s21, s54, 0x27000\n"
  "buffer_load_dwordx4 a[88:91], v73, s[12:15], s21 offen offset:0 sc1\n"
  "s_waitcnt lgkmcnt(6)\n"
  "v_mfma_f32_16x16x32_bf16 v[8:11], a[168:171], a[16:19], v[8:11]\n"
  "ds_read_b64_tr_b16 a[184:185], v76 offset:12288\n"
  "ds_read_b64_tr_b16 a[186:187], v76 offset:14336\n"
  "v_mfma_f32_16x16x32_bf16 v[24:27], a[168:171], a[20:23], v[24:27]\n"
  "buffer_load_dwordx4 a[92:95], v73, s[12:15], s21 offen offset:1024 sc1\n"
  "s_waitcnt lgkmcnt(6)\n"
  "v_mfma_f32_16x16x32_bf16 v[12:15], a[172:175], a[16:19], v[12:15]\n"
  "ds_read_b64_tr_b16 a[188:189], v77 offset:12288\n"
  "ds_read_b64_tr_b16 a[190:191], v77 offset:14336\n"
  "v_mfma_f32_16x16x32_bf16 v[28:31], a[172:175], a[20:23], v[28:31]\n"
  "s_sub_u32 s21, 0x2a000, s54\n"
  "buffer_load_dwordx4 a[96:99], v73, s[12:15], s21 offen offset:0 sc1\n"
  "buffer_load_dwordx4 a[100:103], v73, s[12:15], s21 offen offset:1024 sc1\n"
  "s_barrier\n"
  "v_mov_b32 v108, 1.0\n"
  "s_mov_b64 exec, 15\n"
  "global_store_dword v107, v108, s[76:77] sc1\n"
  "s_mov_b64 exec, -1\n"
  "buffer_store_dwordx4 v[144:147], v73, s[68:71], s72 offen offset:0 sc1\n"
  "buffer_store_dwordx4 v[148:151], v73, s[68:71], s72 offen offset:1024 sc1\n"
  "buffer_store_dwordx4 v[152:155], v73, s[68:71], s72 offen offset:2048 sc1\n"
  "buffer_store_dwordx4 v[156:159], v73, s[68:71], s72 offen offset:3072 sc1\n"
  "buffer_store_dwordx2 v[208:209], v106, s[68:71], s73 offen offset:0 sc1\n"
  "buffer_store_dwordx2 v[212:213], v106, s[68:71], s73 offen offset:1024 sc1\n"
  "buffer_store_dwordx2 v[216:217], v106, s[68:71], s73 offen offset:2048 sc1\n"
  "buffer_store_dwordx2 v[220:221], v106, s[68:71], s73 offen offset:3072 sc1\n"
  "v_mov_b32 v108, 1.0\n"
  "s_waitcnt vmcnt(27) lgkmcnt(6)\n"
  "v_mfma_f32_16x16x32_bf16 v[0:3], a[176:179], a[24:27], v[0:3]\n"
  "ds_read_b64_tr_b16 a[160:161], v74 offset:16384\n"
  "ds_read_b64_tr_b16 a[162:163], v74 offset:18432\n"
  "v_mfma_f32_16x16x32_bf16 v[16:19], a[176:179], a[28:31], v[16:19]\n"
  "s_sub_u32 s21, 0x2e000, s54\n"
  "buffer_load_dwordx4 a[104:107], v73, s[12:15], s21 offen offset:0 sc1\n"
  "s_waitcnt lgkmcnt(6)\n"
  "v_mfma_f32_16x16x32_bf16 v[4:7], a[180:183], a[24:27], v[4:7]\n"
  "ds_read_b64_tr_b16 a[164:165], v75 offset:16384\n"
  "ds_read_b64_tr_b16 a[166:167], v75 offset:18432\n"
  "v_mfma_f32_16x16x32_bf16 v[20:23], a[180:183], a[28:31], v[20:23]\n"
  "buffer_load_dwordx4 a[108:111], v73, s[12:15], s21 offen offset:1024 sc1\n"
  "s_sub_u32 s21, 0x31000, s54\n"
  "buffer_load_dwordx4 a[112:115], v73, s[12:15], s21 offen offset:0 sc1\n"
  "s_waitcnt lgkmcnt(6)\n"
  "v_mfma_f32_16x16x32_bf16 v[8:11], a[184:187], a[24:27], v[8:11]\n"
  "ds_read_b64_tr_b16 a[168:169], v76 offset:16384\n"
  "ds_read_b64_tr_b16 a[170:171], v76 offset:18432\n"
  "v_mfma_f32_16x16x32_bf16 v[24:27], a[184:187], a[28:31], v[24:27]\n"
  "buffer_load_dwordx4 a[116:119], v73, s[12:15], s21 offen offset:1024 sc1\n"
  "s_waitcnt lgkmcnt(6)\n"
  "v_mfma_f32_16x16x32_bf16 v[12:15], a[188:191], a[24:27], v[12:15]\n"
  "ds_read_b64_tr_b16 a[172:173], v77 offset:16384\n"
  "ds_read_b64_tr_b16 a[174:175], v77 offset:18432\n"
  "v_mfma_f32_16x16x32_bf16 v[28:31], a[188:191], a[28:31], v[28:31]\n"
  "s_sub_u32 s21, 0x35000, s54\n"
  "buffer_load_dwordx4 a[120:123], v73, s[12:15], s21 offen offset:0 sc1\n"
  "buffer_load_dwordx4 a[124:127], v73, s[12:15], s21 offen offset:1024 sc1\n"
  "s_waitcnt vmcnt(31) lgkmcnt(6)\n"
  "v_mfma_f32_16x16x32_bf16 v[0:3], a[160:163], a[32:35], v[0:3]\n"
  "ds_read_b64_tr_b16 a[176:177], v74 offset:20480\n"
  "ds_read_b64_tr_b16 a[178:179], v74 offset:22528\n"
  "v_mfma_f32_16x16x32_bf16 v[16:19], a[160:163], a[36:39], v[16:19]\n"
  "s_add_u32 s21, s54, 0x38000\n"
  "buffer_load_dwordx4 a[128:131], v73, s[12:15], s21 offen offset:0 sc1\n"
  "s_waitcnt lgkmcnt(6)\n"
  "v_mfma_f32_16x16x32_bf16 v[4:7], a[164:167], a[32:35], v[4:7]\n"
  "ds_read_b64_tr_b16 a[180:181], v75 offset:20480\n"
  "ds_read_b64_tr_b16 a[182:183], v75 offset:22528\n"
  "v_mfma_f32_16x16x32_bf16 v[20:23], a[164:167], a[36:39], v[20:23]\n"
  "buffer_load_dwordx4 a[132:135], v73, s[12:15], s21 offen offset:1024 sc1\n"
  "s_add_u32 s21, s54, 0x3c000\n"
  "buffer_load_dwordx4 a[136:139], v73, s[12:15], s21 offen offset:0 sc1\n"
  "s_waitcnt lgkmcnt(6)\n"
  "v_mfma_f32_16x16x32_bf16 v[8:11], a[168:171], a[32:35], v[8:11]\n"
  "ds_read_b64_tr_b16 a[184:185], v76 offset:20480\n"
  "ds_read_b64_tr_b16 a[186:187], v76 offset:22528\n"
  "v_mfma_f32_16x16x32_bf16 v[24:27], a[168:171], a[36:39], v[24:27]\n"
  "buffer_load_dwordx4 a[140:143], v73, s[12:15], s21 offen offset:1024 sc1\n"
  "s_waitcnt lgkmcnt(6)\n"
  "v_mfma_f32_16x16x32_bf16 v[12:15], a[172:175], a[32:35], v[12:15]\n"
  "ds_read_b64_tr_b16 a[188:189], v77 offset:20480\n"
  "ds_read_b64_tr_b16 a[190:191], v77 offset:22528\n"
  "v_mfma_f32_16x16x32_bf16 v[28:31], a[172:175], a[36:39], v[28:31]\n"
  "s_add_u32 s21, s54, 0x3f000\n"
  "buffer_load_dwordx4 a[144:147], v73, s[12:15], s21 offen offset:0 sc1\n"
  "buffer_load_dwordx4 a[148:151], v73, s[12:15], s21 offen offset:1024 sc1\n"
  "s_waitcnt vmcnt(35) lgkmcnt(6)\n"
  "v_mfma_f32_16x16x32_bf16 v[0:3], a[176:179], a[40:43], v[0:3]\n"
  "ds_read_b64_tr_b16 a[160:161], v74 offset:24576\n"
  "ds_read_b64_tr_b16 a[162:163], v74 offset:26624\n"
  "v_mfma_f32_16x16x32_bf16 v[16:19], a[176:179], a[44:47], v[16:19]\n"
  "s_add_u32 s21, s54, 0x43000\n"
  "buffer_load_dwordx4 a[152:155], v73, s[12:15], s21 offen offset:0 sc1\n"
  "s_waitcnt lgkmcnt(6)\n"
  "v_mfma_f32_16x16x32_bf16 v[4:7], a[180:183], a[40:43], v[4:7]\n"
  "ds_read_b64_tr_b16 a[164:165], v75 offset:24576\n"
  "ds_read_b64_tr_b16 a[166:167], v75 offset:26624\n"
  "v_mfma_f32_16x16x32_bf16 v[20:23], a[180:183], a[44:47], v[20:23]\n"
  "buffer_load_dwordx4 a[156:159], v73, s[12:15], s21 offen offset:1024 sc1\n"
  "s_sub_u32 s21, 0x46000, s54\n"
  "buffer_load_dwordx4 a[0:3], v73, s[12:15], s21 offen offset:0 sc1\n"
  "s_waitcnt lgkmcnt(6)\n"
  "v_mfma_f32_16x16x32_bf16 v[8:11], a[184:187], a[40:43], v[8:11]\n"
  "ds_read_b64_tr_b16 a[168:169], v76 offset:24576\n"
  "ds_read_b64_tr_b16 a[170:171], v76 offset:26624\n"
  "v_mfma_f32_16x16x32_bf16 v[24:27], a[184:187], a[44:47], v[24:27]\n"
  "buffer_load_dwordx4 a[4:7], v73, s[12:15], s21 offen offset:1024 sc1\n"
  "s_waitcnt lgkmcnt(6)\n"
  "v_mfma_f32_16x16x32_bf16 v[12:15], a[188:191], a[40:43], v[12:15]\n"
  "ds_read_b64_tr_b16 a[172:173], v77 offset:24576\n"
  "ds_read_b64_tr_b16 a[174:175], v77 offset:26624\n"
  "v_mfma_f32_16x16x32_bf16 v[28:31], a[188:191], a[44:47], v[28:31]\n"
  "s_sub_u32 s21, 0x4a000, s54\n"
  "buffer_load_dwordx4 a[8:11], v73, s[12:15], s21 offen offset:0 sc1\n"
  "buffer_load_dwordx4 a[12:15], v73, s[12:15], s21 offen offset:1024 sc1\n"
  "s_waitcnt vmcnt(39) lgkmcnt(6)\n"
  "v_mfma_f32_16x16x32_bf16 v[0:3], a[160:163], a[48:51], v[0:3]\n"
  "ds_read_b64_tr_b16 a[176:177], v74 offset:28672\n"
  "ds_read_b64_tr_b16 a[178:179], v74 offset:30720\n"
  "v_mfma_f32_16x16x32_bf16 v[16:19], a[160:163], a[52:55], v[16:19]\n"
  "s_sub_u32 s21, 0x4d000, s54\n"
  "buffer_load_dwordx4 a[16:19], v73, s[12:15], s21 offen offset:0 sc1\n"
  "s_waitcnt lgkmcnt(6)\n"
  "v_mfma_f32_16x16x32_bf16 v[4:7], a[164:167], a[48:51], v[4:7]\n"
  "ds_read_b64_tr_b16 a[180:181], v75 offset:28672\n"
  "ds_read_b64_tr_b16 a[182:183], v75 offset:30720\n"
  "v_mfma_f32_16x16x32_bf16 v[20:23], a[164:167], a[52:55], v[20:23]\n"
  "buffer_load_dwordx4 a[20:23], v73, s[12:15], s21 offen offset:1024 sc1\n"
  "s_sub_u32 s21, 0x51000, s54\n"
  "buffer_load_dwordx4 a[24:27], v73, s[12:15], s21 offen offset:0 sc1\n"
  "s_waitcnt lgkmcnt(6)\n"
  "v_mfma_f32_16x16x32_bf16 v[8:11], a[168:171], a[48:51], v[8:11]\n"
  "ds_read_b64_tr_b16 a[184:185], v76 offset:28672\n"
  "ds_read_b64_tr_b16 a[186:187], v76 offset:30720\n"
  "v_mfma_f32_16x16x32_bf16 v[24:27], a[168:171], a[52:55], v[24:27]\n"
  "buffer_load_dwordx4 a[28:31], v73, s[12:15], s21 offen offset:1024 sc1\n"
  "s_waitcnt lgkmcnt(6)\n"
  "v_mfma_f32_16x16x32_bf16 v[12:15], a[172:175], a[48:51], v[12:15]\n"
  "ds_read_b64_tr_b16 a[188:189], v77 offset:28672\n"
  "ds_read_b64_tr_b16 a[190:191], v77 offset:30720\n"
  "v_mfma_f32_16x16x32_bf16 v[28:31], a[172:175], a[52:55], v[28:31]\n"
  "s_add_u32 s21, s54, 0x54000\n"
  "buffer_load_dwordx4 a[32:35], v73, s[12:15], s21 offen offset:0 sc1\n"
  "buffer_load_dwordx4 a[36:39], v73, s[12:15], s21 offen offset:1024 sc1\n"
  "s_barrier\n"
  "s_waitcnt vmcnt(43) lgkmcnt(6)\n"
  "v_mfma_f32_16x16x32_bf16 v[0:3], a[176:179], a[56:59], v[0:3]\n"
  "ds_read_b64_tr_b16 a[160:161], v74 offset:32768\n"
  "ds_read_b64_tr_b16 a[162:163], v74 offset:34816\n"
  "v_mfma_f32_16x16x32_bf16 v[16:19], a[176:179], a[60:63], v[16:19]\n"
  "s_add_u32 s20, s57, 0x540000\n"
  "s_add_u32 m0, s22, 0\n"
  "s_nop 0\n"
  "buffer_load_dwordx4 v72, s[8:11], s20 offen lds\n"
  "s_waitcnt lgkmcnt(6)\n"
  "v_mfma_f32_16x16x32_bf16 v[4:7], a[180:183], a[56:59], v[4:7]\n"
  "ds_read_b64_tr_b16 a[164:165], v75 offset:32768\n"
  "ds_read_b64_tr_b16 a[166:167], v75 offset:34816\n"
  "v_mfma_f32_16x16x32_bf16 v[20:23], a[180:183], a[60:63], v[20:23]\n"
  "s_add_u32 s20, s57, 0x580000\n"
  "s_add_u32 m0, s22, 4096\n"
  "s_nop 0\n"
  "buffer_load_dwordx4 v72, s[8:11], s20 offen lds\n"
  "s_add_u32 s20, s57, 0x5b0000\n"
  "s_add_u32 m0, s22, 8192\n"
  "s_nop 0\n"
  "buffer_load_dwordx4 v72, s[8:11], s20 offen lds\n"
  "s_waitcnt lgkmcnt(6)\n"
  "v_mfma_f32_16x16x32_bf16 v[8:11], a[184:187], a[56:59], v[8:11]\n"
  "ds_read_b64_tr_b16 a[168:169], v76 offset:32768\n"
  "ds_read_b64_tr_b16 a[170:171], v76 offset:34816\n"
  "v_mfma_f32_16x16x32_bf16 v[24:27], a[184:187], a[60:63], v[24:27]\n"
  "s_add_u32 s20, s57, 0x5f0000\n"
  "s_add_u32 m0, s22, 12288\n"
  "s_nop 0\n"
  "buffer_load_dwordx4 v72, s[8:11], s20 offen lds\n"
  "s_waitcnt lgkmcnt(6)\n"
  "v_mfma_f32_16x16x32_bf16 v[12:15], a[188:191], a[56:59], v[12:15]\n"
  "ds_read_b64_tr_b16 a[172:173], v77 offset:32768\n"
  "ds_read_b64_tr_b16 a[174:175], v77 offset:34816\n"
  "v_mfma_f32_16x16x32_bf16 v[28:31], a[188:191], a[60:63], v[28:31]\n"
  "s_add_u32 s21, s54, 0x58000\n"
  "buffer_load_dwordx4 a[40:43], v73, s[12:15], s21 offen offset:0 sc1\n"
  "buffer_load_dwordx4 a[44:47], v73, s[12:15], s21 offen offset:1024 sc1\n"
  "s_waitcnt vmcnt(47) lgkmcnt(6)\n"
  "v_mfma_f32_16x16x32_bf16 v[0:3], a[160:163], a[64:67], v[0:3]\n"
  "ds_read_b64_tr_b16 a[176:177], v74 offset:36864\n"
  "ds_read_b64_tr_b16 a[178:179], v74 offset:38912\n"
  "v_mfma_f32_16x16x32_bf16 v[16:19], a[160:163], a[68:71], v[16:19]\n"
  "s_add_u32 s21, s54, 0x5b000\n"
  "buffer_load_dwordx4 a[48:51], v73, s[12:15], s21 offen offset:0 sc1\n"
  "s_waitcnt lgkmcnt(6)\n"
  "v_mfma_f32_16x16x32_bf16 v[4:7], a[164:167], a[64:67], v[4:7]\n"
  "ds_read_b64_tr_b16 a[180:181], v75 offset:36864\n"
  "ds_read_b64_tr_b16 a[182:183], v75 offset:38912\n"
  "v_mfma_f32_16x16x32_bf16 v[20:23], a[164:167], a[68:71], v[20:23]\n"
  "buffer_load_dwordx4 a[52:55], v73, s[12:15], s21 offen offset:1024 sc1\n"
  "s_add_u32 s21, s54, 0x5f000\n"
  "buffer_load_dwordx4 a[56:59], v73, s[12:15], s21 offen offset:0 sc1\n"
  "s_waitcnt lgkmcnt(6)\n"
  "v_mfma_f32_16x16x32_bf16 v[8:11], a[168:171], a[64:67], v[8:11]\n"
  "ds_read_b64_tr_b16 a[184:185], v76 offset:36864\n"
  "ds_read_b64_tr_b16 a[186:187], v76 offset:38912\n"
  "v_mfma_f32_16x16x32_bf16 v[24:27], a[168:171], a[68:71], v[24:27]\n"
  "buffer_load_dwordx4 a[60:63], v73, s[12:15], s21 offen offset:1024 sc1\n"
  "s_waitcnt lgkmcnt(6)\n"
  "v_mfma_f32_16x16x32_bf16 v[12:15], a[172:175], a[64:67], v[12:15]\n"
  "ds_read_b64_tr_b16 a[188:189], v77 offset:36864\n"
  "ds_read_b64_tr_b16 a[190:191], v77 offset:38912\n"
  "v_mfma_f32_16x16x32_bf16 v[28:31], a[172:175], a[68:71], v[28:31]\n"
  "s_waitcnt vmcnt(49) lgkmcnt(6)\n"
  "v_mfma_f32_16x16x32_bf16 v[0:3], a[176:179], a[72:75], v[0:3]\n"
  "ds_read_b64_tr_b16 a[160:161], v74 offset:40960\n"
  "ds_read_b64_tr_b16 a[162:163], v74 offset:43008\n"
  "v_mfma_f32_16x16x32_bf16 v[16:19], a[176:179], a[76:79], v[16:19]\n"
  "s_sub_u32 s21, 0x62000, s54\n"
  "buffer_load_dwordx4 a[64:67], v73, s[12:15], s21 offen offset:0 sc1\n"
  "buffer_load_dwordx4 a[68:71], v73, s[12:15], s21 offen offset:1024 sc1\n"
  "s_waitcnt lgkmcnt(6)\n"
  "v_mfma_f32_16x16x32_bf16 v[4:7], a[180:183], a[72:75], v[4:7]\n"
  "ds_read_b64_tr_b16 a[164:165], v75 offset:40960\n"
  "ds_read_b64_tr_b16 a[166:167], v75 offset:43008\n"
  "v_mfma_f32_16x16x32_bf16 v[20:23], a[180:183], a[76:79], v[20:23]\n"
  "s_waitcnt lgkmcnt(6)\n"
  "v_mfma_f32_16x16x32_bf16 v[8:11], a[184:187], a[72:75], v[8:11]\n"
  "ds_read_b64_tr_b16 a[168:169], v76 offset:40960\n"
  "ds_read_b64_tr_b16 a[170:171], v76 offset:43008\n"
  "v_mfma_f32_16x16x32_bf16 v[24:27], a[184:187], a[76:79], v[24:27]\n"
  "s_waitcnt lgkmcnt(6)\n"
  "v_mfma_f32_16x16x32_bf16 v[12:15], a[188:191], a[72:75], v[12:15]\n"
  "ds_read_b64_tr_b16 a[172:173], v77 offset:40960\n"
  "ds_read_b64_tr_b16 a[174:175], v77 offset:43008\n"
  "v_mfma_f32_16x16x32_bf16 v[28:31], a[188:191], a[76:79], v[28:31]\n"
  "s_waitcnt vmcnt(49) lgkmcnt(6)\n"
  "v_mfma_f32_16x16x32_bf16 v[0:3], a[160:163], a[80:83], v[0:3]\n"
  "ds_read_b64_tr_b16 a[176:177], v74 offset:45056\n"
  "ds_read_b64_tr_b16 a[178:179], v74 offset:47104\n"
  "v_mfma_f32_16x16x32_bf16 v[16:19], a[160:163], a[84:87], v[16:19]\n"
  "global_load_dword v104, v103, s[60:61] sc1\n"
  "s_sub_u32 s21, 0x66000, s54\n"
  "buffer_load_dwordx4 a[72:75], v73, s[12:15], s21 offen offset:0 sc1\n"
  "s_waitcnt lgkmcnt(6)\n"
  "v_mfma_f32_16x16x32_bf16 v[4:7], a[164:167], a[80:83], v[4:7]\n"
  "ds_read_b64_tr_b16 a[180:181], v75 offset:45056\n"
  "ds_read_b64_tr_b16 a[182:183], v75 offset:47104\n"
  "v_mfma_f32_16x16x32_bf16 v[20:23], a[164:167], a[84:87], v[20:23]\n"
  "buffer_load_dwordx4 a[76:79], v73, s[12:15], s21 offen offset:1024 sc1\n"
  "s_waitcnt lgkmcnt(6)\n"
  "v_mfma_f32_16x16x32_bf16 v[8:11], a[168:171], a[80:83], v[8:11]\n"
  "ds_read_b64_tr_b16 a[184:185], v76 offset:45056\n"
  "ds_read_b64_tr_b16 a[186:187], v76 offset:47104\n"
  "v_mfma_f32_16x16x32_bf16 v[24:27], a[168:171], a[84:87], v[24:27]\n"
  "s_waitcnt lgkmcnt(6)\n"
  "v_mfma_f32_16x16x32_bf16 v[12:15], a[172:175], a[80:83], v[12:15]\n"
  "ds_read_b64_tr_b16 a[188:189], v77 offset:45056\n"
  "ds_read_b64_tr_b16 a[190:191], v77 offset:47104\n"
  "v_mfma_f32_16x16x32_bf16 v[28:31], a[172:175], a[84:87], v[28:31]\n"
  "s_barrier\n"
  "s_waitcnt vmcnt(50) lgkmcnt(6)\n"
  "v_mfma_f32_16x16x32_bf16 v[0:3], a[176:179], a[88:91], v[0:3]\n"
  "ds_read_b64_tr_b16 a[160:161], v74 offset:49152\n"
  "ds_read_b64_tr_b16 a[162:163], v74 offset:51200\n"
  "v_mfma_f32_16x16x32_bf16 v[16:19], a[176:179], a[92:95], v[16:19]\n"
  "s_sub_u32 s20, 0x620000, s57\n"
  "s_add_u32 m0, s22, 16384\n"
  "s_nop 0\n"
  "buffer_load_dwordx4 v72, s[8:11], s20 offen lds\n"
  "s_sub_u32 s20, 0x660000, s57\n"
  "s_add_u32 m0, s22, 20480\n"
  "s_nop 0\n"
  "buffer_load_dwordx4 v72, s[8:11], s20 offen lds\n"
  "s_waitcnt lgkmcnt(6)\n"
  "v_mfma_f32_16x16x32_bf16 v[4:7], a[180:183], a[88:91], v[4:7]\n"
  "ds_read_b64_tr_b16 a[164:165], v75 offset:49152\n"
  "ds_read_b64_tr_b16 a[166:167], v75 offset:51200\n"
  "v_mfma_f32_16x16x32_bf16 v[20:23], a[180:183], a[92:95], v[20:23]\n"
  "s_sub_u32 s20, 0x690000, s57\n"
  "s_add_u32 m0, s22, 24576\n"
  "s_nop 0\n"
  "buffer_load_dwordx4 v72, s[8:11], s20 offen lds\n"
  "s_sub_u32 s20, 0x6d0000, s57\n"
  "s_add_u32 m0, s22, 28672\n"
  "s_nop 0\n"
  "buffer_load_dwordx4 v72, s[8:11], s20 offen lds\n"
  "s_waitcnt lgkmcnt(6)\n"
  "v_mfma_f32_16x16x32_bf16 v[8:11], a[184:187], a[88:91], v[8:11]\n"
  "ds_read_b64_tr_b16 a[168:169], v76 offset:49152\n"
  "ds_read_b64_tr_b16 a[170:171], v76 offset:51200\n"
  "v_mfma_f32_16x16x32_bf16 v[24:27], a[184:187], a[92:95], v[24:27]\n"
  "s_sub_u32 s21, 0x69000, s54\n"
  "buffer_load_dwordx4 a[80:83], v73, s[12:15], s21 offen offset:0 sc1\n"
  "s_waitcnt lgkmcnt(6)\n"
  "v_mfma_f32_16x16x32_bf16 v[12:15], a[188:191], a[88:91], v[12:15]\n"
  "ds_read_b64_tr_b16 a[172:173], v77 offset:49152\n"
  "ds_read_b64_tr_b16 a[174:175], v77 offset:51200\n"
  "v_mfma_f32_16x16x32_bf16 v[28:31], a[188:191], a[92:95], v[28:31]\n"
  "buffer_load_dwordx4 a[84:87], v73, s[12:15], s21 offen offset:1024 sc1\n"
  "s_waitcnt vmcnt(54) lgkmcnt(6)\n"
  "v_mfma_f32_16x16x32_bf16 v[0:3], a[160:163], a[96:99], v[0:3]\n"
  "ds_read_b64_tr_b16 a[176:177], v74 offset:53248\n"
  "ds_read_b64_tr_b16 a[178:179], v74 offset:55296\n"
  "v_mfma_f32_16x16x32_bf16 v[16:19], a[160:163], a[100:103], v[16:19]\n"
  "s_sub_u32 s21, 0x6d000, s54\n"
  "buffer_load_dwordx4 a[88:91], v73, s[12:15], s21 offen offset:0 sc1\n"
  "buffer_load_dwordx4 a[92:95], v73, s[12:15], s21 offen offset:1024 sc1\n"
  "s_waitcnt lgkmcnt(6)\n"
  "v_mfma_f32_16x16x32_bf16 v[4:7], a[164:167], a[96:99], v[4:7]\n"
  "ds_read_b64_tr_b16 a[180:181], v75 offset:53248\n"
  "ds_read_b64_tr_b16 a[182:183], v75 offset:55296\n"
  "v_mfma_f32_16x16x32_bf16 v[20:23], a[164:167], a[100:103], v[20:23]\n"
  "s_waitcnt lgkmcnt(6)\n"
  "v_mfma_f32_16x16x32_bf16 v[8:11], a[168:171], a[96:99], v[8:11]\n"
  "ds_read_b64_tr_b16 a[184:185], v76 offset:53248\n"
  "ds_read_b64_tr_b16 a[186:187], v76 offset:55296\n"
  "v_mfma_f32_16x16x32_bf16 v[24:27], a[168:171], a[100:103], v[24:27]\n"
  "s_waitcnt lgkmcnt(6)\n"
  "v_mfma_f32_16x16x32_bf16 v[12:15], a[172:175], a[96:99], v[12:15]\n"
  "ds_read_b64_tr_b16 a[188:189], v77 offset:53248\n"
  "ds_read_b64_tr_b16 a[190:191], v77 offset:55296\n"
  "v_mfma_f32_16x16x32_bf16 v[28:31], a[172:175], a[100:103], v[28:31]\n"
  "s_waitcnt vmcnt(45) lgkmcnt(6)\n"
  "v_mfma_f32_16x16x32_bf16 v[0:3], a[176:179], a[104:107], v[0:3]\n"
  "ds_read_b64_tr_b16 a[160:161], v74 offset:57344\n"
  "ds_read_b64_tr_b16 a[162:163], v74 offset:59392\n"
  "v_mfma_f32_16x16x32_bf16 v[16:19], a[176:179], a[108:111], v[16:19]\n"
  "s_waitcnt lgkmcnt(6)\n"
  "v_mfma_f32_16x16x32_bf16 v[4:7], a[180:183], a[104:107], v[4:7]\n"
  "ds_read_b64_tr_b16 a[164:165], v75 offset:57344\n"
  "ds_read_b64_tr_b16 a[166:167], v75 offset:59392\n"
  "v_mfma_f32_16x16x32_bf16 v[20:23], a[180:183], a[108:111], v[20:23]\n"
  "s_waitcnt lgkmcnt(6)\n"
  "v_mfma_f32_16x16x32_bf16 v[8:11], a[184:187], a[104:107], v[8:11]\n"
  "ds_read_b64_tr_b16 a[168:169], v76 offset:57344\n"
  "ds_read_b64_tr_b16 a[170:171], v76 offset:59392\n"
  "v_mfma_f32_16x16x32_bf16 v[24:27], a[184:187], a[108:111], v[24:27]\n"
  "s_waitcnt lgkmcnt(6)\n"
  "v_mfma_f32_16x16x32_bf16 v[12:15], a[188:191], a[104:107], v[12:15]\n"
  "ds_read_b64_tr_b16 a[172:173], v77 offset:57344\n"
  "ds_read_b64_tr_b16 a[174:175], v77 offset:59392\n"
  "v_mfma_f32_16x16x32_bf16 v[28:31], a[188:191], a[108:111], v[28:31]\n"
  "s_waitcnt vmcnt(43) lgkmcnt(6)\n"
  "v_mfma_f32_16x16x32_bf16 v[0:3], a[160:163], a[112:115], v[0:3]\n"
  "ds_read_b64_tr_b16 a[176:177], v74 offset:61440\n"
  "ds_read_b64_tr_b16 a[178:179], v74 offset:63488\n"
  "v_mfma_f32_16x16x32_bf16 v[16:19], a[160:163], a[116:119], v[16:19]\n"
  "s_waitcnt lgkmcnt(6)\n"
  "v_mfma_f32_16x16x32_bf16 v[4:7], a[164:167], a[112:115], v[4:7]\n"
  "ds_read_b64_tr_b16 a[180:181], v75 offset:61440\n"
  "ds_read_b64_tr_b16 a[182:183], v75 offset:63488\n"
  "v_mfma_f32_16x16x32_bf16 v[20:23], a[164:167], a[116:119], v[20:23]\n"
  "s_waitcnt lgkmcnt(6)\n"
  "v_mfma_f32_16x16x32_bf16 v[8:11], a[168:171], a[112:115], v[8:11]\n"
  "ds_read_b64_tr_b16 a[184:185], v76 offset:61440\n"
  "ds_read_b64_tr_b16 a[186:187], v76 offset:63488\n"
  "v_mfma_f32_16x16x32_bf16 v[24:27], a[168:171], a[116:119], v[24:27]\n"
  "s_waitcnt lgkmcnt(6)\n"
  "v_mfma_f32_16x16x32_bf16 v[12:15], a[172:175], a[112:115], v[12:15]\n"
  "ds_read_b64_tr_b16 a[188:189], v77 offset:61440\n"
  "ds_read_b64_tr_b16 a[190:191], v77 offset:63488\n"
  "v_mfma_f32_16x16x32_bf16 v[28:31], a[172:175], a[116:119], v[28:31]\n"
  "s_barrier\n"
  "s_waitcnt vmcnt(41) lgkmcnt(6)\n"
  "v_mfma_f32_16x16x32_bf16 v[0:3], a[176:179], a[120:123], v[0:3]\n"
  "ds_read_b64_tr_b16 a[160:161], v80 offset:0\n"
  "ds_read_b64_tr_b16 a[162:163], v80 offset:2048\n"
  "v_mfma_f32_16x16x32_bf16 v[16:19], a[176:179], a[124:127], v[16:19]\n"
  "s_add_u32 s20, s57, 0x10000\n"
  "s_add_u32 m0, s22, 32768\n"
  "s_nop 0\n"
  "buffer_load_dwordx4 v72, s[8:11], s20 offen lds\n"
  "s_add_u32 s20, s57, 0x50000\n"
  "s_add_u32 m0, s22, 36864\n"
  "s_nop 0\n"
  "buffer_load_dwordx4 v72, s[8:11], s20 offen lds\n"
  "s_waitcnt lgkmcnt(6)\n"
  "v_mfma_f32_16x16x32_bf16 v[4:7], a[180:183], a[120:123], v[4:7]\n"
  "ds_read_b64_tr_b16 a[164:165], v81 offset:0\n"
  "ds_read_b64_tr_b16 a[166:167], v81 offset:2048\n"
  "v_mfma_f32_16x16x32_bf16 v[20:23], a[180:183], a[124:127], v[20:23]\n"
  "s_add_u32 s20, s57, 0x80000\n"
  "s_add_u32 m0, s22, 40960\n"
  "s_nop 0\n"
  "buffer_load_dwordx4 v72, s[8:11], s20 offen lds\n"
  "s_add_u32 s20, s57, 0xc0000\n"
  "s_add_u32 m0, s22, 45056\n"
  "s_nop 0\n"
  "buffer_load_dwordx4 v72, s[8:11], s20 offen lds\n"
  "s_waitcnt lgkmcnt(6)\n"
  "v_mfma_f32_16x16x32_bf16 v[8:11], a[184:187], a[120:123], v[8:11]\n"
  "ds_read_b64_tr_b16 a[168:169], v82 offset:0\n"
  "ds_read_b64_tr_b16 a[170:171], v82 offset:2048\n"
  "v_mfma_f32_16x16x32_bf16 v[24:27], a[184:187], a[124:127], v[24:27]\n"
  "s_waitcnt lgkmcnt(6)\n"
  "v_mfma_f32_16x16x32_bf16 v[12:15], a[188:191], a[120:123], v[12:15]\n"
  "ds_read_b64_tr_b16 a[172:173], v83 offset:0\n"
  "ds_read_b64_tr_b16 a[174:175], v83 offset:2048\n"
  "v_mfma_f32_16x16x32_bf16 v[28:31], a[188:191], a[124:127], v[28:31]\n"
  "s_waitcnt vmcnt(14)\n"
  "v_cmp_ne_u32 vcc, 1.0, v104\n"
  "s_nop 4\n"
  "s_cbranch_vccz 60f\n"
  "s_mov_b32 s64, 0\n"
  "50:\n"
  "s_sleep 1\n"
  "global_load_dword v104, v103, s[60:61] sc1\n"
  "s_waitcnt vmcnt(0)\n"
  "v_cmp_ne_u32 vcc, 1.0, v104\n"
  "s_nop 4\n"
  "s_cbranch_vccz 60f\n"
  "s_add_u32 s64, s64, 1\n"
  "s_cmp_lt_u32 s64, 0x40000\n"
  "s_cbranch_scc1 50b\n"
  "60:\n"
  "s_mov_b64 exec, 15\n"
  "global_store_dword v107, v108, s[74:75] sc1\n"
  "s_mov_b64 exec, -1\n"
  "s_waitcnt lgkmcnt(6)\n"
  "v_mfma_f32_16x16x32_bf16 v[0:3], a[160:163], a[128:131], v[0:3]\n"
  "ds_read_b64_tr_b16 a[176:177], v80 offset:4096\n"
  "ds_read_b64_tr_b16 a[178:179], v80 offset:6144\n"
  "v_mfma_f32_16x16x32_bf16 v[16:19], a[160:163], a[132:135], v[16:19]\n"
  "s_add_u32 s21, s54, 0x1000\n"
  "buffer_load_dwordx4 a[96:99], v73, s[12:15], s21 offen offset:0 sc1\n"
  "buffer_load_dwordx4 a[100:103], v73, s[12:15], s21 offen offset:1024 sc1\n"
  "s_waitcnt lgkmcnt(6)\n"
  "v_mfma_f32_16x16x32_bf16 v[4:7], a[164:167], a[128:131], v[4:7]\n"
  "ds_read_b64_tr_b16 a[180:181], v81 offset:4096\n"
  "ds_read_b64_tr_b16 a[182:183], v81 offset:6144\n"
  "v_mfma_f32_16x16x32_bf16 v[20:23], a[164:167], a[132:135], v[20:23]\n"
  "s_add_u32 s21, s54, 0x5000\n"
  "buffer_load_dwordx4 a[104:107], v73, s[12:15], s21 offen offset:0 sc1\n"
  "buffer_load_dwordx4 a[108:111], v73, s[12:15], s21 offen offset:1024 sc1\n"
  "s_waitcnt lgkmcnt(6)\n"
  "v_mfma_f32_16x16x32_bf16 v[8:11], a[168:171], a[128:131], v[8:11]\n"
  "ds_read_b64_tr_b16 a[184:185], v82 offset:4096\n"
  "ds_read_b64_tr_b16 a[186:187], v82 offset:6144\n"
  "v_mfma_f32_16x16x32_bf16 v[24:27], a[168:171], a[132:135], v[24:27]\n"
  "s_add_u32 s21, s54, 0x8000\n"
  "buffer_load_dwordx4 a[112:115], v73, s[12:15], s21 offen offset:0 sc1\n"
  "s_waitcnt lgkmcnt(6)\n"
  "v_mfma_f32_16x16x32_bf16 v[12:15], a[172:175], a[128:131], v[12:15]\n"
  "ds_read_b64_tr_b16 a[188:189], v83 offset:4096\n"
  "ds_read_b64_tr_b16 a[190:191], v83 offset:6144\n"
  "v_mfma_f32_16x16x32_bf16 v[28:31], a[172:175], a[132:135], v[28:31]\n"
  "buffer_load_dwordx4 a[116:119], v73, s[12:15], s21 offen offset:1024 sc1\n"
  "s_add_u32 s21, s54, 0xc000\n"
  "buffer_load_dwordx4 a[120:123], v73, s[12:15], s21 offen offset:0 sc1\n"
  "s_waitcnt lgkmcnt(6)\n"
  "v_mfma_f32_16x16x32_bf16 v[0:3], a[176:179], a[136:139], v[0:3]\n"
  "ds_read_b64_tr_b16 a[160:161], v80 offset:8192\n"
  "ds_read_b64_tr_b16 a[162:163], v80 offset:10240\n"
  "v_mfma_f32_16x16x32_bf16 v[16:19], a[176:179], a[140:143], v[16:19]\n"
  "buffer_load_dwordx4 a[124:127], v73, s[12:15], s21 offen offset:1024 sc1\n"
  "s_waitcnt lgkmcnt(6)\n"
  "v_mfma_f32_16x16x32_bf16 v[4:7], a[180:183], a[136:139], v[4:7]\n"
  "ds_read_b64_tr_b16 a[164:165], v81 offset:8192\n"
  "ds_read_b64_tr_b16 a[166:167], v81 offset:10240\n"
  "v_mfma_f32_16x16x32_bf16 v[20:23], a[180:183], a[140:143], v[20:23]\n"
  "s_sub_u32 s21, 0xf000, s54\n"
  "buffer_load_dwordx4 a[128:131], v73, s[12:15], s21 offen offset:0 sc1\n"
  "buffer_load_dwordx4 a[132:135], v73, s[12:15], s21 offen offset:1024 sc1\n"
  "s_waitcnt lgkmcnt(6)\n"
  "v_mfma_f32_16x16x32_bf16 v[8:11], a[184:187], a[136:139], v[8:11]\n"
  "ds_read_b64_tr_b16 a[168:169], v82 offset:8192\n"
  "ds_read_b64_tr_b16 a[170:171], v82 offset:10240\n"
  "v_mfma_f32_16x16x32_bf16 v[24:27], a[184:187], a[140:143], v[24:27]\n"
  "s_waitcnt lgkmcnt(6)\n"
  "v_mfma_f32_16x16x32_bf16 v[12:15], a[188:191], a[136:139], v[12:15]\n"
  "ds_read_b64_tr_b16 a[172:173], v83 offset:8192\n"
  "ds_read_b64_tr_b16 a[174:175], v83 offset:10240\n"
  "v_mfma_f32_16x16x32_bf16 v[28:31], a[188:191], a[140:143], v[28:31]\n"
  "s_waitcnt lgkmcnt(6)\n"
  "v_mfma_f32_16x16x32_bf16 v[0:3], a[160:163], a[144:147], v[0:3]\n"
  "ds_read_b64_tr_b16 a[176:177], v80 offset:12288\n"
  "ds_read_b64_tr_b16 a[178:179], v80 offset:14336\n"
  "v_mfma_f32_16x16x32_bf16 v[16:19], a[160:163], a[148:151], v[16:19]\n"
  "s_sub_u32 s21, 0x13000, s54\n"
  "buffer_load_dwordx4 a[136:139], v73, s[12:15], s21 offen offset:0 sc1\n"
  "buffer_load_dwordx4 a[140:143], v73, s[12:15], s21 offen offset:1024 sc1\n"
  "s_waitcnt lgkmcnt(6)\n"
  "v_mfma_f32_16x16x32_bf16 v[4:7], a[164:167], a[144:147], v[4:7]\n"
  "ds_read_b64_tr_b16 a[180:181], v81 offset:12288\n"
  "ds_read_b64_tr_b16 a[182:183], v81 offset:14336\n"
  "v_mfma_f32_16x16x32_bf16 v[20:23], a[164:167], a[148:151], v[20:23]\n"
  "s_waitcnt lgkmcnt(6)\n"
  "v_mfma_f32_16x16x32_bf16 v[8:11], a[168:171], a[144:147], v[8:11]\n"
  "ds_read_b64_tr_b16 a[184:185], v82 offset:12288\n"
  "ds_read_b64_tr_b16 a[186:187], v82 offset:14336\n"
  "v_mfma_f32_16x16x32_bf16 v[24:27], a[168:171], a[148:151], v[24:27]\n"
  "s_waitcnt lgkmcnt(6)\n"
  "v_mfma_f32_16x16x32_bf16 v[12:15], a[172:175], a[144:147], v[12:15]\n"
  "ds_read_b64_tr_b16 a[188:189], v83 offset:12288\n"
  "ds_read_b64_tr_b16 a[190:191], v83 offset:14336\n"
  "v_mfma_f32_16x16x32_bf16 v[28:31], a[172:175], a[148:151], v[28:31]\n"
  "s_barrier\n"
  "s_waitcnt lgkmcnt(6)\n"
  "v_mfma_f32_16x16x32_bf16 v[0:3], a[176:179], a[152:155], v[0:3]\n"
  "ds_read_b64_tr_b16 a[160:161], v80 offset:16384\n"
  "ds_read_b64_tr_b16 a[162:163], v80 offset:18432\n"
  "v_mfma_f32_16x16x32_bf16 v[16:19], a[176:179], a[156:159], v[16:19]\n"
  "s_sub_u32 s20, 0xf0000, s57\n"
  "s_add_u32 m0, s22, 49152\n"
  "s_nop 0\n"
  "buffer_load_dwordx4 v72, s[8:11], s20 offen lds\n"
  "s_sub_u32 s20, 0x130000, s57\n"
  "s_add_u32 m0, s22, 53248\n"
  "s_nop 0\n"
  "buffer_load_dwordx4 v72, s[8:11], s20 offen lds\n"
  "s_waitcnt lgkmcnt(6)\n"
  "v_mfma_f32_16x16x32_bf16 v[4:7], a[180:183], a[152:155], v[4:7]\n"
  "ds_read_b64_tr_b16 a[164:165], v81 offset:16384\n"
  "ds_read_b64_tr_b16 a[166:167], v81 offset:18432\n"
  "v_mfma_f32_16x16x32_bf16 v[20:23], a[180:183], a[156:159], v[20:23]\n"
  "s_sub_u32 s20, 0x160000, s57\n"
  "s_add_u32 m0, s22, 57344\n"
  "s_nop 0\n"
  "buffer_load_dwordx4 v72, s[8:11], s20 offen lds\n"
  "s_sub_u32 s20, 0x1a0000, s57\n"
  "s_add_u32 m0, s22, 61440\n"
  "s_nop 0\n"
  "buffer_load_dwordx4 v72, s[8:11], s20 offen lds\n"
  "s_waitcnt lgkmcnt(6)\n"
  "v_mfma_f32_16x16x32_bf16 v[8:11], a[184:187], a[152:155], v[8:11]\n"
  "ds_read_b64_tr_b16 a[168:169], v82 offset:16384\n"
  "ds_read_b64_tr_b16 a[170:171], v82 offset:18432\n"
  "v_mfma_f32_16x16x32_bf16 v[24:27], a[184:187], a[156:159], v[24:27]\n"
  "s_sub_u32 s21, 0x16000, s54\n"
  "buffer_load_dwordx4 a[144:147], v73, s[12:15], s21 offen offset:0 sc1\n"
  "s_waitcnt lgkmcnt(6)\n"
  "v_mfma_f32_16x16x32_bf16 v[12:15], a[188:191], a[152:155], v[12:15]\n"
  "ds_read_b64_tr_b16 a[172:173], v83 offset:16384\n"
  "ds_read_b64_tr_b16 a[174:175], v83 offset:18432\n"
  "v_mfma_f32_16x16x32_bf16 v[28:31], a[188:191], a[156:159], v[28:31]\n"
  "buffer_load_dwordx4 a[148:151], v73, s[12:15], s21 offen offset:1024 sc1\n"
  "s_waitcnt lgkmcnt(6)\n"
  "v_mfma_f32_16x16x32_bf16 v[0:3], a[160:163], a[0:3], v[0:3]\n"
  "ds_read_b64_tr_b16 a[176:177], v80 offset:20480\n"
  "ds_read_b64_tr_b16 a[178:179], v80 offset:22528\n"
  "v_mfma_f32_16x16x32_bf16 v[16:19], a[160:163], a[4:7], v[16:19]\n"
  "s_sub_u32 s21, 0x1a000, s54\n"
  "buffer_load_dwordx4 a[152:155], v73, s[12:15], s21 offen offset:0 sc1\n"
  "buffer_load_dwordx4 a[156:159], v73, s[12:15], s21 offen offset:1024 sc1\n"
  "s_waitcnt lgkmcnt(6)\n"
  "v_mfma_f32_16x16x32_bf16 v[4:7], a[164:167], a[0:3], v[4:7]\n"
  "ds_read_b64_tr_b16 a[180:181], v81 offset:20480\n"
  "ds_read_b64_tr_b16 a[182:183], v81 offset:22528\n"
  "v_mfma_f32_16x16x32_bf16 v[20:23], a[164:167], a[4:7], v[20:23]\n"
  "s_waitcnt lgkmcnt(6)\n"
  "v_mfma_f32_16x16x32_bf16 v[8:11], a[168:171], a[0:3], v[8:11]\n"
  "ds_read_b64_tr_b16 a[184:185], v82 offset:20480\n"
  "ds_read_b64_tr_b16 a[186:187], v82 offset:22528\n"
  "v_mfma_f32_16x16x32_bf16 v[24:27], a[168:171], a[4:7], v[24:27]\n"
  "s_waitcnt lgkmcnt(6)\n"
  "v_mfma_f32_16x16x32_bf16 v[12:15], a[172:175], a[0:3], v[12:15]\n"
  "ds_read_b64_tr_b16 a[188:189], v83 offset:20480\n"
  "ds_read_b64_tr_b16 a[190:191], v83 offset:22528\n"
  "v_mfma_f32_16x16x32_bf16 v[28:31], a[172:175], a[4:7], v[28:31]\n"
  "s_waitcnt lgkmcnt(6)\n"
  "v_mfma_f32_16x16x32_bf16 v[0:3], a[176:179], a[8:11], v[0:3]\n"
  "ds_read_b64_tr_b16 a[160:161], v80 offset:24576\n"
  "ds_read_b64_tr_b16 a[162:163], v80 offset:26624\n"
  "v_mfma_f32_16x16x32_bf16 v[16:19], a[176:179], a[12:15], v[16:19]\n"
  "s_add_u32 s21, s54, 0x1d000\n"
  "buffer_load_dwordx4 a[0:3], v73, s[12:15], s21 offen offset:0 sc1\n"
  "buffer_load_dwordx4 a[4:7], v73, s[12:15], s21 offen offset:1024 sc1\n"
  "s_waitcnt lgkmcnt(6)\n"
  "v_mfma_f32_16x16x32_bf16 v[4:7], a[180:183], a[8:11], v[4:7]\n"
  "ds_read_b64_tr_b16 a[164:165], v81 offset:24576\n"
  "ds_read_b64_tr_b16 a[166:167], v81 offset:26624\n"
  "v_mfma_f32_16x16x32_bf16 v[20:23], a[180:183], a[12:15], v[20:23]\n"
  "s_waitcnt lgkmcnt(6)\n"
  "v_mfma_f32_16x16x32_bf16 v[8:11], a[184:187], a[8:11], v[8:11]\n"
  "ds_read_b64_tr_b16 a[168:169], v82 offset:24576\n"
  "ds_read_b64_tr_b16 a[170:171], v82 offset:26624\n"
  "v_mfma_f32_16x16x32_bf16 v[24:27], a[184:187], a[12:15], v[24:27]\n"
  "s_waitcnt lgkmcnt(6)\n"
  "v_mfma_f32_16x16x32_bf16 v[12:15], a[188:191], a[8:11], v[12:15]\n"
  "ds_read_b64_tr_b16 a[172:173], v83 offset:24576\n"
  "ds_read_b64_tr_b16 a[174:175], v83 offset:26624\n"
  "v_mfma_f32_16x16x32_bf16 v[28:31], a[188:191], a[12:15], v[28:31]\n"
  "s_waitcnt lgkmcnt(6)\n"
  "v_mfma_f32_16x16x32_bf16 v[0:3], a[160:163], a[16:19], v[0:3]\n"
  "ds_read_b64_tr_b16 a[176:177], v80 offset:28672\n"
  "ds_read_b64_tr_b16 a[178:179], v80 offset:30720\n"
  "v_mfma_f32_16x16x32_bf16 v[16:19], a[160:163], a[20:23], v[16:19]\n"
  "s_add_u32 s21, s54, 0x21000\n"
  "buffer_load_dwordx4 a[8:11], v73, s[12:15], s21 offen offset:0 sc1\n"
  "buffer_load_dwordx4 a[12:15], v73, s[12:15], s21 offen offset:1024 sc1\n"
  "s_waitcnt lgkmcnt(6)\n"
  "v_mfma_f32_16x16x32_bf16 v[4:7], a[164:167], a[16:19], v[4:7]\n"
  "ds_read_b64_tr_b16 a[180:181], v81 offset:28672\n"
  "ds_read_b64_tr_b16 a[182:183], v81 offset:30720\n"
  "v_mfma_f32_16x16x32_bf16 v[20:23], a[164:167], a[20:23], v[20:23]\n"
  "s_waitcnt lgkmcnt(6)\n"
  "v_mfma_f32_16x16x32_bf16 v[8:11], a[168:171], a[16:19], v[8:11]\n"
  "ds_read_b64_tr_b16 a[184:185], v82 offset:28672\n"
  "ds_read_b64_tr_b16 a[186:187], v82 offset:30720\n"
  "v_mfma_f32_16x16x32_bf16 v[24:27], a[168:171], a[20:23], v[24:27]\n"
  "s_waitcnt lgkmcnt(6)\n"
  "v_mfma_f32_16x16x32_bf16 v[12:15], a[172:175], a[16:19], v[12:15]\n"
  "ds_read_b64_tr_b16 a[188:189], v83 offset:28672\n"
  "ds_read_b64_tr_b16 a[190:191], v83 offset:30720\n"
  "v_mfma_f32_16x16x32_bf16 v[28:31], a[172:175], a[20:23], v[28:31]\n"
  "s_barrier\n"
  "s_waitcnt lgkmcnt(6)\n"
  "v_mfma_f32_16x16x32_bf16 v[0:3], a[176:179], a[24:27], v[0:3]\n"
  "ds_read_b64_tr_b16 a[160:161], v74 offset:0\n"
  "ds_read_b64_tr_b16 a[162:163], v74 offset:2048\n"
  "v_mfma_f32_16x16x32_bf16 v[16:19], a[176:179], a[28:31], v[16:19]\n"
  "s_add_u32 s20, s57, 0x1d0000\n"
  "s_add_u32 m0, s22, 65536\n"
  "s_nop 0\n"
  "buffer_load_dwordx4 v72, s[8:11], s20 offen lds\n"
  "s_add_u32 s20, s57, 0x210000\n"
  "s_add_u32 m0, s22, 69632\n"
  "s_nop 0\n"
  "buffer_load_dwordx4 v72, s[8:11], s20 offen lds\n"
  "s_waitcnt lgkmcnt(6)\n"
  "v_mfma_f32_16x16x32_bf16 v[4:7], a[180:183], a[24:27], v[4:7]\n"
  "ds_read_b64_tr_b16 a[164:165], v75 offset:0\n"
  "ds_read_b64_tr_b16 a[166:167], v75 offset:2048\n"
  "v_mfma_f32_16x16x32_bf16 v[20:23], a[180:183], a[28:31], v[20:23]\n"
  "s_add_u32 s20, s57, 0x240000\n"
  "s_add_u32 m0, s22, 73728\n"
  "s_nop 0\n"
  "buffer_load_dwordx4 v72, s[8:11], s20 offen lds\n"
  "s_add_u32 s20, s57, 0x280000\n"
  "s_add_u32 m0, s22, 77824\n"
  "s_nop 0\n"
  "buffer_load_dwordx4 v72, s[8:11], s20 offen lds\n"
  "s_waitcnt lgkmcnt(6)\n"
  "v_mfma_f32_16x16x32_bf16 v[8:11], a[184:187], a[24:27], v[8:11]\n"
  "ds_read_b64_tr_b16 a[168:169], v76 offset:0\n"
  "ds_read_b64_tr_b16 a[170:171], v76 offset:2048\n"
  "v_mfma_f32_16x16x32_bf16 v[24:27], a[184:187], a[28:31], v[24:27]\n"
  "s_add_u32 s21, s54, 0x24000\n"
  "buffer_load_dwordx4 a[16:19], v73, s[12:15], s21 offen offset:0 sc1\n"
  "s_waitcnt lgkmcnt(6)\n"
  "v_mfma_f32_16x16x32_bf16 v[12:15], a[188:191], a[24:27], v[12:15]\n"
  "ds_read_b64_tr_b16 a[172:173], v77 offset:0\n"
  "ds_read_b64_tr_b16 a[174:175], v77 offset:2048\n"
  "v_mfma_f32_16x16x32_bf16 v[28:31], a[188:191], a[28:31], v[28:31]\n"
  "buffer_load_dwordx4 a[20:23], v73, s[12:15], s21 offen offset:1024 sc1\n"
  "s_waitcnt lgkmcnt(6)\n"
  "v_mfma_f32_16x16x32_bf16 v[0:3], a[160:163], a[32:35], v[0:3]\n"
  "ds_read_b64_tr_b16 a[176:177], v74 offset:4096\n"
  "ds_read_b64_tr_b16 a[178:179], v74 offset:6144\n"
  "v_mfma_f32_16x16x32_bf16 v[16:19], a[160:163], a[36:39], v[16:19]\n"
  "s_add_u32 s21, s54, 0x28000\n"
  "buffer_load_dwordx4 a[24:27], v73, s[12:15], s21 offen offset:0 sc1\n"
  "buffer_load_dwordx4 a[28:31], v73, s[12:15], s21 offen offset:1024 sc1\n"
  "s_waitcnt lgkmcnt(6)\n"
  "v_mfma_f32_16x16x32_bf16 v[4:7], a[164:167], a[32:35], v[4:7]\n"
  "ds_read_b64_tr_b16 a[180:181], v75 offset:4096\n"
  "ds_read_b64_tr_b16 a[182:183], v75 offset:6144\n"
  "v_mfma_f32_16x16x32_bf16 v[20:23], a[164:167], a[36:39], v[20:23]\n"
  "s_waitcnt lgkmcnt(6)\n"
  "v_mfma_f32_16x16x32_bf16 v[8:11], a[168:171], a[32:35], v[8:11]\n"
  "ds_read_b64_tr_b16 a[184:185], v76 offset:4096\n"
  "ds_read_b64_tr_b16 a[186:187], v76 offset:6144\n"
  "v_mfma_f32_16x16x32_bf16 v[24:27], a[168:171], a[36:39], v[24:27]\n"
  "s_waitcnt lgkmcnt(6)\n"
  "v_mfma_f32_16x16x32_bf16 v[12:15], a[172:175], a[32:35], v[12:15]\n"
  "ds_read_b64_tr_b16 a[188:189], v77 offset:4096\n"
  "ds_read_b64_tr_b16 a[190:191], v77 offset:6144\n"
  "v_mfma_f32_16x16x32_bf16 v[28:31], a[172:175], a[36:39], v[28:31]\n"
  "s_waitcnt lgkmcnt(6)\n"
  "v_mfma_f32_16x16x32_bf16 v[0:3], a[176:179], a[40:43], v[0:3]\n"
  "ds_read_b64_tr_b16 a[160:161], v74 offset:8192\n"
  "ds_read_b64_tr_b16 a[162:163], v74 offset:10240\n"
  "v_mfma_f32_16x16x32_bf16 v[16:19], a[176:179], a[44:47], v[16:19]\n"
  "s_sub_u32 s21, 0x2b000, s54\n"
  "buffer_load_dwordx4 a[32:35], v73, s[12:15], s21 offen offset:0 sc1\n"
  "buffer_load_dwordx4 a[36:39], v73, s[12:15], s21 offen offset:1024 sc1\n"
  "s_waitcnt lgkmcnt(6)\n"
  "v_mfma_f32_16x16x32_bf16 v[4:7], a[180:183], a[40:43], v[4:7]\n"
  "ds_read_b64_tr_b16 a[164:165], v75 offset:8192\n"
  "ds_read_b64_tr_b16 a[166:167], v75 offset:10240\n"
  "v_mfma_f32_16x16x32_bf16 v[20:23], a[180:183], a[44:47], v[20:23]\n"
  "s_waitcnt lgkmcnt(6)\n"
  "v_mfma_f32_16x16x32_bf16 v[8:11], a[184:187], a[40:43], v[8:11]\n"
  "ds_read_b64_tr_b16 a[168:169], v76 offset:8192\n"
  "ds_read_b64_tr_b16 a[170:171], v76 offset:10240\n"
  "v_mfma_f32_16x16x32_bf16 v[24:27], a[184:187], a[44:47], v[24:27]\n"
  "s_waitcnt lgkmcnt(6)\n"
  "v_mfma_f32_16x16x32_bf16 v[12:15], a[188:191], a[40:43], v[12:15]\n"
  "ds_read_b64_tr_b16 a[172:173], v77 offset:8192\n"
  "ds_read_b64_tr_b16 a[174:175], v77 offset:10240\n"
  "v_mfma_f32_16x16x32_bf16 v[28:31], a[188:191], a[44:47], v[28:31]\n"
  "s_waitcnt lgkmcnt(6)\n"
  "v_mfma_f32_16x16x32_bf16 v[0:3], a[160:163], a[48:51], v[0:3]\n"
  "ds_read_b64_tr_b16 a[176:177], v74 offset:12288\n"
  "ds_read_b64_tr_b16 a[178:179], v74 offset:14336\n"
  "v_mfma_f32_16x16x32_bf16 v[16:19], a[160:163], a[52:55], v[16:19]\n"
  "s_sub_u32 s21, 0x2f000, s54\n"
  "buffer_load_dwordx4 a[40:43], v73, s[12:15], s21 offen offset:0 sc1\n"
  "buffer_load_dwordx4 a[44:47], v73, s[12:15], s21 offen offset:1024 sc1\n"
  "s_waitcnt lgkmcnt(6)\n"
  "v_mfma_f32_16x16x32_bf16 v[4:7], a[164:167], a[48:51], v[4:7]\n"
  "ds_read_b64_tr_b16 a[180:181], v75 offset:12288\n"
  "ds_read_b64_tr_b16 a[182:183], v75 offset:14336\n"
  "v_mfma_f32_16x16x32_bf16 v[20:23], a[164:167], a[52:55], v[20:23]\n"
  "s_waitcnt lgkmcnt(6)\n"
  "v_mfma_f32_16x16x32_bf16 v[8:11], a[168:171], a[48:51], v[8:11]\n"
  "ds_read_b64_tr_b16 a[184:185], v76 offset:12288\n"
  "ds_read_b64_tr_b16 a[186:187], v76 offset:14336\n"
  "v_mfma_f32_16x16x32_bf16 v[24:27], a[168:171], a[52:55], v[24:27]\n"
  "s_waitcnt lgkmcnt(6)\n"
  "v_mfma_f32_16x16x32_bf16 v[12:15], a[172:175], a[48:51], v[12:15]\n"
  "ds_read_b64_tr_b16 a[188:189], v77 offset:12288\n"
  "ds_read_b64_tr_b16 a[190:191], v77 offset:14336\n"
  "v_mfma_f32_16x16x32_bf16 v[28:31], a[172:175], a[52:55], v[28:31]\n"
  "s_waitcnt vmcnt(45)\n"
  "s_barrier\n"
  "s_waitcnt lgkmcnt(6)\n"
  "v_mfma_f32_16x16x32_bf16 v[0:3], a[176:179], a[56:59], v[0:3]\n"
  "ds_read_b64_tr_b16 a[160:161], v74 offset:16384\n"
  "ds_read_b64_tr_b16 a[162:163], v74 offset:18432\n"
  "v_mfma_f32_16x16x32_bf16 v[16:19], a[176:179], a[60:63], v[16:19]\n"
  "s_sub_u32 s20, 0x2b0000, s57\n"
  "s_add_u32 m0, s22, 81920\n"
  "s_nop 0\n"
  "buffer_load_dwordx4 v72, s[8:11], s20 offen lds\n"
  "s_sub_u32 s20, 0x2f0000, s57\n"
  "s_add_u32 m0, s22, 86016\n"
  "s_nop 0\n"
  "buffer_load_dwordx4 v72, s[8:11], s20 offen lds\n"
  "s_waitcnt lgkmcnt(6)\n"
  "v_mfma_f32_16x16x32_bf16 v[4:7], a[180:183], a[56:59], v[4:7]\n"
  "ds_read_b64_tr_b16 a[164:165], v75 offset:16384\n"
  "ds_read_b64_tr_b16 a[166:167], v75 offset:18432\n"
  "v_mfma_f32_16x16x32_bf16 v[20:23], a[180:183], a[60:63], v[20:23]\n"
  "s_sub_u32 s20, 0x320000, s57\n"
  "s_add_u32 m0, s22, 90112\n"
  "s_nop 0\n"
  "buffer_load_dwordx4 v72, s[8:11], s20 offen lds\n"
  "s_sub_u32 s20, 0x360000, s57\n"
  "s_add_u32 m0, s22, 94208\n"
  "s_nop 0\n"
  "buffer_load_dwordx4 v72, s[8:11], s20 offen lds\n"
  "s_waitcnt lgkmcnt(6)\n"
  "v_mfma_f32_16x16x32_bf16 v[8:11], a[184:187], a[56:59], v[8:11]\n"
  "ds_read_b64_tr_b16 a[168:169], v76 offset:16384\n"
  "ds_read_b64_tr_b16 a[170:171], v76 offset:18432\n"
  "v_mfma_f32_16x16x32_bf16 v[24:27], a[184:187], a[60:63], v[24:27]\n"
  "s_sub_u32 s21, 0x32000, s54\n"
  "buffer_load_dwordx4 a[48:51], v73, s[12:15], s21 offen offset:0 sc1\n"
  "s_waitcnt lgkmcnt(6)\n"
  "v_mfma_f32_16x16x32_bf16 v[12:15], a[188:191], a[56:59], v[12:15]\n"
  "ds_read_b64_tr_b16 a[172:173], v77 offset:16384\n"
  "ds_read_b64_tr_b16 a[174:175], v77 offset:18432\n"
  "v_mfma_f32_16x16x32_bf16 v[28:31], a[188:191], a[60:63], v[28:31]\n"
  "buffer_load_dwordx4 a[52:55], v73, s[12:15], s21 offen offset:1024 sc1\n"
  "s_waitcnt lgkmcnt(6)\n"
  "v_mfma_f32_16x16x32_bf16 v[0:3], a[160:163], a[64:67], v[0:3]\n"
  "ds_read_b64_tr_b16 a[176:177], v74 offset:20480\n"
  "ds_read_b64_tr_b16 a[178:179], v74 offset:22528\n"
  "v_mfma_f32_16x16x32_bf16 v[16:19], a[160:163], a[68:71], v[16:19]\n"
  "s_sub_u32 s21, 0x36000, s54\n"
  "buffer_load_dwordx4 a[56:59], v73, s[12:15], s21 offen offset:0 sc1\n"
  "buffer_load_dwordx4 a[60:63], v73, s[12:15], s21 offen offset:1024 sc1\n"
  "s_waitcnt lgkmcnt(6)\n"
  "v_mfma_f32_16x16x32_bf16 v[4:7], a[164:167], a[64:67], v[4:7]\n"
  "ds_read_b64_tr_b16 a[180:181], v75 offset:20480\n"
  "ds_read_b64_tr_b16 a[182:183], v75 offset:22528\n"
  "v_mfma_f32_16x16x32_bf16 v[20:23], a[164:167], a[68:71], v[20:23]\n"
  "s_waitcnt lgkmcnt(6)\n"
  "v_mfma_f32_16x16x32_bf16 v[8:11], a[168:171], a[64:67], v[8:11]\n"
  "ds_read_b64_tr_b16 a[184:185], v76 offset:20480\n"
  "ds_read_b64_tr_b16 a[186:187], v76 offset:22528\n"
  "v_mfma_f32_16x16x32_bf16 v[24:27], a[168:171], a[68:71], v[24:27]\n"
  "s_waitcnt lgkmcnt(6)\n"
  "v_mfma_f32_16x16x32_bf16 v[12:15], a[172:175], a[64:67], v[12:15]\n"
  "ds_read_b64_tr_b16 a[188:189], v77 offset:20480\n"
  "ds_read_b64_tr_b16 a[190:191], v77 offset:22528\n"
  "v_mfma_f32_16x16x32_bf16 v[28:31], a[172:175], a[68:71], v[28:31]\n"
  "s_waitcnt lgkmcnt(6)\n"
  "v_mfma_f32_16x16x32_bf16 v[0:3], a[176:179], a[72:75], v[0:3]\n"
  "ds_read_b64_tr_b16 a[160:161], v74 offset:24576\n"
  "ds_read_b64_tr_b16 a[162:163], v74 offset:26624\n"
  "v_mfma_f32_16x16x32_bf16 v[16:19], a[176:179], a[76:79], v[16:19]\n"
  "s_add_u32 s21, s54, 0x39000\n"
  "buffer_load_dwordx4 a[64:67], v73, s[12:15], s21 offen offset:0 sc1\n"
  "buffer_load_dwordx4 a[68:71], v73, s[12:15], s21 offen offset:1024 sc1\n"
  "s_waitcnt lgkmcnt(6)\n"
  "v_mfma_f32_16x16x32_bf16 v[4:7], a[180:183], a[72:75], v[4:7]\n"
  "ds_read_b64_tr_b16 a[164:165], v75 offset:24576\n"
  "ds_read_b64_tr_b16 a[166:167], v75 offset:26624\n"
  "v_mfma_f32_16x16x32_bf16 v[20:23], a[180:183], a[76:79], v[20:23]\n"
  "s_waitcnt lgkmcnt(6)\n"
  "v_mfma_f32_16x16x32_bf16 v[8:11], a[184:187], a[72:75], v[8:11]\n"
  "ds_read_b64_tr_b16 a[168:169], v76 offset:24576\n"
  "ds_read_b64_tr_b16 a[170:171], v76 offset:26624\n"
  "v_mfma_f32_16x16x32_bf16 v[24:27], a[184:187], a[76:79], v[24:27]\n"
  "s_waitcnt lgkmcnt(6)\n"
  "v_mfma_f32_16x16x32_bf16 v[12:15], a[188:191], a[72:75], v[12:15]\n"
  "ds_read_b64_tr_b16 a[172:173], v77 offset:24576\n"
  "ds_read_b64_tr_b16 a[174:175], v77 offset:26624\n"
  "v_mfma_f32_16x16x32_bf16 v[28:31], a[188:191], a[76:79], v[28:31]\n"
  "s_waitcnt vmcnt(53) lgkmcnt(6)\n"
  "v_mfma_f32_16x16x32_bf16 v[0:3], a[160:163], a[80:83], v[0:3]\n"
  "ds_read_b64_tr_b16 a[176:177], v74 offset:28672\n"
  "ds_read_b64_tr_b16 a[178:179], v74 offset:30720\n"
  "v_mfma_f32_16x16x32_bf16 v[16:19], a[160:163], a[84:87], v[16:19]\n"
  "s_add_u32 s21, s54, 0x3d000\n"
  "buffer_load_dwordx4 a[72:75], v73, s[12:15], s21 offen offset:0 sc1\n"
  "buffer_load_dwordx4 a[76:79], v73, s[12:15], s21 offen offset:1024 sc1\n"
  "s_waitcnt lgkmcnt(6)\n"
  "v_mfma_f32_16x16x32_bf16 v[4:7], a[164:167], a[80:83], v[4:7]\n"
  "ds_read_b64_tr_b16 a[180:181], v75 offset:28672\n"
  "ds_read_b64_tr_b16 a[182:183], v75 offset:30720\n"
  "v_mfma_f32_16x16x32_bf16 v[20:23], a[164:167], a[84:87], v[20:23]\n"
  "s_waitcnt lgkmcnt(6)\n"
  "v_mfma_f32_16x16x32_bf16 v[8:11], a[168:171], a[80:83], v[8:11]\n"
  "ds_read_b64_tr_b16 a[184:185], v76 offset:28672\n"
  "ds_read_b64_tr_b16 a[186:187], v76 offset:30720\n"
  "v_mfma_f32_16x16x32_bf16 v[24:27], a[168:171], a[84:87], v[24:27]\n"
  "s_waitcnt lgkmcnt(6)\n"
  "v_mfma_f32_16x16x32_bf16 v[12:15], a[172:175], a[80:83], v[12:15]\n"
  "ds_read_b64_tr_b16 a[188:189], v77 offset:28672\n"
  "ds_read_b64_tr_b16 a[190:191], v77 offset:30720\n"
  "v_mfma_f32_16x16x32_bf16 v[28:31], a[172:175], a[84:87], v[28:31]\n"
  "s_waitcnt vmcnt(49)\n"
  "s_barrier\n"
  "s_waitcnt lgkmcnt(6)\n"
  "v_mfma_f32_16x16x32_bf16 v[0:3], a[176:179], a[88:91], v[0:3]\n"
  "ds_read_b64_tr_b16 a[160:161], v74 offset:32768\n"
  "ds_read_b64_tr_b16 a[162:163], v74 offset:34816\n"
  "v_mfma_f32_16x16x32_bf16 v[16:19], a[176:179], a[92:95], v[16:19]\n"
  "s_add_u32 s20, s57, 0x390000\n"
  "s_add_u32 m0, s22, 0\n"
  "s_nop 0\n"
  "buffer_load_dwordx4 v72, s[8:11], s20 offen lds\n"
  "s_add_u32 s20, s57, 0x3d0000\n"
  "s_add_u32 m0, s22, 4096\n"
  "s_nop 0\n"
  "buffer_load_dwordx4 v72, s[8:11], s20 offen lds\n"
  "s_waitcnt lgkmcnt(6)\n"
  "v_mfma_f32_16x16x32_bf16 v[4:7], a[180:183], a[88:91], v[4:7]\n"
  "ds_read_b64_tr_b16 a[164:165], v75 offset:32768\n"
  "ds_read_b64_tr_b16 a[166:167], v75 offset:34816\n"
  "v_mfma_f32_16x16x32_bf16 v[20:23], a[180:183], a[92:95], v[20:23]\n"
  "s_add_u32 s20, s57, 0x400000\n"
  "s_add_u32 m0, s22, 8192\n"
  "s_nop 0\n"
  "buffer_load_dwordx4 v72, s[8:11], s20 offen lds\n"
  "s_add_u32 s20, s57, 0x440000\n"
  "s_add_u32 m0, s22, 12288\n"
  "s_nop 0\n"
  "buffer_load_dwordx4 v72, s[8:11], s20 offen lds\n"
  "s_waitcnt lgkmcnt(6)\n"
  "v_mfma_f32_16x16x32_bf16 v[8:11], a[184:187], a[88:91], v[8:11]\n"
  "ds_read_b64_tr_b16 a[168:169], v76 offset:32768\n"
  "ds_read_b64_tr_b16 a[170:171], v76 offset:34816\n"
  "v_mfma_f32_16x16x32_bf16 v[24:27], a[184:187], a[92:95], v[24:27]\n"
  "s_add_u32 s21, s54, 0x40000\n"
  "buffer_load_dwordx4 a[80:83], v73, s[12:15], s21 offen offset:0 sc1\n"
  "s_waitcnt lgkmcnt(6)\n"
  "v_mfma_f32_16x16x32_bf16 v[12:15], a[188:191], a[88:91], v[12:15]\n"
  "ds_read_b64_tr_b16 a[172:173], v77 offset:32768\n"
  "ds_read_b64_tr_b16 a[174:175], v77 offset:34816\n"
  "v_mfma_f32_16x16x32_bf16 v[28:31], a[188:191], a[92:95], v[28:31]\n"
  "buffer_load_dwordx4 a[84:87], v73, s[12:15], s21 offen offset:1024 sc1\n"
  "s_waitcnt vmcnt(52) lgkmcnt(6)\n"
  "v_mfma_f32_16x16x32_bf16 v[0:3], a[160:163], a[96:99], v[0:3]\n"
  "ds_read_b64_tr_b16 a[176:177], v74 offset:36864\n"
  "ds_read_b64_tr_b16 a[178:179], v74 offset:38912\n"
  "v_mfma_f32_16x16x32_bf16 v[16:19], a[160:163], a[100:103], v[16:19]\n"
  "s_add_u32 s21, s54, 0x44000\n"
  "buffer_load_dwordx4 a[88:91], v73, s[12:15], s21 offen offset:0 sc1\n"
  "buffer_load_dwordx4 a[92:95], v73, s[12:15], s21 offen offset:1024 sc1\n"
  "s_waitcnt lgkmcnt(6)\n"
  "v_mfma_f32_16x16x32_bf16 v[4:7], a[164:167], a[96:99], v[4:7]\n"
  "ds_read_b64_tr_b16 a[180:181], v75 offset:36864\n"
  "ds_read_b64_tr_b16 a[182:183], v75 offset:38912\n"
  "v_mfma_f32_16x16x32_bf16 v[20:23], a[164:167], a[100:103], v[20:23]\n"
  "s_waitcnt lgkmcnt(6)\n"
  "v_mfma_f32_16x16x32_bf16 v[8:11], a[168:171], a[96:99], v[8:11]\n"
  "ds_read_b64_tr_b16 a[184:185], v76 offset:36864\n"
  "ds_read_b64_tr_b16 a[186:187], v76 offset:38912\n"
  "v_mfma_f32_16x16x32_bf16 v[24:27], a[168:171], a[100:103], v[24:27]\n"
  "s_waitcnt lgkmcnt(6)\n"
  "v_mfma_f32_16x16x32_bf16 v[12:15], a[172:175], a[96:99], v[12:15]\n"
  "ds_read_b64_tr_b16 a[188:189], v77 offset:36864\n"
  "ds_read_b64_tr_b16 a[190:191], v77 offset:38912\n"
  "v_mfma_f32_16x16x32_bf16 v[28:31], a[172:175], a[100:103], v[28:31]\n"
  "s_waitcnt vmcnt(52) lgkmcnt(6)\n"
  "v_mfma_f32_16x16x32_bf16 v[0:3], a[176:179], a[104:107], v[0:3]\n"
  "ds_read_b64_tr_b16 a[160:161], v74 offset:40960\n"
  "ds_read_b64_tr_b16 a[162:163], v74 offset:43008\n"
  "v_mfma_f32_16x16x32_bf16 v[16:19], a[176:179], a[108:111], v[16:19]\n"
  "s_sub_u32 s21, 0x47000, s54\n"
  "buffer_load_dwordx4 a[96:99], v73, s[12:15], s21 offen offset:0 sc1\n"
  "buffer_load_dwordx4 a[100:103], v73, s[12:15], s21 offen offset:1024 sc1\n"
  "s_waitcnt lgkmcnt(6)\n"
  "v_mfma_f32_16x16x32_bf16 v[4:7], a[180:183], a[104:107], v[4:7]\n"
  "ds_read_b64_tr_b16 a[164:165], v75 offset:40960\n"
  "ds_read_b64_tr_b16 a[166:167], v75 offset:43008\n"
  "v_mfma_f32_16x16x32_bf16 v[20:23], a[180:183], a[108:111], v[20:23]\n"
  "s_waitcnt lgkmcnt(6)\n"
  "v_mfma_f32_16x16x32_bf16 v[8:11], a[184:187], a[104:107], v[8:11]\n"
  "ds_read_b64_tr_b16 a[168:169], v76 offset:40960\n"
  "ds_read_b64_tr_b16 a[170:171], v76 offset:43008\n"
  "v_mfma_f32_16x16x32_bf16 v[24:27], a[184:187], a[108:111], v[24:27]\n"
  "s_waitcnt lgkmcnt(6)\n"
  "v_mfma_f32_16x16x32_bf16 v[12:15], a[188:191], a[104:107], v[12:15]\n"
  "ds_read_b64_tr_b16 a[172:173], v77 offset:40960\n"
  "ds_read_b64_tr_b16 a[174:175], v77 offset:43008\n"
  "v_mfma_f32_16x16x32_bf16 v[28:31], a[188:191], a[108:111], v[28:31]\n"
  "s_waitcnt vmcnt(52) lgkmcnt(6)\n"
  "v_mfma_f32_16x16x32_bf16 v[0:3], a[160:163], a[112:115], v[0:3]\n"
  "ds_read_b64_tr_b16 a[176:177], v74 offset:45056\n"
  "ds_read_b64_tr_b16 a[178:179], v74 offset:47104\n"
  "v_mfma_f32_16x16x32_bf16 v[16:19], a[160:163], a[116:119], v[16:19]\n"
  "s_sub_u32 s21, 0x4b000, s54\n"
  "buffer_load_dwordx4 a[104:107], v73, s[12:15], s21 offen offset:0 sc1\n"
  "buffer_load_dwordx4 a[108:111], v73, s[12:15], s21 offen offset:1024 sc1\n"
  "s_waitcnt lgkmcnt(6)\n"
  "v_mfma_f32_16x16x32_bf16 v[4:7], a[164:167], a[112:115], v[4:7]\n"
  "ds_read_b64_tr_b16 a[180:181], v75 offset:45056\n"
  "ds_read_b64_tr_b16 a[182:183], v75 offset:47104\n"
  "v_mfma_f32_16x16x32_bf16 v[20:23], a[164:167], a[116:119], v[20:23]\n"
  "s_waitcnt lgkmcnt(6)\n"
  "v_mfma_f32_16x16x32_bf16 v[8:11], a[168:171], a[112:115], v[8:11]\n"
  "ds_read_b64_tr_b16 a[184:185], v76 offset:45056\n"
  "ds_read_b64_tr_b16 a[186:187], v76 offset:47104\n"
  "v_mfma_f32_16x16x32_bf16 v[24:27], a[168:171], a[116:119], v[24:27]\n"
  "s_waitcnt lgkmcnt(6)\n"
  "v_mfma_f32_16x16x32_bf16 v[12:15], a[172:175], a[112:115], v[12:15]\n"
  "ds_read_b64_tr_b16 a[188:189], v77 offset:45056\n"
  "ds_read_b64_tr_b16 a[190:191], v77 offset:47104\n"
  "v_mfma_f32_16x16x32_bf16 v[28:31], a[172:175], a[116:119], v[28:31]\n"
  "s_waitcnt vmcnt(44)\n"
  "s_barrier\n"
  "s_waitcnt lgkmcnt(6)\n"
  "v_mfma_f32_16x16x32_bf16 v[0:3], a[176:179], a[120:123], v[0:3]\n"
  "ds_read_b64_tr_b16 a[160:161], v74 offset:49152\n"
  "ds_read_b64_tr_b16 a[162:163], v74 offset:51200\n"
  "v_mfma_f32_16x16x32_bf16 v[16:19], a[176:179], a[124:127], v[16:19]\n"
  "global_load_dword v105, v103, s[62:63] sc1\n"
  "s_sub_u32 s20, 0x470000, s57\n"
  "s_add_u32 m0, s22, 16384\n"
  "s_nop 0\n"
  "buffer_load_dwordx4 v72, s[8:11], s20 offen lds\n"
  "s_waitcnt lgkmcnt(6)\n"
  "v_mfma_f32_16x16x32_bf16 v[4:7], a[180:183], a[120:123], v[4:7]\n"
  "ds_read_b64_tr_b16 a[164:165], v75 offset:49152\n"
  "ds_read_b64_tr_b16 a[166:167], v75 offset:51200\n"
  "v_mfma_f32_16x16x32_bf16 v[20:23], a[180:183], a[124:127], v[20:23]\n"
  "s_sub_u32 s20, 0x4b0000, s57\n"
  "s_add_u32 m0, s22, 20480\n"
  "s_nop 0\n"
  "buffer_load_dwordx4 v72, s[8:11], s20 offen lds\n"
  "s_sub_u32 s20, 0x4e0000, s57\n"
  "s_add_u32 m0, s22, 24576\n"
  "s_nop 0\n"
  "buffer_load_dwordx4 v72, s[8:11], s20 offen lds\n"
  "s_waitcnt lgkmcnt(6)\n"
  "v_mfma_f32_16x16x32_bf16 v[8:11], a[184:187], a[120:123], v[8:11]\n"
  "ds_read_b64_tr_b16 a[168:169], v76 offset:49152\n"
  "ds_read_b64_tr_b16 a[170:171], v76 offset:51200\n"
  "v_mfma_f32_16x16x32_bf16 v[24:27], a[184:187], a[124:127], v[24:27]\n"
  "s_sub_u32 s20, 0x520000, s57\n"
  "s_add_u32 m0, s22, 28672\n"
  "s_nop 0\n"
  "buffer_load_dwordx4 v72, s[8:11], s20 offen lds\n"
  "s_waitcnt lgkmcnt(6)\n"
  "v_mfma_f32_16x16x32_bf16 v[12:15], a[188:191], a[120:123], v[12:15]\n"
  "ds_read_b64_tr_b16 a[172:173], v77 offset:49152\n"
  "ds_read_b64_tr_b16 a[174:175], v77 offset:51200\n"
  "v_mfma_f32_16x16x32_bf16 v[28:31], a[188:191], a[124:127], v[28:31]\n"
  "s_sub_u32 s21, 0x4e000, s54\n"
  "buffer_load_dwordx4 a[112:115], v73, s[12:15], s21 offen offset:0 sc1\n"
  "buffer_load_dwordx4 a[116:119], v73, s[12:15], s21 offen offset:1024 sc1\n"
  "s_waitcnt lgkmcnt(6)\n"
  "v_mfma_f32_16x16x32_bf16 v[0:3], a[160:163], a[128:131], v[0:3]\n"
  "ds_read_b64_tr_b16 a[176:177], v74 offset:53248\n"
  "ds_read_b64_tr_b16 a[178:179], v74 offset:55296\n"
  "v_mfma_f32_16x16x32_bf16 v[16:19], a[160:163], a[132:135], v[16:19]\n"
  "s_sub_u32 s21, 0x52000, s54\n"
  "buffer_load_dwordx4 a[120:123], v73, s[12:15], s21 offen offset:0 sc1\n"
  "s_waitcnt lgkmcnt(6)\n"
  "v_mfma_f32_16x16x32_bf16 v[4:7], a[164:167], a[128:131], v[4:7]\n"
  "ds_read_b64_tr_b16 a[180:181], v75 offset:53248\n"
  "ds_read_b64_tr_b16 a[182:183], v75 offset:55296\n"
  "v_mfma_f32_16x16x32_bf16 v[20:23], a[164:167], a[132:135], v[20:23]\n"
  "buffer_load_dwordx4 a[124:127], v73, s[12:15], s21 offen offset:1024 sc1\n"
  "s_waitcnt lgkmcnt(6)\n"
  "v_mfma_f32_16x16x32_bf16 v[8:11], a[168:171], a[128:131], v[8:11]\n"
  "ds_read_b64_tr_b16 a[184:185], v76 offset:53248\n"
  "ds_read_b64_tr_b16 a[186:187], v76 offset:55296\n"
  "v_mfma_f32_16x16x32_bf16 v[24:27], a[168:171], a[132:135], v[24:27]\n"
  "s_waitcnt lgkmcnt(6)\n"
  "v_mfma_f32_16x16x32_bf16 v[12:15], a[172:175], a[128:131], v[12:15]\n"
  "ds_read_b64_tr_b16 a[188:189], v77 offset:53248\n"
  "ds_read_b64_tr_b16 a[190:191], v77 offset:55296\n"
  "v_mfma_f32_16x16x32_bf16 v[28:31], a[172:175], a[132:135], v[28:31]\n"
  "s_waitcnt lgkmcnt(6)\n"
  "v_mfma_f32_16x16x32_bf16 v[0:3], a[176:179], a[136:139], v[0:3]\n"
  "ds_read_b64_tr_b16 a[160:161], v74 offset:57344\n"
  "ds_read_b64_tr_b16 a[162:163], v74 offset:59392\n"
  "v_mfma_f32_16x16x32_bf16 v[16:19], a[176:179], a[140:143], v[16:19]\n"
  "s_add_u32 s21, s54, 0x55000\n"
  "buffer_load_dwordx4 a[128:131], v73, s[12:15], s21 offen offset:0 sc1\n"
  "buffer_load_dwordx4 a[132:135], v73, s[12:15], s21 offen offset:1024 sc1\n"
  "s_waitcnt lgkmcnt(6)\n"
  "v_mfma_f32_16x16x32_bf16 v[4:7], a[180:183], a[136:139], v[4:7]\n"
  "ds_read_b64_tr_b16 a[164:165], v75 offset:57344\n"
  "ds_read_b64_tr_b16 a[166:167], v75 offset:59392\n"
  "v_mfma_f32_16x16x32_bf16 v[20:23], a[180:183], a[140:143], v[20:23]\n"
  "s_waitcnt lgkmcnt(6)\n"
  "v_mfma_f32_16x16x32_bf16 v[8:11], a[184:187], a[136:139], v[8:11]\n"
  "ds_read_b64_tr_b16 a[168:169], v76 offset:57344\n"
  "ds_read_b64_tr_b16 a[170:171], v76 offset:59392\n"
  "v_mfma_f32_16x16x32_bf16 v[24:27], a[184:187], a[140:143], v[24:27]\n"
  "s_waitcnt lgkmcnt(6)\n"
  "v_mfma_f32_16x16x32_bf16 v[12:15], a[188:191], a[136:139], v[12:15]\n"
  "ds_read_b64_tr_b16 a[172:173], v77 offset:57344\n"
  "ds_read_b64_tr_b16 a[174:175], v77 offset:59392\n"
  "v_mfma_f32_16x16x32_bf16 v[28:31], a[188:191], a[140:143], v[28:31]\n"
  "s_waitcnt vmcnt(53) lgkmcnt(6)\n"
  "v_mfma_f32_16x16x32_bf16 v[0:3], a[160:163], a[144:147], v[0:3]\n"
  "ds_read_b64_tr_b16 a[176:177], v74 offset:61440\n"
  "ds_read_b64_tr_b16 a[178:179], v74 offset:63488\n"
  "v_mfma_f32_16x16x32_bf16 v[16:19], a[160:163], a[148:151], v[16:19]\n"
  "s_add_u32 s21, s54, 0x59000\n"
  "buffer_load_dwordx4 a[136:139], v73, s[12:15], s21 offen offset:0 sc1\n"
  "buffer_load_dwordx4 a[140:143], v73, s[12:15], s21 offen offset:1024 sc1\n"
  "s_waitcnt lgkmcnt(6)\n"
  "v_mfma_f32_16x16x32_bf16 v[4:7], a[164:167], a[144:147], v[4:7]\n"
  "ds_read_b64_tr_b16 a[180:181], v75 offset:61440\n"
  "ds_read_b64_tr_b16 a[182:183], v75 offset:63488\n"
  "v_mfma_f32_16x16x32_bf16 v[20:23], a[164:167], a[148:151], v[20:23]\n"
  "s_waitcnt lgkmcnt(6)\n"
  "v_mfma_f32_16x16x32_bf16 v[8:11], a[168:171], a[144:147], v[8:11]\n"
  "ds_read_b64_tr_b16 a[184:185], v76 offset:61440\n"
  "ds_read_b64_tr_b16 a[186:187], v76 offset:63488\n"
  "v_mfma_f32_16x16x32_bf16 v[24:27], a[168:171], a[148:151], v[24:27]\n"
  "s_waitcnt lgkmcnt(6)\n"
  "v_mfma_f32_16x16x32_bf16 v[12:15], a[172:175], a[144:147], v[12:15]\n"
  "ds_read_b64_tr_b16 a[188:189], v77 offset:61440\n"
  "ds_read_b64_tr_b16 a[190:191], v77 offset:63488\n"
  "v_mfma_f32_16x16x32_bf16 v[28:31], a[172:175], a[148:151], v[28:31]\n"
  "s_waitcnt vmcnt(45)\n"
  "s_barrier\n"
  "s_waitcnt lgkmcnt(6)\n"
  "v_mfma_f32_16x16x32_bf16 v[0:3], a[176:179], a[152:155], v[0:3]\n"
  "ds_read_b64_tr_b16 a[160:161], v80 offset:0\n"
  "ds_read_b64_tr_b16 a[162:163], v80 offset:2048\n"
  "v_mfma_f32_16x16x32_bf16 v[16:19], a[176:179], a[156:159], v[16:19]\n"
  "s_add_u32 s20, s57, 0x550000\n"
  "s_add_u32 m0, s22, 32768\n"
  "s_nop 0\n"
  "buffer_load_dwordx4 v72, s[8:11], s20 offen lds\n"
  "s_add_u32 s20, s57, 0x590000\n"
  "s_add_u32 m0, s22, 36864\n"
  "s_nop 0\n"
  "buffer_load_dwordx4 v72, s[8:11], s20 offen lds\n"
  "s_waitcnt lgkmcnt(6)\n"
  "v_mfma_f32_16x16x32_bf16 v[4:7], a[180:183], a[152:155], v[4:7]\n"
  "ds_read_b64_tr_b16 a[164:165], v81 offset:0\n"
  "ds_read_b64_tr_b16 a[166:167], v81 offset:2048\n"
  "v_mfma_f32_16x16x32_bf16 v[20:23], a[180:183], a[156:159], v[20:23]\n"
  "s_add_u32 s20, s57, 0x5c0000\n"
  "s_add_u32 m0, s22, 40960\n"
  "s_nop 0\n"
  "buffer_load_dwordx4 v72, s[8:11], s20 offen lds\n"
  "s_add_u32 s20, s57, 0x600000\n"
  "s_add_u32 m0, s22, 45056\n"
  "s_nop 0\n"
  "buffer_load_dwordx4 v72, s[8:11], s20 offen lds\n"
  "s_waitcnt lgkmcnt(6)\n"
  "v_mfma_f32_16x16x32_bf16 v[8:11], a[184:187], a[152:155], v[8:11]\n"
  "ds_read_b64_tr_b16 a[168:169], v82 offset:0\n"
  "ds_read_b64_tr_b16 a[170:171], v82 offset:2048\n"
  "v_mfma_f32_16x16x32_bf16 v[24:27], a[184:187], a[156:159], v[24:27]\n"
  "s_add_u32 s21, s54, 0x5c000\n"
  "buffer_load_dwordx4 a[144:147], v73, s[12:15], s21 offen offset:0 sc1\n"
  "s_waitcnt lgkmcnt(6)\n"
  "v_mfma_f32_16x16x32_bf16 v[12:15], a[188:191], a[152:155], v[12:15]\n"
  "ds_read_b64_tr_b16 a[172:173], v83 offset:0\n"
  "ds_read_b64_tr_b16 a[174:175], v83 offset:2048\n"
  "v_mfma_f32_16x16x32_bf16 v[28:31], a[188:191], a[156:159], v[28:31]\n"
  "buffer_load_dwordx4 a[148:151], v73, s[12:15], s21 offen offset:1024 sc1\n"
  "s_waitcnt lgkmcnt(6)\n"
  "v_mfma_f32_16x16x32_bf16 v[0:3], a[160:163], a[0:3], v[0:3]\n"
  "ds_read_b64_tr_b16 a[176:177], v80 offset:4096\n"
  "ds_read_b64_tr_b16 a[178:179], v80 offset:6144\n"
  "v_mfma_f32_16x16x32_bf16 v[16:19], a[160:163], a[4:7], v[16:19]\n"
  "s_add_u32 s21, s54, 0x60000\n"
  "buffer_load_dwordx4 a[152:155], v73, s[12:15], s21 offen offset:0 sc1\n"
  "buffer_load_dwordx4 a[156:159], v73, s[12:15], s21 offen offset:1024 sc1\n"
  "s_waitcnt lgkmcnt(6)\n"
  "v_mfma_f32_16x16x32_bf16 v[4:7], a[164:167], a[0:3], v[4:7]\n"
  "ds_read_b64_tr_b16 a[180:181], v81 offset:4096\n"
  "ds_read_b64_tr_b16 a[182:183], v81 offset:6144\n"
  "v_mfma_f32_16x16x32_bf16 v[20:23], a[164:167], a[4:7], v[20:23]\n"
  "s_waitcnt lgkmcnt(6)\n"
  "v_mfma_f32_16x16x32_bf16 v[8:11], a[168:171], a[0:3], v[8:11]\n"
  "ds_read_b64_tr_b16 a[184:185], v82 offset:4096\n"
  "ds_read_b64_tr_b16 a[186:187], v82 offset:6144\n"
  "v_mfma_f32_16x16x32_bf16 v[24:27], a[168:171], a[4:7], v[24:27]\n"
  "s_waitcnt lgkmcnt(6)\n"
  "v_mfma_f32_16x16x32_bf16 v[12:15], a[172:175], a[0:3], v[12:15]\n"
  "ds_read_b64_tr_b16 a[188:189], v83 offset:4096\n"
  "ds_read_b64_tr_b16 a[190:191], v83 offset:6144\n"
  "v_mfma_f32_16x16x32_bf16 v[28:31], a[172:175], a[4:7], v[28:31]\n"
  "s_waitcnt lgkmcnt(6)\n"
  "v_mfma_f32_16x16x32_bf16 v[0:3], a[176:179], a[8:11], v[0:3]\n"
  "ds_read_b64_tr_b16 a[160:161], v80 offset:8192\n"
  "ds_read_b64_tr_b16 a[162:163], v80 offset:10240\n"
  "v_mfma_f32_16x16x32_bf16 v[16:19], a[176:179], a[12:15], v[16:19]\n"
  "s_sub_u32 s21, 0x63000, s54\n"
  "buffer_load_dwordx4 a[0:3], v73, s[12:15], s21 offen offset:0 sc1\n"
  "buffer_load_dwordx4 a[4:7], v73, s[12:15], s21 offen offset:1024 sc1\n"
  "s_waitcnt lgkmcnt(6)\n"
  "v_mfma_f32_16x16x32_bf16 v[4:7], a[180:183], a[8:11], v[4:7]\n"
  "ds_read_b64_tr_b16 a[164:165], v81 offset:8192\n"
  "ds_read_b64_tr_b16 a[166:167], v81 offset:10240\n"
  "v_mfma_f32_16x16x32_bf16 v[20:23], a[180:183], a[12:15], v[20:23]\n"
  "s_waitcnt lgkmcnt(6)\n"
  "v_mfma_f32_16x16x32_bf16 v[8:11], a[184:187], a[8:11], v[8:11]\n"
  "ds_read_b64_tr_b16 a[168:169], v82 offset:8192\n"
  "ds_read_b64_tr_b16 a[170:171], v82 offset:10240\n"
  "v_mfma_f32_16x16x32_bf16 v[24:27], a[184:187], a[12:15], v[24:27]\n"
  "s_waitcnt lgkmcnt(6)\n"
  "v_mfma_f32_16x16x32_bf16 v[12:15], a[188:191], a[8:11], v[12:15]\n"
  "ds_read_b64_tr_b16 a[172:173], v83 offset:8192\n"
  "ds_read_b64_tr_b16 a[174:175], v83 offset:10240\n"
  "v_mfma_f32_16x16x32_bf16 v[28:31], a[188:191], a[12:15], v[28:31]\n"
  "s_waitcnt vmcnt(53) lgkmcnt(6)\n"
  "v_mfma_f32_16x16x32_bf16 v[0:3], a[160:163], a[16:19], v[0:3]\n"
  "ds_read_b64_tr_b16 a[176:177], v80 offset:12288\n"
  "ds_read_b64_tr_b16 a[178:179], v80 offset:14336\n"
  "v_mfma_f32_16x16x32_bf16 v[16:19], a[160:163], a[20:23], v[16:19]\n"
  "s_sub_u32 s21, 0x67000, s54\n"
  "buffer_load_dwordx4 a[8:11], v73, s[12:15], s21 offen offset:0 sc1\n"
  "buffer_load_dwordx4 a[12:15], v73, s[12:15], s21 offen offset:1024 sc1\n"
  "s_waitcnt lgkmcnt(6)\n"
  "v_mfma_f32_16x16x32_bf16 v[4:7], a[164:167], a[16:19], v[4:7]\n"
  "ds_read_b64_tr_b16 a[180:181], v81 offset:12288\n"
  "ds_read_b64_tr_b16 a[182:183], v81 offset:14336\n"
  "v_mfma_f32_16x16x32_bf16 v[20:23], a[164:167], a[20:23], v[20:23]\n"
  "s_waitcnt lgkmcnt(6)\n"
  "v_mfma_f32_16x16x32_bf16 v[8:11], a[168:171], a[16:19], v[8:11]\n"
  "ds_read_b64_tr_b16 a[184:185], v82 offset:12288\n"
  "ds_read_b64_tr_b16 a[186:187], v82 offset:14336\n"
  "v_mfma_f32_16x16x32_bf16 v[24:27], a[168:171], a[20:23], v[24:27]\n"
  "s_waitcnt lgkmcnt(6)\n"
  "v_mfma_f32_16x16x32_bf16 v[12:15], a[172:175], a[16:19], v[12:15]\n"
  "ds_read_b64_tr_b16 a[188:189], v83 offset:12288\n"
  "ds_read_b64_tr_b16 a[190:191], v83 offset:14336\n"
  "v_mfma_f32_16x16x32_bf16 v[28:31], a[172:175], a[20:23], v[28:31]\n"
  "s_waitcnt vmcnt(45)\n"
  "s_barrier\n"
  "s_waitcnt lgkmcnt(6)\n"
  "v_mfma_f32_16x16x32_bf16 v[0:3], a[176:179], a[24:27], v[0:3]\n"
  "ds_read_b64_tr_b16 a[160:161], v80 offset:16384\n"
  "ds_read_b64_tr_b16 a[162:163], v80 offset:18432\n"
  "v_mfma_f32_16x16x32_bf16 v[16:19], a[176:179], a[28:31], v[16:19]\n"
  "s_sub_u32 s20, 0x630000, s57\n"
  "s_add_u32 m0, s22, 49152\n"
  "s_nop 0\n"
  "buffer_load_dwordx4 v72, s[8:11], s20 offen lds\n"
  "s_sub_u32 s20, 0x670000, s57\n"
  "s_add_u32 m0, s22, 53248\n"
  "s_nop 0\n"
  "buffer_load_dwordx4 v72, s[8:11], s20 offen lds\n"
  "s_waitcnt lgkmcnt(6)\n"
  "v_mfma_f32_16x16x32_bf16 v[4:7], a[180:183], a[24:27], v[4:7]\n"
  "ds_read_b64_tr_b16 a[164:165], v81 offset:16384\n"
  "ds_read_b64_tr_b16 a[166:167], v81 offset:18432\n"
  "v_mfma_f32_16x16x32_bf16 v[20:23], a[180:183], a[28:31], v[20:23]\n"
  "s_sub_u32 s20, 0x6a0000, s57\n"
  "s_add_u32 m0, s22, 57344\n"
  "s_nop 0\n"
  "buffer_load_dwordx4 v72, s[8:11], s20 offen lds\n"
  "s_sub_u32 s20, 0x6e0000, s57\n"
  "s_add_u32 m0, s22, 61440\n"
  "s_nop 0\n"
  "buffer_load_dwordx4 v72, s[8:11], s20 offen lds\n"
  "s_waitcnt lgkmcnt(6)\n"
  "v_mfma_f32_16x16x32_bf16 v[8:11], a[184:187], a[24:27], v[8:11]\n"
  "ds_read_b64_tr_b16 a[168:169], v82 offset:16384\n"
  "ds_read_b64_tr_b16 a[170:171], v82 offset:18432\n"
  "v_mfma_f32_16x16x32_bf16 v[24:27], a[184:187], a[28:31], v[24:27]\n"
  "s_sub_u32 s21, 0x6a000, s54\n"
  "buffer_load_dwordx4 a[16:19], v73, s[12:15], s21 offen offset:0 sc1\n"
  "s_waitcnt lgkmcnt(6)\n"
  "v_mfma_f32_16x16x32_bf16 v[12:15], a[188:191], a[24:27], v[12:15]\n"
  "ds_read_b64_tr_b16 a[172:173], v83 offset:16384\n"
  "ds_read_b64_tr_b16 a[174:175], v83 offset:18432\n"
  "v_mfma_f32_16x16x32_bf16 v[28:31], a[188:191], a[28:31], v[28:31]\n"
  "buffer_load_dwordx4 a[20:23], v73, s[12:15], s21 offen offset:1024 sc1\n"
  "s_waitcnt lgkmcnt(6)\n"
  "v_mfma_f32_16x16x32_bf16 v[0:3], a[160:163], a[32:35], v[0:3]\n"
  "ds_read_b64_tr_b16 a[176:177], v80 offset:20480\n"
  "ds_read_b64_tr_b16 a[178:179], v80 offset:22528\n"
  "v_mfma_f32_16x16x32_bf16 v[16:19], a[160:163], a[36:39], v[16:19]\n"
  "s_sub_u32 s21, 0x6e000, s54\n"
  "buffer_load_dwordx4 a[24:27], v73, s[12:15], s21 offen offset:0 sc1\n"
  "buffer_load_dwordx4 a[28:31], v73, s[12:15], s21 offen offset:1024 sc1\n"
  "s_waitcnt lgkmcnt(6)\n"
  "v_mfma_f32_16x16x32_bf16 v[4:7], a[164:167], a[32:35], v[4:7]\n"
  "ds_read_b64_tr_b16 a[180:181], v81 offset:20480\n"
  "ds_read_b64_tr_b16 a[182:183], v81 offset:22528\n"
  "v_mfma_f32_16x16x32_bf16 v[20:23], a[164:167], a[36:39], v[20:23]\n"
  "s_waitcnt lgkmcnt(6)\n"
  "v_mfma_f32_16x16x32_bf16 v[8:11], a[168:171], a[32:35], v[8:11]\n"
  "ds_read_b64_tr_b16 a[184:185], v82 offset:20480\n"
  "ds_read_b64_tr_b16 a[186:187], v82 offset:22528\n"
  "v_mfma_f32_16x16x32_bf16 v[24:27], a[168:171], a[36:39], v[24:27]\n"
  "s_waitcnt lgkmcnt(6)\n"
  "v_mfma_f32_16x16x32_bf16 v[12:15], a[172:175], a[32:35], v[12:15]\n"
  "ds_read_b64_tr_b16 a[188:189], v83 offset:20480\n"
  "ds_read_b64_tr_b16 a[190:191], v83 offset:22528\n"
  "v_mfma_f32_16x16x32_bf16 v[28:31], a[172:175], a[36:39], v[28:31]\n"
  "s_waitcnt lgkmcnt(6)\n"
  "v_mfma_f32_16x16x32_bf16 v[0:3], a[176:179], a[40:43], v[0:3]\n"
  "ds_read_b64_tr_b16 a[160:161], v80 offset:24576\n"
  "ds_read_b64_tr_b16 a[162:163], v80 offset:26624\n"
  "v_mfma_f32_16x16x32_bf16 v[16:19], a[176:179], a[44:47], v[16:19]\n"
  "s_waitcnt lgkmcnt(6)\n"
  "v_mfma_f32_16x16x32_bf16 v[4:7], a[180:183], a[40:43], v[4:7]\n"
  "ds_read_b64_tr_b16 a[164:165], v81 offset:24576\n"
  "ds_read_b64_tr_b16 a[166:167], v81 offset:26624\n"
  "v_mfma_f32_16x16x32_bf16 v[20:23], a[180:183], a[44:47], v[20:23]\n"
  "s_waitcnt lgkmcnt(6)\n"
  "v_mfma_f32_16x16x32_bf16 v[8:11], a[184:187], a[40:43], v[8:11]\n"
  "ds_read_b64_tr_b16 a[168:169], v82 offset:24576\n"
  "ds_read_b64_tr_b16 a[170:171], v82 offset:26624\n"
  "v_mfma_f32_16x16x32_bf16 v[24:27], a[184:187], a[44:47], v[24:27]\n"
  "s_waitcnt lgkmcnt(6)\n"
  "v_mfma_f32_16x16x32_bf16 v[12:15], a[188:191], a[40:43], v[12:15]\n"
  "ds_read_b64_tr_b16 a[172:173], v83 offset:24576\n"
  "ds_read_b64_tr_b16 a[174:175], v83 offset:26624\n"
  "v_mfma_f32_16x16x32_bf16 v[28:31], a[188:191], a[44:47], v[28:31]\n"
  "s_waitcnt vmcnt(32)\n"
  "v_cmp_ne_u32 vcc, 1.0, v105\n"
  "s_nop 4\n"
  "s_cbranch_vccz 61f\n"
  "s_mov_b32 s64, 0\n"
  "51:\n"
  "s_sleep 1\n"
  "global_load_dword v105, v103, s[62:63] sc1\n"
  "s_waitcnt vmcnt(0)\n"
  "v_cmp_ne_u32 vcc, 1.0, v105\n"
  "s_nop 4\n"
  "s_cbranch_vccz 61f\n"
  "s_add_u32 s64, s64, 1\n"
  "s_cmp_lt_u32 s64, 0x40000\n"
  "s_cbranch_scc1 51b\n"
  "61:\n"
  "s_waitcnt lgkmcnt(6)\n"
  "v_mfma_f32_16x16x32_bf16 v[0:3], a[160:163], a[48:51], v[0:3]\n"
  "ds_read_b64_tr_b16 a[176:177], v80 offset:28672\n"
  "ds_read_b64_tr_b16 a[178:179], v80 offset:30720\n"
  "v_mfma_f32_16x16x32_bf16 v[16:19], a[160:163], a[52:55], v[16:19]\n"
  "s_add_u32 s21, s56, 0x2000\n"
  "buffer_load_dwordx4 a[32:35], v73, s[12:15], s21 offen offset:0 sc1\n"
  "buffer_load_dwordx4 a[36:39], v73, s[12:15], s21 offen offset:1024 sc1\n"
  "s_waitcnt lgkmcnt(6)\n"
  "v_mfma_f32_16x16x32_bf16 v[4:7], a[164:167], a[48:51], v[4:7]\n"
  "ds_read_b64_tr_b16 a[180:181], v81 offset:28672\n"
  "ds_read_b64_tr_b16 a[182:183], v81 offset:30720\n"
  "v_mfma_f32_16x16x32_bf16 v[20:23], a[164:167], a[52:55], v[20:23]\n"
  "s_add_u32 s21, s55, 0x3000\n"
  "buffer_load_dwordx4 a[40:43], v73, s[12:15], s21 offen offset:0 sc1\n"
  "buffer_load_dwordx4 a[44:47], v73, s[12:15], s21 offen offset:1024 sc1\n"
  "s_waitcnt lgkmcnt(6)\n"
  "v_mfma_f32_16x16x32_bf16 v[8:11], a[168:171], a[48:51], v[8:11]\n"
  "ds_read_b64_tr_b16 a[184:185], v82 offset:28672\n"
  "ds_read_b64_tr_b16 a[186:187], v82 offset:30720\n"
  "v_mfma_f32_16x16x32_bf16 v[24:27], a[168:171], a[52:55], v[24:27]\n"
  "s_waitcnt lgkmcnt(6)\n"
  "v_mfma_f32_16x16x32_bf16 v[12:15], a[172:175], a[48:51], v[12:15]\n"
  "ds_read_b64_tr_b16 a[188:189], v83 offset:28672\n"
  "ds_read_b64_tr_b16 a[190:191], v83 offset:30720\n"
  "v_mfma_f32_16x16x32_bf16 v[28:31], a[172:175], a[52:55], v[28:31]\n"
  "s_barrier\n"
  "s_waitcnt lgkmcnt(6)\n"
  "v_mfma_f32_16x16x32_bf16 v[0:3], a[176:179], a[56:59], v[0:3]\n"
  "ds_read_b64_tr_b16 a[160:161], v74 offset:0\n"
  "ds_read_b64_tr_b16 a[162:163], v74 offset:2048\n"
  "v_mfma_f32_16x16x32_bf16 v[16:19], a[176:179], a[60:63], v[16:19]\n"
  "s_add_u32 s20, s66, 0x20000\n"
  "s_add_u32 m0, s22, 65536\n"
  "s_nop 0\n"
  "buffer_load_dwordx4 v72, s[8:11], s20 offen lds\n"
  "s_add_u32 s20, s65, 0x30000\n"
  "s_add_u32 m0, s22, 69632\n"
  "s_nop 0\n"
  "buffer_load_dwordx4 v72, s[8:11], s20 offen lds\n"
  "s_waitcnt lgkmcnt(6)\n"
  "v_mfma_f32_16x16x32_bf16 v[4:7], a[180:183], a[56:59], v[4:7]\n"
  "ds_read_b64_tr_b16 a[164:165], v75 offset:0\n"
  "ds_read_b64_tr_b16 a[166:167], v75 offset:2048\n"
  "v_mfma_f32_16x16x32_bf16 v[20:23], a[180:183], a[60:63], v[20:23]\n"
  "s_add_u32 s20, s65, 0x60000\n"
  "s_add_u32 m0, s22, 73728\n"
  "s_nop 0\n"
  "buffer_load_dwordx4 v72, s[8:11], s20 offen lds\n"
  "s_add_u32 s20, s66, 0x90000\n"
  "s_add_u32 m0, s22, 77824\n"
  "s_nop 0\n"
  "buffer_load_dwordx4 v72, s[8:11], s20 offen lds\n"
  "s_waitcnt lgkmcnt(6)\n"
  "v_mfma_f32_16x16x32_bf16 v[8:11], a[184:187], a[56:59], v[8:11]\n"
  "ds_read_b64_tr_b16 a[168:169], v76 offset:0\n"
  "ds_read_b64_tr_b16 a[170:171], v76 offset:2048\n"
  "v_mfma_f32_16x16x32_bf16 v[24:27], a[184:187], a[60:63], v[24:27]\n"
  "s_add_u32 s21, s55, 0x6000\n"
  "buffer_load_dwordx4 a[48:51], v73, s[12:15], s21 offen offset:0 sc1\n"
  "s_waitcnt lgkmcnt(6)\n"
  "v_mfma_f32_16x16x32_bf16 v[12:15], a[188:191], a[56:59], v[12:15]\n"
  "ds_read_b64_tr_b16 a[172:173], v77 offset:0\n"
  "ds_read_b64_tr_b16 a[174:175], v77 offset:2048\n"
  "v_mfma_f32_16x16x32_bf16 v[28:31], a[188:191], a[60:63], v[28:31]\n"
  "buffer_load_dwordx4 a[52:55], v73, s[12:15], s21 offen offset:1024 sc1\n"
  "s_waitcnt lgkmcnt(6)\n"
  "v_mfma_f32_16x16x32_bf16 v[0:3], a[160:163], a[64:67], v[0:3]\n"
  "ds_read_b64_tr_b16 a[176:177], v74 offset:4096\n"
  "ds_read_b64_tr_b16 a[178:179], v74 offset:6144\n"
  "v_mfma_f32_16x16x32_bf16 v[16:19], a[160:163], a[68:71], v[16:19]\n"
  "s_add_u32 s21, s56, 0x9000\n"
  "buffer_load_dwordx4 a[56:59], v73, s[12:15], s21 offen offset:0 sc1\n"
  "buffer_load_dwordx4 a[60:63], v73, s[12:15], s21 offen offset:1024 sc1\n"
  "s_waitcnt lgkmcnt(6)\n"
  "v_mfma_f32_16x16x32_bf16 v[4:7], a[164:167], a[64:67], v[4:7]\n"
  "ds_read_b64_tr_b16 a[180:181], v75 offset:4096\n"
  "ds_read_b64_tr_b16 a[182:183], v75 offset:6144\n"
  "v_mfma_f32_16x16x32_bf16 v[20:23], a[164:167], a[68:71], v[20:23]\n"
  "s_waitcnt lgkmcnt(6)\n"
  "v_mfma_f32_16x16x32_bf16 v[8:11], a[168:171], a[64:67], v[8:11]\n"
  "ds_read_b64_tr_b16 a[184:185], v76 offset:4096\n"
  "ds_read_b64_tr_b16 a[186:187], v76 offset:6144\n"
  "v_mfma_f32_16x16x32_bf16 v[24:27], a[168:171], a[68:71], v[24:27]\n"
  "s_waitcnt lgkmcnt(6)\n"
  "v_mfma_f32_16x16x32_bf16 v[12:15], a[172:175], a[64:67], v[12:15]\n"
  "ds_read_b64_tr_b16 a[188:189], v77 offset:4096\n"
  "ds_read_b64_tr_b16 a[190:191], v77 offset:6144\n"
  "v_mfma_f32_16x16x32_bf16 v[28:31], a[172:175], a[68:71], v[28:31]\n"
  "s_waitcnt lgkmcnt(6)\n"
  "v_mfma_f32_16x16x32_bf16 v[0:3], a[176:179], a[72:75], v[0:3]\n"
  "ds_read_b64_tr_b16 a[160:161], v74 offset:8192\n"
  "ds_read_b64_tr_b16 a[162:163], v74 offset:10240\n"
  "v_mfma_f32_16x16x32_bf16 v[16:19], a[176:179], a[76:79], v[16:19]\n"
  "s_sub_u32 s21, 0xa000, s56\n"
  "buffer_load_dwordx4 a[64:67], v73, s[12:15], s21 offen offset:0 sc1\n"
  "buffer_load_dwordx4 a[68:71], v73, s[12:15], s21 offen offset:1024 sc1\n"
  "s_waitcnt lgkmcnt(6)\n"
  "v_mfma_f32_16x16x32_bf16 v[4:7], a[180:183], a[72:75], v[4:7]\n"
  "ds_read_b64_tr_b16 a[164:165], v75 offset:8192\n"
  "ds_read_b64_tr_b16 a[166:167], v75 offset:10240\n"
  "v_mfma_f32_16x16x32_bf16 v[20:23], a[180:183], a[76:79], v[20:23]\n"
  "s_waitcnt lgkmcnt(6)\n"
  "v_mfma_f32_16x16x32_bf16 v[8:11], a[184:187], a[72:75], v[8:11]\n"
  "ds_read_b64_tr_b16 a[168:169], v76 offset:8192\n"
  "ds_read_b64_tr_b16 a[170:171], v76 offset:10240\n"
  "v_mfma_f32_16x16x32_bf16 v[24:27], a[184:187], a[76:79], v[24:27]\n"
  "s_waitcnt lgkmcnt(6)\n"
  "v_mfma_f32_16x16x32_bf16 v[12:15], a[188:191], a[72:75], v[12:15]\n"
  "ds_read_b64_tr_b16 a[172:173], v77 offset:8192\n"
  "ds_read_b64_tr_b16 a[174:175], v77 offset:10240\n"
  "v_mfma_f32_16x16x32_bf16 v[28:31], a[188:191], a[76:79], v[28:31]\n"
  "s_waitcnt lgkmcnt(6)\n"
  "v_mfma_f32_16x16x32_bf16 v[0:3], a[160:163], a[80:83], v[0:3]\n"
  "ds_read_b64_tr_b16 a[176:177], v74 offset:12288\n"
  "ds_read_b64_tr_b16 a[178:179], v74 offset:14336\n"
  "v_mfma_f32_16x16x32_bf16 v[16:19], a[160:163], a[84:87], v[16:19]\n"
  "s_sub_u32 s21, 0xd000, s55\n"
  "buffer_load_dwordx4 a[72:75], v73, s[12:15], s21 offen offset:0 sc1\n"
  "buffer_load_dwordx4 a[76:79], v73, s[12:15], s21 offen offset:1024 sc1\n"
  "s_waitcnt lgkmcnt(6)\n"
  "v_mfma_f32_16x16x32_bf16 v[4:7], a[164:167], a[80:83], v[4:7]\n"
  "ds_read_b64_tr_b16 a[180:181], v75 offset:12288\n"
  "ds_read_b64_tr_b16 a[182:183], v75 offset:14336\n"
  "v_mfma_f32_16x16x32_bf16 v[20:23], a[164:167], a[84:87], v[20:23]\n"
  "s_waitcnt lgkmcnt(6)\n"
  "v_mfma_f32_16x16x32_bf16 v[8:11], a[168:171], a[80:83], v[8:11]\n"
  "ds_read_b64_tr_b16 a[184:185], v76 offset:12288\n"
  "ds_read_b64_tr_b16 a[186:187], v76 offset:14336\n"
  "v_mfma_f32_16x16x32_bf16 v[24:27], a[168:171], a[84:87], v[24:27]\n"
  "s_waitcnt lgkmcnt(6)\n"
  "v_mfma_f32_16x16x32_bf16 v[12:15], a[172:175], a[80:83], v[12:15]\n"
  "ds_read_b64_tr_b16 a[188:189], v77 offset:12288\n"
  "ds_read_b64_tr_b16 a[190:191], v77 offset:14336\n"
  "v_mfma_f32_16x16x32_bf16 v[28:31], a[172:175], a[84:87], v[28:31]\n"
  "s_waitcnt vmcnt(44)\n"
  "s_barrier\n"
  "s_waitcnt lgkmcnt(6)\n"
  "v_mfma_f32_16x16x32_bf16 v[0:3], a[176:179], a[88:91], v[0:3]\n"
  "ds_read_b64_tr_b16 a[160:161], v74 offset:16384\n"
  "ds_read_b64_tr_b16 a[162:163], v74 offset:18432\n"
  "v_mfma_f32_16x16x32_bf16 v[16:19], a[176:179], a[92:95], v[16:19]\n"
  "s_sub_u32 s20, 0xa0000, s66\n"
  "s_add_u32 m0, s22, 81920\n"
  "s_nop 0\n"
  "buffer_load_dwordx4 v72, s[8:11], s20 offen lds\n"
  "s_sub_u32 s20, 0xd0000, s65\n"
  "s_add_u32 m0, s22, 86016\n"
  "s_nop 0\n"
  "buffer_load_dwordx4 v72, s[8:11], s20 offen lds\n"
  "s_waitcnt lgkmcnt(6)\n"
  "v_mfma_f32_16x16x32_bf16 v[4:7], a[180:183], a[88:91], v[4:7]\n"
  "ds_read_b64_tr_b16 a[164:165], v75 offset:16384\n"
  "ds_read_b64_tr_b16 a[166:167], v75 offset:18432\n"
  "v_mfma_f32_16x16x32_bf16 v[20:23], a[180:183], a[92:95], v[20:23]\n"
  "s_sub_u32 s20, 0x100000, s65\n"
  "s_add_u32 m0, s22, 90112\n"
  "s_nop 0\n"
  "buffer_load_dwordx4 v72, s[8:11], s20 offen lds\n"
  "s_sub_u32 s20, 0x110000, s66\n"
  "s_add_u32 m0, s22, 94208\n"
  "s_nop 0\n"
  "buffer_load_dwordx4 v72, s[8:11], s20 offen lds\n"
  "s_waitcnt lgkmcnt(6)\n"
  "v_mfma_f32_16x16x32_bf16 v[8:11], a[184:187], a[88:91], v[8:11]\n"
  "ds_read_b64_tr_b16 a[168:169], v76 offset:16384\n"
  "ds_read_b64_tr_b16 a[170:171], v76 offset:18432\n"
  "v_mfma_f32_16x16x32_bf16 v[24:27], a[184:187], a[92:95], v[24:27]\n"
  "s_sub_u32 s21, 0x10000, s55\n"
  "buffer_load_dwordx4 a[80:83], v73, s[12:15], s21 offen offset:0 sc1\n"
  "s_waitcnt lgkmcnt(6)\n"
  "v_mfma_f32_16x16x32_bf16 v[12:15], a[188:191], a[88:91], v[12:15]\n"
  "ds_read_b64_tr_b16 a[172:173], v77 offset:16384\n"
  "ds_read_b64_tr_b16 a[174:175], v77 offset:18432\n"
  "v_mfma_f32_16x16x32_bf16 v[28:31], a[188:191], a[92:95], v[28:31]\n"
  "buffer_load_dwordx4 a[84:87], v73, s[12:15], s21 offen offset:1024 sc1\n"
  "s_waitcnt lgkmcnt(6)\n"
  "v_mfma_f32_16x16x32_bf16 v[0:3], a[160:163], a[96:99], v[0:3]\n"
  "ds_read_b64_tr_b16 a[176:177], v74 offset:20480\n"
  "ds_read_b64_tr_b16 a[178:179], v74 offset:22528\n"
  "v_mfma_f32_16x16x32_bf16 v[16:19], a[160:163], a[100:103], v[16:19]\n"
  "s_sub_u32 s21, 0x11000, s56\n"
  "buffer_load_dwordx4 a[88:91], v73, s[12:15], s21 offen offset:0 sc1\n"
  "buffer_load_dwordx4 a[92:95], v73, s[12:15], s21 offen offset:1024 sc1\n"
  "s_waitcnt lgkmcnt(6)\n"
  "v_mfma_f32_16x16x32_bf16 v[4:7], a[164:167], a[96:99], v[4:7]\n"
  "ds_read_b64_tr_b16 a[180:181], v75 offset:20480\n"
  "ds_read_b64_tr_b16 a[182:183], v75 offset:22528\n"
  "v_mfma_f32_16x16x32_bf16 v[20:23], a[164:167], a[100:103], v[20:23]\n"
  "s_waitcnt lgkmcnt(6)\n"
  "v_mfma_f32_16x16x32_bf16 v[8:11], a[168:171], a[96:99], v[8:11]\n"
  "ds_read_b64_tr_b16 a[184:185], v76 offset:20480\n"
  "ds_read_b64_tr_b16 a[186:187], v76 offset:22528\n"
  "v_mfma_f32_16x16x32_bf16 v[24:27], a[168:171], a[100:103], v[24:27]\n"
  "s_waitcnt lgkmcnt(6)\n"
  "v_mfma_f32_16x16x32_bf16 v[12:15], a[172:175], a[96:99], v[12:15]\n"
  "ds_read_b64_tr_b16 a[188:189], v77 offset:20480\n"
  "ds_read_b64_tr_b16 a[190:191], v77 offset:22528\n"
  "v_mfma_f32_16x16x32_bf16 v[28:31], a[172:175], a[100:103], v[28:31]\n"
  "s_waitcnt lgkmcnt(6)\n"
  "v_mfma_f32_16x16x32_bf16 v[0:3], a[176:179], a[104:107], v[0:3]\n"
  "ds_read_b64_tr_b16 a[160:161], v74 offset:24576\n"
  "ds_read_b64_tr_b16 a[162:163], v74 offset:26624\n"
  "v_mfma_f32_16x16x32_bf16 v[16:19], a[176:179], a[108:111], v[16:19]\n"
  "s_add_u32 s21, s55, 0x14000\n"
  "buffer_load_dwordx4 a[96:99], v73, s[12:15], s21 offen offset:0 sc1\n"
  "buffer_load_dwordx4 a[100:103], v73, s[12:15], s21 offen offset:1024 sc1\n"
  "s_waitcnt lgkmcnt(6)\n"
  "v_mfma_f32_16x16x32_bf16 v[4:7], a[180:183], a[104:107], v[4:7]\n"
  "ds_read_b64_tr_b16 a[164:165], v75 offset:24576\n"
  "ds_read_b64_tr_b16 a[166:167], v75 offset:26624\n"
  "v_mfma_f32_16x16x32_bf16 v[20:23], a[180:183], a[108:111], v[20:23]\n"
  "s_waitcnt lgkmcnt(6)\n"
  "v_mfma_f32_16x16x32_bf16 v[8:11], a[184:187], a[104:107], v[8:11]\n"
  "ds_read_b64_tr_b16 a[168:169], v76 offset:24576\n"
  "ds_read_b64_tr_b16 a[170:171], v76 offset:26624\n"
  "v_mfma_f32_16x16x32_bf16 v[24:27], a[184:187], a[108:111], v[24:27]\n"
  "s_waitcnt lgkmcnt(6)\n"
  "v_mfma_f32_16x16x32_bf16 v[12:15], a[188:191], a[104:107], v[12:15]\n"
  "ds_read_b64_tr_b16 a[172:173], v77 offset:24576\n"
  "ds_read_b64_tr_b16 a[174:175], v77 offset:26624\n"
  "v_mfma_f32_16x16x32_bf16 v[28:31], a[188:191], a[108:111], v[28:31]\n"
  "s_waitcnt vmcnt(52) lgkmcnt(6)\n"
  "v_mfma_f32_16x16x32_bf16 v[0:3], a[160:163], a[112:115], v[0:3]\n"
  "ds_read_b64_tr_b16 a[176:177], v74 offset:28672\n"
  "ds_read_b64_tr_b16 a[178:179], v74 offset:30720\n"
  "v_mfma_f32_16x16x32_bf16 v[16:19], a[160:163], a[116:119], v[16:19]\n"
  "s_add_u32 s21, s56, 0x17000\n"
  "buffer_load_dwordx4 a[104:107], v73, s[12:15], s21 offen offset:0 sc1\n"
  "buffer_load_dwordx4 a[108:111], v73, s[12:15], s21 offen offset:1024 sc1\n"
  "s_waitcnt lgkmcnt(6)\n"
  "v_mfma_f32_16x16x32_bf16 v[4:7], a[164:167], a[112:115], v[4:7]\n"
  "ds_read_b64_tr_b16 a[180:181], v75 offset:28672\n"
  "ds_read_b64_tr_b16 a[182:183], v75 offset:30720\n"
  "v_mfma_f32_16x16x32_bf16 v[20:23], a[164:167], a[116:119], v[20:23]\n"
  "s_waitcnt lgkmcnt(6)\n"
  "v_mfma_f32_16x16x32_bf16 v[8:11], a[168:171], a[112:115], v[8:11]\n"
  "ds_read_b64_tr_b16 a[184:185], v76 offset:28672\n"
  "ds_read_b64_tr_b16 a[186:187], v76 offset:30720\n"
  "v_mfma_f32_16x16x32_bf16 v[24:27], a[168:171], a[116:119], v[24:27]\n"
  "s_waitcnt lgkmcnt(6)\n"
  "v_mfma_f32_16x16x32_bf16 v[12:15], a[172:175], a[112:115], v[12:15]\n"
  "ds_read_b64_tr_b16 a[188:189], v77 offset:28672\n"
  "ds_read_b64_tr_b16 a[190:191], v77 offset:30720\n"
  "v_mfma_f32_16x16x32_bf16 v[28:31], a[172:175], a[116:119], v[28:31]\n"
  "s_waitcnt vmcnt(44)\n"
  "s_barrier\n"
  "s_waitcnt lgkmcnt(6)\n"
  "v_mfma_f32_16x16x32_bf16 v[0:3], a[176:179], a[120:123], v[0:3]\n"
  "ds_read_b64_tr_b16 a[160:161], v74 offset:32768\n"
  "ds_read_b64_tr_b16 a[162:163], v74 offset:34816\n"
  "v_mfma_f32_16x16x32_bf16 v[16:19], a[176:179], a[124:127], v[16:19]\n"
  "s_add_u32 s20, s65, 0x140000\n"
  "s_add_u32 m0, s22, 0\n"
  "s_nop 0\n"
  "buffer_load_dwordx4 v72, s[8:11], s20 offen lds\n"
  "s_add_u32 s20, s66, 0x170000\n"
  "s_add_u32 m0, s22, 4096\n"
  "s_nop 0\n"
  "buffer_load_dwordx4 v72, s[8:11], s20 offen lds\n"
  "s_waitcnt lgkmcnt(6)\n"
  "v_mfma_f32_16x16x32_bf16 v[4:7], a[180:183], a[120:123], v[4:7]\n"
  "ds_read_b64_tr_b16 a[164:165], v75 offset:32768\n"
  "ds_read_b64_tr_b16 a[166:167], v75 offset:34816\n"
  "v_mfma_f32_16x16x32_bf16 v[20:23], a[180:183], a[124:127], v[20:23]\n"
  "s_add_u32 s20, s65, 0x180000\n"
  "s_add_u32 m0, s22, 8192\n"
  "s_nop 0\n"
  "buffer_load_dwordx4 v72, s[8:11], s20 offen lds\n"
  "s_add_u32 s20, s65, 0x1b0000\n"
  "s_add_u32 m0, s22, 12288\n"
  "s_nop 0\n"
  "buffer_load_dwordx4 v72, s[8:11], s20 offen lds\n"
  "s_waitcnt lgkmcnt(6)\n"
  "v_mfma_f32_16x16x32_bf16 v[8:11], a[184:187], a[120:123], v[8:11]\n"
  "ds_read_b64_tr_b16 a[168:169], v76 offset:32768\n"
  "ds_read_b64_tr_b16 a[170:171], v76 offset:34816\n"
  "v_mfma_f32_16x16x32_bf16 v[24:27], a[184:187], a[124:127], v[24:27]\n"
  "s_add_u32 s21, s55, 0x18000\n"
  "buffer_load_dwordx4 a[112:115], v73, s[12:15], s21 offen offset:0 sc1\n"
  "s_waitcnt lgkmcnt(6)\n"
  "v_mfma_f32_16x16x32_bf16 v[12:15], a[188:191], a[120:123], v[12:15]\n"
  "ds_read_b64_tr_b16 a[172:173], v77 offset:32768\n"
  "ds_read_b64_tr_b16 a[174:175], v77 offset:34816\n"
  "v_mfma_f32_16x16x32_bf16 v[28:31], a[188:191], a[124:127], v[28:31]\n"
  "buffer_load_dwordx4 a[116:119], v73, s[12:15], s21 offen offset:1024 sc1\n"
  "s_waitcnt lgkmcnt(6)\n"
  "v_mfma_f32_16x16x32_bf16 v[0:3], a[160:163], a[128:131], v[0:3]\n"
  "ds_read_b64_tr_b16 a[176:177], v74 offset:36864\n"
  "ds_read_b64_tr_b16 a[178:179], v74 offset:38912\n"
  "v_mfma_f32_16x16x32_bf16 v[16:19], a[160:163], a[132:135], v[16:19]\n"
  "s_add_u32 s21, s55, 0x1b000\n"
  "buffer_load_dwordx4 a[120:123], v73, s[12:15], s21 offen offset:0 sc1\n"
  "buffer_load_dwordx4 a[124:127], v73, s[12:15], s21 offen offset:1024 sc1\n"
  "s_waitcnt lgkmcnt(6)\n"
  "v_mfma_f32_16x16x32_bf16 v[4:7], a[164:167], a[128:131], v[4:7]\n"
  "ds_read_b64_tr_b16 a[180:181], v75 offset:36864\n"
  "ds_read_b64_tr_b16 a[182:183], v75 offset:38912\n"
  "v_mfma_f32_16x16x32_bf16 v[20:23], a[164:167], a[132:135], v[20:23]\n"
  "s_waitcnt lgkmcnt(6)\n"
  "v_mfma_f32_16x16x32_bf16 v[8:11], a[168:171], a[128:131], v[8:11]\n"
  "ds_read_b64_tr_b16 a[184:185], v76 offset:36864\n"
  "ds_read_b64_tr_b16 a[186:187], v76 offset:38912\n"
  "v_mfma_f32_16x16x32_bf16 v[24:27], a[168:171], a[132:135], v[24:27]\n"
  "s_waitcnt lgkmcnt(6)\n"
  "v_mfma_f32_16x16x32_bf16 v[12:15], a[172:175], a[128:131], v[12:15]\n"
  "ds_read_b64_tr_b16 a[188:189], v77 offset:36864\n"
  "ds_read_b64_tr_b16 a[190:191], v77 offset:38912\n"
  "v_mfma_f32_16x16x32_bf16 v[28:31], a[172:175], a[132:135], v[28:31]\n"
  "s_waitcnt lgkmcnt(6)\n"
  "v_mfma_f32_16x16x32_bf16 v[0:3], a[176:179], a[136:139], v[0:3]\n"
  "ds_read_b64_tr_b16 a[160:161], v74 offset:40960\n"
  "ds_read_b64_tr_b16 a[162:163], v74 offset:43008\n"
  "v_mfma_f32_16x16x32_bf16 v[16:19], a[176:179], a[140:143], v[16:19]\n"
  "s_sub_u32 s21, 0x1e000, s55\n"
  "buffer_load_dwordx4 a[128:131], v73, s[12:15], s21 offen offset:0 sc1\n"
  "buffer_load_dwordx4 a[132:135], v73, s[12:15], s21 offen offset:1024 sc1\n"
  "s_waitcnt lgkmcnt(6)\n"
  "v_mfma_f32_16x16x32_bf16 v[4:7], a[180:183], a[136:139], v[4:7]\n"
  "ds_read_b64_tr_b16 a[164:165], v75 offset:40960\n"
  "ds_read_b64_tr_b16 a[166:167], v75 offset:43008\n"
  "v_mfma_f32_16x16x32_bf16 v[20:23], a[180:183], a[140:143], v[20:23]\n"
  "s_waitcnt lgkmcnt(6)\n"
  "v_mfma_f32_16x16x32_bf16 v[8:11], a[184:187], a[136:139], v[8:11]\n"
  "ds_read_b64_tr_b16 a[168:169], v76 offset:40960\n"
  "ds_read_b64_tr_b16 a[170:171], v76 offset:43008\n"
  "v_mfma_f32_16x16x32_bf16 v[24:27], a[184:187], a[140:143], v[24:27]\n"
  "s_waitcnt lgkmcnt(6)\n"
  "v_mfma_f32_16x16x32_bf16 v[12:15], a[188:191], a[136:139], v[12:15]\n"
  "ds_read_b64_tr_b16 a[172:173], v77 offset:40960\n"
  "ds_read_b64_tr_b16 a[174:175], v77 offset:43008\n"
  "v_mfma_f32_16x16x32_bf16 v[28:31], a[188:191], a[140:143], v[28:31]\n"
  "s_waitcnt vmcnt(52) lgkmcnt(6)\n"
  "v_mfma_f32_16x16x32_bf16 v[0:3], a[160:163], a[144:147], v[0:3]\n"
  "ds_read_b64_tr_b16 a[176:177], v74 offset:45056\n"
  "ds_read_b64_tr_b16 a[178:179], v74 offset:47104\n"
  "v_mfma_f32_16x16x32_bf16 v[16:19], a[160:163], a[148:151], v[16:19]\n"
  "s_sub_u32 s21, 0x1f000, s56\n"
  "buffer_load_dwordx4 a[136:139], v73, s[12:15], s21 offen offset:0 sc1\n"
  "buffer_load_dwordx4 a[140:143], v73, s[12:15], s21 offen offset:1024 sc1\n"
  "s_waitcnt lgkmcnt(6)\n"
  "v_mfma_f32_16x16x32_bf16 v[4:7], a[164:167], a[144:147], v[4:7]\n"
  "ds_read_b64_tr_b16 a[180:181], v75 offset:45056\n"
  "ds_read_b64_tr_b16 a[182:183], v75 offset:47104\n"
  "v_mfma_f32_16x16x32_bf16 v[20:23], a[164:167], a[148:151], v[20:23]\n"
  "s_waitcnt lgkmcnt(6)\n"
  "v_mfma_f32_16x16x32_bf16 v[8:11], a[168:171], a[144:147], v[8:11]\n"
  "ds_read_b64_tr_b16 a[184:185], v76 offset:45056\n"
  "ds_read_b64_tr_b16 a[186:187], v76 offset:47104\n"
  "v_mfma_f32_16x16x32_bf16 v[24:27], a[168:171], a[148:151], v[24:27]\n"
  "s_waitcnt lgkmcnt(6)\n"
  "v_mfma_f32_16x16x32_bf16 v[12:15], a[172:175], a[144:147], v[12:15]\n"
  "ds_read_b64_tr_b16 a[188:189], v77 offset:45056\n"
  "ds_read_b64_tr_b16 a[190:191], v77 offset:47104\n"
  "v_mfma_f32_16x16x32_bf16 v[28:31], a[172:175], a[148:151], v[28:31]\n"
  "s_waitcnt vmcnt(44)\n"
  "s_barrier\n"
  "s_waitcnt lgkmcnt(6)\n"
  "v_mfma_f32_16x16x32_bf16 v[0:3], a[176:179], a[152:155], v[0:3]\n"
  "ds_read_b64_tr_b16 a[160:161], v74 offset:49152\n"
  "ds_read_b64_tr_b16 a[162:163], v74 offset:51200\n"
  "v_mfma_f32_16x16x32_bf16 v[16:19], a[176:179], a[156:159], v[16:19]\n"
  "s_sub_u32 s20, 0x1e0000, s65\n"
  "s_add_u32 m0, s22, 16384\n"
  "s_nop 0\n"
  "buffer_load_dwordx4 v72, s[8:11], s20 offen lds\n"
  "s_sub_u32 s20, 0x1f0000, s66\n"
  "s_add_u32 m0, s22, 20480\n"
  "s_nop 0\n"
  "buffer_load_dwordx4 v72, s[8:11], s20 offen lds\n"
  "s_waitcnt lgkmcnt(6)\n"
  "v_mfma_f32_16x16x32_bf16 v[4:7], a[180:183], a[152:155], v[4:7]\n"
  "ds_read_b64_tr_b16 a[164:165], v75 offset:49152\n"
  "ds_read_b64_tr_b16 a[166:167], v75 offset:51200\n"
  "v_mfma_f32_16x16x32_bf16 v[20:23], a[180:183], a[156:159], v[20:23]\n"
  "s_sub_u32 s20, 0x220000, s65\n"
  "s_add_u32 m0, s22, 24576\n"
  "s_nop 0\n"
  "buffer_load_dwordx4 v72, s[8:11], s20 offen lds\n"
  "s_sub_u32 s20, 0x250000, s65\n"
  "s_add_u32 m0, s22, 28672\n"
  "s_nop 0\n"
  "buffer_load_dwordx4 v72, s[8:11], s20 offen lds\n"
  "s_waitcnt lgkmcnt(6)\n"
  "v_mfma_f32_16x16x32_bf16 v[8:11], a[184:187], a[152:155], v[8:11]\n"
  "ds_read_b64_tr_b16 a[168:169], v76 offset:49152\n"
  "ds_read_b64_tr_b16 a[170:171], v76 offset:51200\n"
  "v_mfma_f32_16x16x32_bf16 v[24:27], a[184:187], a[156:159], v[24:27]\n"
  "s_sub_u32 s21, 0x22000, s55\n"
  "buffer_load_dwordx4 a[144:147], v73, s[12:15], s21 offen offset:0 sc1\n"
  "s_waitcnt lgkmcnt(6)\n"
  "v_mfma_f32_16x16x32_bf16 v[12:15], a[188:191], a[152:155], v[12:15]\n"
  "ds_read_b64_tr_b16 a[172:173], v77 offset:49152\n"
  "ds_read_b64_tr_b16 a[174:175], v77 offset:51200\n"
  "v_mfma_f32_16x16x32_bf16 v[28:31], a[188:191], a[156:159], v[28:31]\n"
  "buffer_load_dwordx4 a[148:151], v73, s[12:15], s21 offen offset:1024 sc1\n"
  "s_waitcnt lgkmcnt(6)\n"
  "v_mfma_f32_16x16x32_bf16 v[0:3], a[160:163], a[0:3], v[0:3]\n"
  "ds_read_b64_tr_b16 a[176:177], v74 offset:53248\n"
  "ds_read_b64_tr_b16 a[178:179], v74 offset:55296\n"
  "v_mfma_f32_16x16x32_bf16 v[16:19], a[160:163], a[4:7], v[16:19]\n"
  "s_sub_u32 s21, 0x25000, s55\n"
  "buffer_load_dwordx4 a[152:155], v73, s[12:15], s21 offen offset:0 sc1\n"
  "buffer_load_dwordx4 a[156:159], v73, s[12:15], s21 offen offset:1024 sc1\n"
  "s_waitcnt lgkmcnt(6)\n"
  "v_mfma_f32_16x16x32_bf16 v[4:7], a[164:167], a[0:3], v[4:7]\n"
  "ds_read_b64_tr_b16 a[180:181], v75 offset:53248\n"
  "ds_read_b64_tr_b16 a[182:183], v75 offset:55296\n"
  "v_mfma_f32_16x16x32_bf16 v[20:23], a[164:167], a[4:7], v[20:23]\n"
  "s_waitcnt lgkmcnt(6)\n"
  "v_mfma_f32_16x16x32_bf16 v[8:11], a[168:171], a[0:3], v[8:11]\n"
  "ds_read_b64_tr_b16 a[184:185], v76 offset:53248\n"
  "ds_read_b64_tr_b16 a[186:187], v76 offset:55296\n"
  "v_mfma_f32_16x16x32_bf16 v[24:27], a[168:171], a[4:7], v[24:27]\n"
  "s_waitcnt lgkmcnt(6)\n"
  "v_mfma_f32_16x16x32_bf16 v[12:15], a[172:175], a[0:3], v[12:15]\n"
  "ds_read_b64_tr_b16 a[188:189], v77 offset:53248\n"
  "ds_read_b64_tr_b16 a[190:191], v77 offset:55296\n"
  "v_mfma_f32_16x16x32_bf16 v[28:31], a[172:175], a[4:7], v[28:31]\n"
  "s_waitcnt lgkmcnt(6)\n"
  "v_mfma_f32_16x16x32_bf16 v[0:3], a[176:179], a[8:11], v[0:3]\n"
  "ds_read_b64_tr_b16 a[160:161], v74 offset:57344\n"
  "ds_read_b64_tr_b16 a[162:163], v74 offset:59392\n"
  "v_mfma_f32_16x16x32_bf16 v[16:19], a[176:179], a[12:15], v[16:19]\n"
  "s_add_u32 s21, s55, 0x26000\n"
  "buffer_load_dwordx4 a[0:3], v73, s[12:15], s21 offen offset:0 sc1\n"
  "buffer_load_dwordx4 a[4:7], v73, s[12:15], s21 offen offset:1024 sc1\n"
  "s_waitcnt lgkmcnt(6)\n"
  "v_mfma_f32_16x16x32_bf16 v[4:7], a[180:183], a[8:11], v[4:7]\n"
  "ds_read_b64_tr_b16 a[164:165], v75 offset:57344\n"
  "ds_read_b64_tr_b16 a[166:167], v75 offset:59392\n"
  "v_mfma_f32_16x16x32_bf16 v[20:23], a[180:183], a[12:15], v[20:23]\n"
  "s_waitcnt lgkmcnt(6)\n"
  "v_mfma_f32_16x16x32_bf16 v[8:11], a[184:187], a[8:11], v[8:11]\n"
  "ds_read_b64_tr_b16 a[168:169], v76 offset:57344\n"
  "ds_read_b64_tr_b16 a[170:171], v76 offset:59392\n"
  "v_mfma_f32_16x16x32_bf16 v[24:27], a[184:187], a[12:15], v[24:27]\n"
  "s_waitcnt lgkmcnt(6)\n"
  "v_mfma_f32_16x16x32_bf16 v[12:15], a[188:191], a[8:11], v[12:15]\n"
  "ds_read_b64_tr_b16 a[172:173], v77 offset:57344\n"
  "ds_read_b64_tr_b16 a[174:175], v77 offset:59392\n"
  "v_mfma_f32_16x16x32_bf16 v[28:31], a[188:191], a[12:15], v[28:31]\n"
  "s_waitcnt vmcnt(52) lgkmcnt(6)\n"
  "v_mfma_f32_16x16x32_bf16 v[0:3], a[160:163], a[16:19], v[0:3]\n"
  "ds_read_b64_tr_b16 a[176:177], v74 offset:61440\n"
  "ds_read_b64_tr_b16 a[178:179], v74 offset:63488\n"
  "v_mfma_f32_16x16x32_bf16 v[16:19], a[160:163], a[20:23], v[16:19]\n"
  "s_add_u32 s21, s55, 0x29000\n"
  "buffer_load_dwordx4 a[8:11], v73, s[12:15], s21 offen offset:0 sc1\n"
  "buffer_load_dwordx4 a[12:15], v73, s[12:15], s21 offen offset:1024 sc1\n"
  "s_waitcnt lgkmcnt(6)\n"
  "v_mfma_f32_16x16x32_bf16 v[4:7], a[164:167], a[16:19], v[4:7]\n"
  "ds_read_b64_tr_b16 a[180:181], v75 offset:61440\n"
  "ds_read_b64_tr_b16 a[182:183], v75 offset:63488\n"
  "v_mfma_f32_16x16x32_bf16 v[20:23], a[164:167], a[20:23], v[20:23]\n"
  "s_waitcnt lgkmcnt(6)\n"
  "v_mfma_f32_16x16x32_bf16 v[8:11], a[168:171], a[16:19], v[8:11]\n"
  "ds_read_b64_tr_b16 a[184:185], v76 offset:61440\n"
  "ds_read_b64_tr_b16 a[186:187], v76 offset:63488\n"
  "v_mfma_f32_16x16x32_bf16 v[24:27], a[168:171], a[20:23], v[24:27]\n"
  "s_waitcnt lgkmcnt(6)\n"
  "v_mfma_f32_16x16x32_bf16 v[12:15], a[172:175], a[16:19], v[12:15]\n"
  "ds_read_b64_tr_b16 a[188:189], v77 offset:61440\n"
  "ds_read_b64_tr_b16 a[190:191], v77 offset:63488\n"
  "v_mfma_f32_16x16x32_bf16 v[28:31], a[172:175], a[20:23], v[28:31]\n"
  "s_waitcnt vmcnt(44)\n"
  "s_barrier\n"
  "s_waitcnt lgkmcnt(6)\n"
  "v_mfma_f32_16x16x32_bf16 v[0:3], a[176:179], a[24:27], v[0:3]\n"
  "ds_read_b64_tr_b16 a[160:161], v80 offset:0\n"
  "ds_read_b64_tr_b16 a[162:163], v80 offset:2048\n"
  "v_mfma_f32_16x16x32_bf16 v[16:19], a[176:179], a[28:31], v[16:19]\n"
  "s_add_u32 s20, s65, 0x260000\n"
  "s_add_u32 m0, s22, 32768\n"
  "s_nop 0\n"
  "buffer_load_dwordx4 v72, s[8:11], s20 offen lds\n"
  "s_add_u32 s20, s65, 0x290000\n"
  "s_add_u32 m0, s22, 36864\n"
  "s_nop 0\n"
  "buffer_load_dwordx4 v72, s[8:11], s20 offen lds\n"
  "s_waitcnt lgkmcnt(6)\n"
  "v_mfma_f32_16x16x32_bf16 v[4:7], a[180:183], a[24:27], v[4:7]\n"
  "ds_read_b64_tr_b16 a[164:165], v81 offset:0\n"
  "ds_read_b64_tr_b16 a[166:167], v81 offset:2048\n"
  "v_mfma_f32_16x16x32_bf16 v[20:23], a[180:183], a[28:31], v[20:23]\n"
  "s_add_u32 s20, s66, 0x2c0000\n"
  "s_add_u32 m0, s22, 40960\n"
  "s_nop 0\n"
  "buffer_load_dwordx4 v72, s[8:11], s20 offen lds\n"
  "s_add_u32 s20, s65, 0x2d0000\n"
  "s_add_u32 m0, s22, 45056\n"
  "s_nop 0\n"
  "buffer_load_dwordx4 v72, s[8:11], s20 offen lds\n"
  "s_waitcnt lgkmcnt(6)\n"
  "v_mfma_f32_16x16x32_bf16 v[8:11], a[184:187], a[24:27], v[8:11]\n"
  "ds_read_b64_tr_b16 a[168:169], v82 offset:0\n"
  "ds_read_b64_tr_b16 a[170:171], v82 offset:2048\n"
  "v_mfma_f32_16x16x32_bf16 v[24:27], a[184:187], a[28:31], v[24:27]\n"
  "s_add_u32 s21, s56, 0x2c000\n"
  "buffer_load_dwordx4 a[16:19], v73, s[12:15], s21 offen offset:0 sc1\n"
  "s_waitcnt lgkmcnt(6)\n"
  "v_mfma_f32_16x16x32_bf16 v[12:15], a[188:191], a[24:27], v[12:15]\n"
  "ds_read_b64_tr_b16 a[172:173], v83 offset:0\n"
  "ds_read_b64_tr_b16 a[174:175], v83 offset:2048\n"
  "v_mfma_f32_16x16x32_bf16 v[28:31], a[188:191], a[28:31], v[28:31]\n"
  "buffer_load_dwordx4 a[20:23], v73, s[12:15], s21 offen offset:1024 sc1\n"
  "s_waitcnt lgkmcnt(6)\n"
  "v_mfma_f32_16x16x32_bf16 v[0:3], a[160:163], a[32:35], v[0:3]\n"
  "ds_read_b64_tr_b16 a[176:177], v80 offset:4096\n"
  "ds_read_b64_tr_b16 a[178:179], v80 offset:6144\n"
  "v_mfma_f32_16x16x32_bf16 v[16:19], a[160:163], a[36:39], v[16:19]\n"
  "s_add_u32 s21, s55, 0x2d000\n"
  "buffer_load_dwordx4 a[24:27], v73, s[12:15], s21 offen offset:0 sc1\n"
  "buffer_load_dwordx4 a[28:31], v73, s[12:15], s21 offen offset:1024 sc1\n"
  "s_waitcnt lgkmcnt(6)\n"
  "v_mfma_f32_16x16x32_bf16 v[4:7], a[164:167], a[32:35], v[4:7]\n"
  "ds_read_b64_tr_b16 a[180:181], v81 offset:4096\n"
  "ds_read_b64_tr_b16 a[182:183], v81 offset:6144\n"
  "v_mfma_f32_16x16x32_bf16 v[20:23], a[164:167], a[36:39], v[20:23]\n"
  "s_waitcnt lgkmcnt(6)\n"
  "v_mfma_f32_16x16x32_bf16 v[8:11], a[168:171], a[32:35], v[8:11]\n"
  "ds_read_b64_tr_b16 a[184:185], v82 offset:4096\n"
  "ds_read_b64_tr_b16 a[186:187], v82 offset:6144\n"
  "v_mfma_f32_16x16x32_bf16 v[24:27], a[168:171], a[36:39], v[24:27]\n"
  "s_waitcnt lgkmcnt(6)\n"
  "v_mfma_f32_16x16x32_bf16 v[12:15], a[172:175], a[32:35], v[12:15]\n"
  "ds_read_b64_tr_b16 a[188:189], v83 offset:4096\n"
  "ds_read_b64_tr_b16 a[190:191], v83 offset:6144\n"
  "v_mfma_f32_16x16x32_bf16 v[28:31], a[172:175], a[36:39], v[28:31]\n"
  "s_waitcnt lgkmcnt(6)\n"
  "v_mfma_f32_16x16x32_bf16 v[0:3], a[176:179], a[40:43], v[0:3]\n"
  "ds_read_b64_tr_b16 a[160:161], v80 offset:8192\n"
  "ds_read_b64_tr_b16 a[162:163], v80 offset:10240\n"
  "v_mfma_f32_16x16x32_bf16 v[16:19], a[176:179], a[44:47], v[16:19]\n"
  "s_sub_u32 s21, 0x30000, s55\n"
  "buffer_load_dwordx4 a[32:35], v73, s[12:15], s21 offen offset:0 sc1\n"
  "buffer_load_dwordx4 a[36:39], v73, s[12:15], s21 offen offset:1024 sc1\n"
  "s_waitcnt lgkmcnt(6)\n"
  "v_mfma_f32_16x16x32_bf16 v[4:7], a[180:183], a[40:43], v[4:7]\n"
  "ds_read_b64_tr_b16 a[164:165], v81 offset:8192\n"
  "ds_read_b64_tr_b16 a[166:167], v81 offset:10240\n"
  "v_mfma_f32_16x16x32_bf16 v[20:23], a[180:183], a[44:47], v[20:23]\n"
  "s_waitcnt lgkmcnt(6)\n"
  "v_mfma_f32_16x16x32_bf16 v[8:11], a[184:187], a[40:43], v[8:11]\n"
  "ds_read_b64_tr_b16 a[168:169], v82 offset:8192\n"
  "ds_read_b64_tr_b16 a[170:171], v82 offset:10240\n"
  "v_mfma_f32_16x16x32_bf16 v[24:27], a[184:187], a[44:47], v[24:27]\n"
  "s_waitcnt lgkmcnt(6)\n"
  "v_mfma_f32_16x16x32_bf16 v[12:15], a[188:191], a[40:43], v[12:15]\n"
  "ds_read_b64_tr_b16 a[172:173], v83 offset:8192\n"
  "ds_read_b64_tr_b16 a[174:175], v83 offset:10240\n"
  "v_mfma_f32_16x16x32_bf16 v[28:31], a[188:191], a[44:47], v[28:31]\n"
  "s_waitcnt vmcnt(52) lgkmcnt(6)\n"
  "v_mfma_f32_16x16x32_bf16 v[0:3], a[160:163], a[48:51], v[0:3]\n"
  "ds_read_b64_tr_b16 a[176:177], v80 offset:12288\n"
  "ds_read_b64_tr_b16 a[178:179], v80 offset:14336\n"
  "v_mfma_f32_16x16x32_bf16 v[16:19], a[160:163], a[52:55], v[16:19]\n"
  "s_sub_u32 s21, 0x33000, s55\n"
  "buffer_load_dwordx4 a[40:43], v73, s[12:15], s21 offen offset:0 sc1\n"
  "buffer_load_dwordx4 a[44:47], v73, s[12:15], s21 offen offset:1024 sc1\n"
  "s_waitcnt lgkmcnt(6)\n"
  "v_mfma_f32_16x16x32_bf16 v[4:7], a[164:167], a[48:51], v[4:7]\n"
  "ds_read_b64_tr_b16 a[180:181], v81 offset:12288\n"
  "ds_read_b64_tr_b16 a[182:183], v81 offset:14336\n"
  "v_mfma_f32_16x16x32_bf16 v[20:23], a[164:167], a[52:55], v[20:23]\n"
  "s_waitcnt lgkmcnt(6)\n"
  "v_mfma_f32_16x16x32_bf16 v[8:11], a[168:171], a[48:51], v[8:11]\n"
  "ds_read_b64_tr_b16 a[184:185], v82 offset:12288\n"
  "ds_read_b64_tr_b16 a[186:187], v82 offset:14336\n"
  "v_mfma_f32_16x16x32_bf16 v[24:27], a[168:171], a[52:55], v[24:27]\n"
  "s_waitcnt lgkmcnt(6)\n"
  "v_mfma_f32_16x16x32_bf16 v[12:15], a[172:175], a[48:51], v[12:15]\n"
  "ds_read_b64_tr_b16 a[188:189], v83 offset:12288\n"
  "ds_read_b64_tr_b16 a[190:191], v83 offset:14336\n"
  "v_mfma_f32_16x16x32_bf16 v[28:31], a[172:175], a[52:55], v[28:31]\n"
  "s_waitcnt vmcnt(44)\n"
  "s_barrier\n"
  "s_waitcnt lgkmcnt(6)\n"
  "v_mfma_f32_16x16x32_bf16 v[0:3], a[176:179], a[56:59], v[0:3]\n"
  "ds_read_b64_tr_b16 a[160:161], v80 offset:16384\n"
  "ds_read_b64_tr_b16 a[162:163], v80 offset:18432\n"
  "v_mfma_f32_16x16x32_bf16 v[16:19], a[176:179], a[60:63], v[16:19]\n"
  "s_sub_u32 s20, 0x300000, s65\n"
  "s_add_u32 m0, s22, 49152\n"
  "s_nop 0\n"
  "buffer_load_dwordx4 v72, s[8:11], s20 offen lds\n"
  "s_sub_u32 s20, 0x330000, s65\n"
  "s_add_u32 m0, s22, 53248\n"
  "s_nop 0\n"
  "buffer_load_dwordx4 v72, s[8:11], s20 offen lds\n"
  "s_waitcnt lgkmcnt(6)\n"
  "v_mfma_f32_16x16x32_bf16 v[4:7], a[180:183], a[56:59], v[4:7]\n"
  "ds_read_b64_tr_b16 a[164:165], v81 offset:16384\n"
  "ds_read_b64_tr_b16 a[166:167], v81 offset:18432\n"
  "v_mfma_f32_16x16x32_bf16 v[20:23], a[180:183], a[60:63], v[20:23]\n"
  "s_sub_u32 s20, 0x340000, s66\n"
  "s_add_u32 m0, s22, 57344\n"
  "s_nop 0\n"
  "buffer_load_dwordx4 v72, s[8:11], s20 offen lds\n"
  "s_sub_u32 s20, 0x370000, s65\n"
  "s_add_u32 m0, s22, 61440\n"
  "s_nop 0\n"
  "buffer_load_dwordx4 v72, s[8:11], s20 offen lds\n"
  "s_waitcnt lgkmcnt(6)\n"
  "v_mfma_f32_16x16x32_bf16 v[8:11], a[184:187], a[56:59], v[8:11]\n"
  "ds_read_b64_tr_b16 a[168:169], v82 offset:16384\n"
  "ds_read_b64_tr_b16 a[170:171], v82 offset:18432\n"
  "v_mfma_f32_16x16x32_bf16 v[24:27], a[184:187], a[60:63], v[24:27]\n"
  "s_sub_u32 s21, 0x34000, s56\n"
  "buffer_load_dwordx4 a[48:51], v73, s[12:15], s21 offen offset:0 sc1\n"
  "s_waitcnt lgkmcnt(6)\n"
  "v_mfma_f32_16x16x32_bf16 v[12:15], a[188:191], a[56:59], v[12:15]\n"
  "ds_read_b64_tr_b16 a[172:173], v83 offset:16384\n"
  "ds_read_b64_tr_b16 a[174:175], v83 offset:18432\n"
  "v_mfma_f32_16x16x32_bf16 v[28:31], a[188:191], a[60:63], v[28:31]\n"
  "buffer_load_dwordx4 a[52:55], v73, s[12:15], s21 offen offset:1024 sc1\n"
  "s_waitcnt lgkmcnt(6)\n"
  "v_mfma_f32_16x16x32_bf16 v[0:3], a[160:163], a[64:67], v[0:3]\n"
  "ds_read_b64_tr_b16 a[176:177], v80 offset:20480\n"
  "ds_read_b64_tr_b16 a[178:179], v80 offset:22528\n"
  "v_mfma_f32_16x16x32_bf16 v[16:19], a[160:163], a[68:71], v[16:19]\n"
  "s_sub_u32 s21, 0x37000, s55\n"
  "buffer_load_dwordx4 a[56:59], v73, s[12:15], s21 offen offset:0 sc1\n"
  "buffer_load_dwordx4 a[60:63], v73, s[12:15], s21 offen offset:1024 sc1\n"
  "s_waitcnt lgkmcnt(6)\n"
  "v_mfma_f32_16x16x32_bf16 v[4:7], a[164:167], a[64:67], v[4:7]\n"
  "ds_read_b64_tr_b16 a[180:181], v81 offset:20480\n"
  "ds_read_b64_tr_b16 a[182:183], v81 offset:22528\n"
  "v_mfma_f32_16x16x32_bf16 v[20:23], a[164:167], a[68:71], v[20:23]\n"
  "s_waitcnt lgkmcnt(6)\n"
  "v_mfma_f32_16x16x32_bf16 v[8:11], a[168:171], a[64:67], v[8:11]\n"
  "ds_read_b64_tr_b16 a[184:185], v82 offset:20480\n"
  "ds_read_b64_tr_b16 a[186:187], v82 offset:22528\n"
  "v_mfma_f32_16x16x32_bf16 v[24:27], a[168:171], a[68:71], v[24:27]\n"
  "s_waitcnt lgkmcnt(6)\n"
  "v_mfma_f32_16x16x32_bf16 v[12:15], a[172:175], a[64:67], v[12:15]\n"
  "ds_read_b64_tr_b16 a[188:189], v83 offset:20480\n"
  "ds_read_b64_tr_b16 a[190:191], v83 offset:22528\n"
  "v_mfma_f32_16x16x32_bf16 v[28:31], a[172:175], a[68:71], v[28:31]\n"
  "s_waitcnt lgkmcnt(6)\n"
  "v_mfma_f32_16x16x32_bf16 v[0:3], a[176:179], a[72:75], v[0:3]\n"
  "ds_read_b64_tr_b16 a[160:161], v80 offset:24576\n"
  "ds_read_b64_tr_b16 a[162:163], v80 offset:26624\n"
  "v_mfma_f32_16x16x32_bf16 v[16:19], a[176:179], a[76:79], v[16:19]\n"
  "s_add_u32 s21, s56, 0x3a000\n"
  "buffer_load_dwordx4 a[64:67], v73, s[12:15], s21 offen offset:0 sc1\n"
  "buffer_load_dwordx4 a[68:71], v73, s[12:15], s21 offen offset:1024 sc1\n"
  "s_waitcnt lgkmcnt(6)\n"
  "v_mfma_f32_16x16x32_bf16 v[4:7], a[180:183], a[72:75], v[4:7]\n"
  "ds_read_b64_tr_b16 a[164:165], v81 offset:24576\n"
  "ds_read_b64_tr_b16 a[166:167], v81 offset:26624\n"
  "v_mfma_f32_16x16x32_bf16 v[20:23], a[180:183], a[76:79], v[20:23]\n"
  "s_waitcnt lgkmcnt(6)\n"
  "v_mfma_f32_16x16x32_bf16 v[8:11], a[184:187], a[72:75], v[8:11]\n"
  "ds_read_b64_tr_b16 a[168:169], v82 offset:24576\n"
  "ds_read_b64_tr_b16 a[170:171], v82 offset:26624\n"
  "v_mfma_f32_16x16x32_bf16 v[24:27], a[184:187], a[76:79], v[24:27]\n"
  "s_waitcnt lgkmcnt(6)\n"
  "v_mfma_f32_16x16x32_bf16 v[12:15], a[188:191], a[72:75], v[12:15]\n"
  "ds_read_b64_tr_b16 a[172:173], v83 offset:24576\n"
  "ds_read_b64_tr_b16 a[174:175], v83 offset:26624\n"
  "v_mfma_f32_16x16x32_bf16 v[28:31], a[188:191], a[76:79], v[28:31]\n"
  "s_waitcnt vmcnt(52) lgkmcnt(6)\n"
  "v_mfma_f32_16x16x32_bf16 v[0:3], a[160:163], a[80:83], v[0:3]\n"
  "ds_read_b64_tr_b16 a[176:177], v80 offset:28672\n"
  "ds_read_b64_tr_b16 a[178:179], v80 offset:30720\n"
  "v_mfma_f32_16x16x32_bf16 v[16:19], a[160:163], a[84:87], v[16:19]\n"
  "s_add_u32 s21, s55, 0x3b000\n"
  "buffer_load_dwordx4 a[72:75], v73, s[12:15], s21 offen offset:0 sc1\n"
  "buffer_load_dwordx4 a[76:79], v73, s[12:15], s21 offen offset:1024 sc1\n"
  "s_waitcnt lgkmcnt(6)\n"
  "v_mfma_f32_16x16x32_bf16 v[4:7], a[164:167], a[80:83], v[4:7]\n"
  "ds_read_b64_tr_b16 a[180:181], v81 offset:28672\n"
  "ds_read_b64_tr_b16 a[182:183], v81 offset:30720\n"
  "v_mfma_f32_16x16x32_bf16 v[20:23], a[164:167], a[84:87], v[20:23]\n"
  "s_waitcnt lgkmcnt(6)\n"
  "v_mfma_f32_16x16x32_bf16 v[8:11], a[168:171], a[80:83], v[8:11]\n"
  "ds_read_b64_tr_b16 a[184:185], v82 offset:28672\n"
  "ds_read_b64_tr_b16 a[186:187], v82 offset:30720\n"
  "v_mfma_f32_16x16x32_bf16 v[24:27], a[168:171], a[84:87], v[24:27]\n"
  "s_waitcnt lgkmcnt(6)\n"
  "v_mfma_f32_16x16x32_bf16 v[12:15], a[172:175], a[80:83], v[12:15]\n"
  "ds_read_b64_tr_b16 a[188:189], v83 offset:28672\n"
  "ds_read_b64_tr_b16 a[190:191], v83 offset:30720\n"
  "v_mfma_f32_16x16x32_bf16 v[28:31], a[172:175], a[84:87], v[28:31]\n"
  "s_waitcnt vmcnt(44)\n"
  "s_barrier\n"
  "s_waitcnt lgkmcnt(6)\n"
  "v_mfma_f32_16x16x32_bf16 v[0:3], a[176:179], a[88:91], v[0:3]\n"
  "ds_read_b64_tr_b16 a[160:161], v74 offset:0\n"
  "ds_read_b64_tr_b16 a[162:163], v74 offset:2048\n"
  "v_mfma_f32_16x16x32_bf16 v[16:19], a[176:179], a[92:95], v[16:19]\n"
  "s_add_u32 s20, s66, 0x3a0000\n"
  "s_add_u32 m0, s22, 65536\n"
  "s_nop 0\n"
  "buffer_load_dwordx4 v72, s[8:11], s20 offen lds\n"
  "s_add_u32 s20, s65, 0x3b0000\n"
  "s_add_u32 m0, s22, 69632\n"
  "s_nop 0\n"
  "buffer_load_dwordx4 v72, s[8:11], s20 offen lds\n"
  "s_waitcnt lgkmcnt(6)\n"
  "v_mfma_f32_16x16x32_bf16 v[4:7], a[180:183], a[88:91], v[4:7]\n"
  "ds_read_b64_tr_b16 a[164:165], v75 offset:0\n"
  "ds_read_b64_tr_b16 a[166:167], v75 offset:2048\n"
  "v_mfma_f32_16x16x32_bf16 v[20:23], a[180:183], a[92:95], v[20:23]\n"
  "s_add_u32 s20, s65, 0x3e0000\n"
  "s_add_u32 m0, s22, 73728\n"
  "s_nop 0\n"
  "buffer_load_dwordx4 v72, s[8:11], s20 offen lds\n"
  "s_add_u32 s20, s66, 0x410000\n"
  "s_add_u32 m0, s22, 77824\n"
  "s_nop 0\n"
  "buffer_load_dwordx4 v72, s[8:11], s20 offen lds\n"
  "s_waitcnt lgkmcnt(6)\n"
  "v_mfma_f32_16x16x32_bf16 v[8:11], a[184:187], a[88:91], v[8:11]\n"
  "ds_read_b64_tr_b16 a[168:169], v76 offset:0\n"
  "ds_read_b64_tr_b16 a[170:171], v76 offset:2048\n"
  "v_mfma_f32_16x16x32_bf16 v[24:27], a[184:187], a[92:95], v[24:27]\n"
  "s_add_u32 s21, s55, 0x3e000\n"
  "buffer_load_dwordx4 a[80:83], v73, s[12:15], s21 offen offset:0 sc1\n"
  "s_waitcnt lgkmcnt(6)\n"
  "v_mfma_f32_16x16x32_bf16 v[12:15], a[188:191], a[88:91], v[12:15]\n"
  "ds_read_b64_tr_b16 a[172:173], v77 offset:0\n"
  "ds_read_b64_tr_b16 a[174:175], v77 offset:2048\n"
  "v_mfma_f32_16x16x32_bf16 v[28:31], a[188:191], a[92:95], v[28:31]\n"
  "buffer_load_dwordx4 a[84:87], v73, s[12:15], s21 offen offset:1024 sc1\n"
  "s_waitcnt lgkmcnt(6)\n"
  "v_mfma_f32_16x16x32_bf16 v[0:3], a[160:163], a[96:99], v[0:3]\n"
  "ds_read_b64_tr_b16 a[176:177], v74 offset:4096\n"
  "ds_read_b64_tr_b16 a[178:179], v74 offset:6144\n"
  "v_mfma_f32_16x16x32_bf16 v[16:19], a[160:163], a[100:103], v[16:19]\n"
  "s_add_u32 s21, s56, 0x41000\n"
  "buffer_load_dwordx4 a[88:91], v73, s[12:15], s21 offen offset:0 sc1\n"
  "buffer_load_dwordx4 a[92:95], v73, s[12:15], s21 offen offset:1024 sc1\n"
  "s_waitcnt lgkmcnt(6)\n"
  "v_mfma_f32_16x16x32_bf16 v[4:7], a[164:167], a[96:99], v[4:7]\n"
  "ds_read_b64_tr_b16 a[180:181], v75 offset:4096\n"
  "ds_read_b64_tr_b16 a[182:183], v75 offset:6144\n"
  "v_mfma_f32_16x16x32_bf16 v[20:23], a[164:167], a[100:103], v[20:23]\n"
  "s_waitcnt lgkmcnt(6)\n"
  "v_mfma_f32_16x16x32_bf16 v[8:11], a[168:171], a[96:99], v[8:11]\n"
  "ds_read_b64_tr_b16 a[184:185], v76 offset:4096\n"
  "ds_read_b64_tr_b16 a[186:187], v76 offset:6144\n"
  "v_mfma_f32_16x16x32_bf16 v[24:27], a[168:171], a[100:103], v[24:27]\n"
  "s_waitcnt lgkmcnt(6)\n"
  "v_mfma_f32_16x16x32_bf16 v[12:15], a[172:175], a[96:99], v[12:15]\n"
  "ds_read_b64_tr_b16 a[188:189], v77 offset:4096\n"
  "ds_read_b64_tr_b16 a[190:191], v77 offset:6144\n"
  "v_mfma_f32_16x16x32_bf16 v[28:31], a[172:175], a[100:103], v[28:31]\n"
  "s_waitcnt lgkmcnt(6)\n"
  "v_mfma_f32_16x16x32_bf16 v[0:3], a[176:179], a[104:107], v[0:3]\n"
  "ds_read_b64_tr_b16 a[160:161], v74 offset:8192\n"
  "ds_read_b64_tr_b16 a[162:163], v74 offset:10240\n"
  "v_mfma_f32_16x16x32_bf16 v[16:19], a[176:179], a[108:111], v[16:19]\n"
  "s_sub_u32 s21, 0x42000, s56\n"
  "buffer_load_dwordx4 a[96:99], v73, s[12:15], s21 offen offset:0 sc1\n"
  "buffer_load_dwordx4 a[100:103], v73, s[12:15], s21 offen offset:1024 sc1\n"
  "s_waitcnt lgkmcnt(6)\n"
  "v_mfma_f32_16x16x32_bf16 v[4:7], a[180:183], a[104:107], v[4:7]\n"
  "ds_read_b64_tr_b16 a[164:165], v75 offset:8192\n"
  "ds_read_b64_tr_b16 a[166:167], v75 offset:10240\n"
  "v_mfma_f32_16x16x32_bf16 v[20:23], a[180:183], a[108:111], v[20:23]\n"
  "s_waitcnt lgkmcnt(6)\n"
  "v_mfma_f32_16x16x32_bf16 v[8:11], a[184:187], a[104:107], v[8:11]\n"
  "ds_read_b64_tr_b16 a[168:169], v76 offset:8192\n"
  "ds_read_b64_tr_b16 a[170:171], v76 offset:10240\n"
  "v_mfma_f32_16x16x32_bf16 v[24:27], a[184:187], a[108:111], v[24:27]\n"
  "s_waitcnt lgkmcnt(6)\n"
  "v_mfma_f32_16x16x32_bf16 v[12:15], a[188:191], a[104:107], v[12:15]\n"
  "ds_read_b64_tr_b16 a[172:173], v77 offset:8192\n"
  "ds_read_b64_tr_b16 a[174:175], v77 offset:10240\n"
  "v_mfma_f32_16x16x32_bf16 v[28:31], a[188:191], a[108:111], v[28:31]\n"
  "s_waitcnt vmcnt(52) lgkmcnt(6)\n"
  "v_mfma_f32_16x16x32_bf16 v[0:3], a[160:163], a[112:115], v[0:3]\n"
  "ds_read_b64_tr_b16 a[176:177], v74 offset:12288\n"
  "ds_read_b64_tr_b16 a[178:179], v74 offset:14336\n"
  "v_mfma_f32_16x16x32_bf16 v[16:19], a[160:163], a[116:119], v[16:19]\n"
  "s_sub_u32 s21, 0x45000, s55\n"
  "buffer_load_dwordx4 a[104:107], v73, s[12:15], s21 offen offset:0 sc1\n"
  "buffer_load_dwordx4 a[108:111], v73, s[12:15], s21 offen offset:1024 sc1\n"
  "s_waitcnt lgkmcnt(6)\n"
  "v_mfma_f32_16x16x32_bf16 v[4:7], a[164:167], a[112:115], v[4:7]\n"
  "ds_read_b64_tr_b16 a[180:181], v75 offset:12288\n"
  "ds_read_b64_tr_b16 a[182:183], v75 offset:14336\n"
  "v_mfma_f32_16x16x32_bf16 v[20:23], a[164:167], a[116:119], v[20:23]\n"
  "s_waitcnt lgkmcnt(6)\n"
  "v_mfma_f32_16x16x32_bf16 v[8:11], a[168:171], a[112:115], v[8:11]\n"
  "ds_read_b64_tr_b16 a[184:185], v76 offset:12288\n"
  "ds_read_b64_tr_b16 a[186:187], v76 offset:14336\n"
  "v_mfma_f32_16x16x32_bf16 v[24:27], a[168:171], a[116:119], v[24:27]\n"
  "s_waitcnt lgkmcnt(6)\n"
  "v_mfma_f32_16x16x32_bf16 v[12:15], a[172:175], a[112:115], v[12:15]\n"
  "ds_read_b64_tr_b16 a[188:189], v77 offset:12288\n"
  "ds_read_b64_tr_b16 a[190:191], v77 offset:14336\n"
  "v_mfma_f32_16x16x32_bf16 v[28:31], a[172:175], a[116:119], v[28:31]\n"
  "s_waitcnt vmcnt(44)\n"
  "s_barrier\n"
  "s_waitcnt lgkmcnt(6)\n"
  "v_mfma_f32_16x16x32_bf16 v[0:3], a[176:179], a[120:123], v[0:3]\n"
  "ds_read_b64_tr_b16 a[160:161], v74 offset:16384\n"
  "ds_read_b64_tr_b16 a[162:163], v74 offset:18432\n"
  "v_mfma_f32_16x16x32_bf16 v[16:19], a[176:179], a[124:127], v[16:19]\n"
  "s_sub_u32 s20, 0x420000, s66\n"
  "s_add_u32 m0, s22, 81920\n"
  "s_nop 0\n"
  "buffer_load_dwordx4 v72, s[8:11], s20 offen lds\n"
  "s_sub_u32 s20, 0x450000, s65\n"
  "s_add_u32 m0, s22, 86016\n"
  "s_nop 0\n"
  "buffer_load_dwordx4 v72, s[8:11], s20 offen lds\n"
  "s_waitcnt lgkmcnt(6)\n"
  "v_mfma_f32_16x16x32_bf16 v[4:7], a[180:183], a[120:123], v[4:7]\n"
  "ds_read_b64_tr_b16 a[164:165], v75 offset:16384\n"
  "ds_read_b64_tr_b16 a[166:167], v75 offset:18432\n"
  "v_mfma_f32_16x16x32_bf16 v[20:23], a[180:183], a[124:127], v[20:23]\n"
  "s_sub_u32 s20, 0x480000, s65\n"
  "s_add_u32 m0, s22, 90112\n"
  "s_nop 0\n"
  "buffer_load_dwordx4 v72, s[8:11], s20 offen lds\n"
  "s_sub_u32 s20, 0x490000, s66\n"
  "s_add_u32 m0, s22, 94208\n"
  "s_nop 0\n"
  "buffer_load_dwordx4 v72, s[8:11], s20 offen lds\n"
  "s_waitcnt lgkmcnt(6)\n"
  "v_mfma_f32_16x16x32_bf16 v[8:11], a[184:187], a[120:123], v[8:11]\n"
  "ds_read_b64_tr_b16 a[168:169], v76 offset:16384\n"
  "ds_read_b64_tr_b16 a[170:171], v76 offset:18432\n"
  "v_mfma_f32_16x16x32_bf16 v[24:27], a[184:187], a[124:127], v[24:27]\n"
  "s_sub_u32 s21, 0x48000, s55\n"
  "buffer_load_dwordx4 a[112:115], v73, s[12:15], s21 offen offset:0 sc1\n"
  "s_waitcnt lgkmcnt(6)\n"
  "v_mfma_f32_16x16x32_bf16 v[12:15], a[188:191], a[120:123], v[12:15]\n"
  "ds_read_b64_tr_b16 a[172:173], v77 offset:16384\n"
  "ds_read_b64_tr_b16 a[174:175], v77 offset:18432\n"
  "v_mfma_f32_16x16x32_bf16 v[28:31], a[188:191], a[124:127], v[28:31]\n"
  "buffer_load_dwordx4 a[116:119], v73, s[12:15], s21 offen offset:1024 sc1\n"
  "s_waitcnt lgkmcnt(6)\n"
  "v_mfma_f32_16x16x32_bf16 v[0:3], a[160:163], a[128:131], v[0:3]\n"
  "ds_read_b64_tr_b16 a[176:177], v74 offset:20480\n"
  "ds_read_b64_tr_b16 a[178:179], v74 offset:22528\n"
  "v_mfma_f32_16x16x32_bf16 v[16:19], a[160:163], a[132:135], v[16:19]\n"
  "s_sub_u32 s21, 0x49000, s56\n"
  "buffer_load_dwordx4 a[120:123], v73, s[12:15], s21 offen offset:0 sc1\n"
  "buffer_load_dwordx4 a[124:127], v73, s[12:15], s21 offen offset:1024 sc1\n"
  "s_waitcnt lgkmcnt(6)\n"
  "v_mfma_f32_16x16x32_bf16 v[4:7], a[164:167], a[128:131], v[4:7]\n"
  "ds_read_b64_tr_b16 a[180:181], v75 offset:20480\n"
  "ds_read_b64_tr_b16 a[182:183], v75 offset:22528\n"
  "v_mfma_f32_16x16x32_bf16 v[20:23], a[164:167], a[132:135], v[20:23]\n"
  "s_waitcnt lgkmcnt(6)\n"
  "v_mfma_f32_16x16x32_bf16 v[8:11], a[168:171], a[128:131], v[8:11]\n"
  "ds_read_b64_tr_b16 a[184:185], v76 offset:20480\n"
  "ds_read_b64_tr_b16 a[186:187], v76 offset:22528\n"
  "v_mfma_f32_16x16x32_bf16 v[24:27], a[168:171], a[132:135], v[24:27]\n"
  "s_waitcnt lgkmcnt(6)\n"
  "v_mfma_f32_16x16x32_bf16 v[12:15], a[172:175], a[128:131], v[12:15]\n"
  "ds_read_b64_tr_b16 a[188:189], v77 offset:20480\n"
  "ds_read_b64_tr_b16 a[190:191], v77 offset:22528\n"
  "v_mfma_f32_16x16x32_bf16 v[28:31], a[172:175], a[132:135], v[28:31]\n"
  "s_waitcnt lgkmcnt(6)\n"
  "v_mfma_f32_16x16x32_bf16 v[0:3], a[176:179], a[136:139], v[0:3]\n"
  "ds_read_b64_tr_b16 a[160:161], v74 offset:24576\n"
  "ds_read_b64_tr_b16 a[162:163], v74 offset:26624\n"
  "v_mfma_f32_16x16x32_bf16 v[16:19], a[176:179], a[140:143], v[16:19]\n"
  "s_add_u32 s21, s55, 0x4c000\n"
  "buffer_load_dwordx4 a[128:131], v73, s[12:15], s21 offen offset:0 sc1\n"
  "buffer_load_dwordx4 a[132:135], v73, s[12:15], s21 offen offset:1024 sc1\n"
  "s_waitcnt lgkmcnt(6)\n"
  "v_mfma_f32_16x16x32_bf16 v[4:7], a[180:183], a[136:139], v[4:7]\n"
  "ds_read_b64_tr_b16 a[164:165], v75 offset:24576\n"
  "ds_read_b64_tr_b16 a[166:167], v75 offset:26624\n"
  "v_mfma_f32_16x16x32_bf16 v[20:23], a[180:183], a[140:143], v[20:23]\n"
  "s_waitcnt lgkmcnt(6)\n"
  "v_mfma_f32_16x16x32_bf16 v[8:11], a[184:187], a[136:139], v[8:11]\n"
  "ds_read_b64_tr_b16 a[168:169], v76 offset:24576\n"
  "ds_read_b64_tr_b16 a[170:171], v76 offset:26624\n"
  "v_mfma_f32_16x16x32_bf16 v[24:27], a[184:187], a[140:143], v[24:27]\n"
  "s_waitcnt lgkmcnt(6)\n"
  "v_mfma_f32_16x16x32_bf16 v[12:15], a[188:191], a[136:139], v[12:15]\n"
  "ds_read_b64_tr_b16 a[172:173], v77 offset:24576\n"
  "ds_read_b64_tr_b16 a[174:175], v77 offset:26624\n"
  "v_mfma_f32_16x16x32_bf16 v[28:31], a[188:191], a[140:143], v[28:31]\n"
  "s_waitcnt vmcnt(52) lgkmcnt(6)\n"
  "v_mfma_f32_16x16x32_bf16 v[0:3], a[160:163], a[144:147], v[0:3]\n"
  "ds_read_b64_tr_b16 a[176:177], v74 offset:28672\n"
  "ds_read_b64_tr_b16 a[178:179], v74 offset:30720\n"
  "v_mfma_f32_16x16x32_bf16 v[16:19], a[160:163], a[148:151], v[16:19]\n"
  "s_add_u32 s21, s56, 0x4f000\n"
  "buffer_load_dwordx4 a[136:139], v73, s[12:15], s21 offen offset:0 sc1\n"
  "buffer_load_dwordx4 a[140:143], v73, s[12:15], s21 offen offset:1024 sc1\n"
  "s_waitcnt lgkmcnt(6)\n"
  "v_mfma_f32_16x16x32_bf16 v[4:7], a[164:167], a[144:147], v[4:7]\n"
  "ds_read_b64_tr_b16 a[180:181], v75 offset:28672\n"
  "ds_read_b64_tr_b16 a[182:183], v75 offset:30720\n"
  "v_mfma_f32_16x16x32_bf16 v[20:23], a[164:167], a[148:151], v[20:23]\n"
  "s_waitcnt lgkmcnt(6)\n"
  "v_mfma_f32_16x16x32_bf16 v[8:11], a[168:171], a[144:147], v[8:11]\n"
  "ds_read_b64_tr_b16 a[184:185], v76 offset:28672\n"
  "ds_read_b64_tr_b16 a[186:187], v76 offset:30720\n"
  "v_mfma_f32_16x16x32_bf16 v[24:27], a[168:171], a[148:151], v[24:27]\n"
  "s_waitcnt lgkmcnt(6)\n"
  "v_mfma_f32_16x16x32_bf16 v[12:15], a[172:175], a[144:147], v[12:15]\n"
  "ds_read_b64_tr_b16 a[188:189], v77 offset:28672\n"
  "ds_read_b64_tr_b16 a[190:191], v77 offset:30720\n"
  "v_mfma_f32_16x16x32_bf16 v[28:31], a[172:175], a[148:151], v[28:31]\n"
  "s_waitcnt vmcnt(44)\n"
  "s_barrier\n"
  "s_waitcnt lgkmcnt(6)\n"
  "v_mfma_f32_16x16x32_bf16 v[0:3], a[176:179], a[152:155], v[0:3]\n"
  "ds_read_b64_tr_b16 a[160:161], v74 offset:32768\n"
  "ds_read_b64_tr_b16 a[162:163], v74 offset:34816\n"
  "v_mfma_f32_16x16x32_bf16 v[16:19], a[176:179], a[156:159], v[16:19]\n"
  "s_add_u32 s20, s65, 0x4c0000\n"
  "s_add_u32 m0, s22, 0\n"
  "s_nop 0\n"
  "buffer_load_dwordx4 v72, s[8:11], s20 offen lds\n"
  "s_add_u32 s20, s66, 0x4f0000\n"
  "s_add_u32 m0, s22, 4096\n"
  "s_nop 0\n"
  "buffer_load_dwordx4 v72, s[8:11], s20 offen lds\n"
  "s_waitcnt lgkmcnt(6)\n"
  "v_mfma_f32_16x16x32_bf16 v[4:7], a[180:183], a[152:155], v[4:7]\n"
  "ds_read_b64_tr_b16 a[164:165], v75 offset:32768\n"
  "ds_read_b64_tr_b16 a[166:167], v75 offset:34816\n"
  "v_mfma_f32_16x16x32_bf16 v[20:23], a[180:183], a[156:159], v[20:23]\n"
  "s_add_u32 s20, s65, 0x500000\n"
  "s_add_u32 m0, s22, 8192\n"
  "s_nop 0\n"
  "buffer_load_dwordx4 v72, s[8:11], s20 offen lds\n"
  "s_add_u32 s20, s65, 0x530000\n"
  "s_add_u32 m0, s22, 12288\n"
  "s_nop 0\n"
  "buffer_load_dwordx4 v72, s[8:11], s20 offen lds\n"
  "s_waitcnt lgkmcnt(6)\n"
  "v_mfma_f32_16x16x32_bf16 v[8:11], a[184:187], a[152:155], v[8:11]\n"
  "ds_read_b64_tr_b16 a[168:169], v76 offset:32768\n"
  "ds_read_b64_tr_b16 a[170:171], v76 offset:34816\n"
  "v_mfma_f32_16x16x32_bf16 v[24:27], a[184:187], a[156:159], v[24:27]\n"
  "s_add_u32 s21, s55, 0x50000\n"
  "buffer_load_dwordx4 a[144:147], v73, s[12:15], s21 offen offset:0 sc1\n"
  "s_waitcnt lgkmcnt(6)\n"
  "v_mfma_f32_16x16x32_bf16 v[12:15], a[188:191], a[152:155], v[12:15]\n"
  "ds_read_b64_tr_b16 a[172:173], v77 offset:32768\n"
  "ds_read_b64_tr_b16 a[174:175], v77 offset:34816\n"
  "v_mfma_f32_16x16x32_bf16 v[28:31], a[188:191], a[156:159], v[28:31]\n"
  "buffer_load_dwordx4 a[148:151], v73, s[12:15], s21 offen offset:1024 sc1\n"
  "s_waitcnt lgkmcnt(6)\n"
  "v_mfma_f32_16x16x32_bf16 v[0:3], a[160:163], a[0:3], v[0:3]\n"
  "ds_read_b64_tr_b16 a[176:177], v74 offset:36864\n"
  "ds_read_b64_tr_b16 a[178:179], v74 offset:38912\n"
  "v_mfma_f32_16x16x32_bf16 v[16:19], a[160:163], a[4:7], v[16:19]\n"
  "s_add_u32 s21, s55, 0x53000\n"
  "buffer_load_dwordx4 a[152:155], v73, s[12:15], s21 offen offset:0 sc1\n"
  "buffer_load_dwordx4 a[156:159], v73, s[12:15], s21 offen offset:1024 sc1\n"
  "s_waitcnt lgkmcnt(6)\n"
  "v_mfma_f32_16x16x32_bf16 v[4:7], a[164:167], a[0:3], v[4:7]\n"
  "ds_read_b64_tr_b16 a[180:181], v75 offset:36864\n"
  "ds_read_b64_tr_b16 a[182:183], v75 offset:38912\n"
  "v_mfma_f32_16x16x32_bf16 v[20:23], a[164:167], a[4:7], v[20:23]\n"
  "s_waitcnt lgkmcnt(6)\n"
  "v_mfma_f32_16x16x32_bf16 v[8:11], a[168:171], a[0:3], v[8:11]\n"
  "ds_read_b64_tr_b16 a[184:185], v76 offset:36864\n"
  "ds_read_b64_tr_b16 a[186:187], v76 offset:38912\n"
  "v_mfma_f32_16x16x32_bf16 v[24:27], a[168:171], a[4:7], v[24:27]\n"
  "s_waitcnt lgkmcnt(6)\n"
  "v_mfma_f32_16x16x32_bf16 v[12:15], a[172:175], a[0:3], v[12:15]\n"
  "ds_read_b64_tr_b16 a[188:189], v77 offset:36864\n"
  "ds_read_b64_tr_b16 a[190:191], v77 offset:38912\n"
  "v_mfma_f32_16x16x32_bf16 v[28:31], a[172:175], a[4:7], v[28:31]\n"
  "s_waitcnt lgkmcnt(6)\n"
  "v_mfma_f32_16x16x32_bf16 v[0:3], a[176:179], a[8:11], v[0:3]\n"
  "ds_read_b64_tr_b16 a[160:161], v74 offset:40960\n"
  "ds_read_b64_tr_b16 a[162:163], v74 offset:43008\n"
  "v_mfma_f32_16x16x32_bf16 v[16:19], a[176:179], a[12:15], v[16:19]\n"
  "s_sub_u32 s21, 0x56000, s55\n"
  "buffer_load_dwordx4 a[0:3], v73, s[12:15], s21 offen offset:0 sc1\n"
  "buffer_load_dwordx4 a[4:7], v73, s[12:15], s21 offen offset:1024 sc1\n"
  "s_waitcnt lgkmcnt(6)\n"
  "v_mfma_f32_16x16x32_bf16 v[4:7], a[180:183], a[8:11], v[4:7]\n"
  "ds_read_b64_tr_b16 a[164:165], v75 offset:40960\n"
  "ds_read_b64_tr_b16 a[166:167], v75 offset:43008\n"
  "v_mfma_f32_16x16x32_bf16 v[20:23], a[180:183], a[12:15], v[20:23]\n"
  "s_mov_b32 s58, 0\n"
  "buffer_load_dwordx4 v[48:51], v73, s[24:27], s58 offen offset:0 sc1\n"
  "s_mov_b32 s58, 0\n"
  "buffer_load_dwordx4 v[52:55], v73, s[24:27], s58 offen offset:1024 sc1\n"
  "s_waitcnt lgkmcnt(6)\n"
  "v_mfma_f32_16x16x32_bf16 v[8:11], a[184:187], a[8:11], v[8:11]\n"
  "ds_read_b64_tr_b16 a[168:169], v76 offset:40960\n"
  "ds_read_b64_tr_b16 a[170:171], v76 offset:43008\n"
  "v_mfma_f32_16x16x32_bf16 v[24:27], a[184:187], a[12:15], v[24:27]\n"
  "s_waitcnt lgkmcnt(6)\n"
  "v_mfma_f32_16x16x32_bf16 v[12:15], a[188:191], a[8:11], v[12:15]\n"
  "ds_read_b64_tr_b16 a[172:173], v77 offset:40960\n"
  "ds_read_b64_tr_b16 a[174:175], v77 offset:43008\n"
  "v_mfma_f32_16x16x32_bf16 v[28:31], a[188:191], a[12:15], v[28:31]\n"
  "s_mov_b32 s58, 4096\n"
  "buffer_load_dwordx4 v[56:59], v73, s[24:27], s58 offen offset:0 sc1\n"
  "s_waitcnt vmcnt(55) lgkmcnt(6)\n"
  "v_mfma_f32_16x16x32_bf16 v[0:3], a[160:163], a[16:19], v[0:3]\n"
  "ds_read_b64_tr_b16 a[176:177], v74 offset:45056\n"
  "ds_read_b64_tr_b16 a[178:179], v74 offset:47104\n"
  "v_mfma_f32_16x16x32_bf16 v[16:19], a[160:163], a[20:23], v[16:19]\n"
  "s_sub_u32 s21, 0x57000, s56\n"
  "buffer_load_dwordx4 a[8:11], v73, s[12:15], s21 offen offset:0 sc1\n"
  "buffer_load_dwordx4 a[12:15], v73, s[12:15], s21 offen offset:1024 sc1\n"
  "s_waitcnt lgkmcnt(6)\n"
  "v_mfma_f32_16x16x32_bf16 v[4:7], a[164:167], a[16:19], v[4:7]\n"
  "ds_read_b64_tr_b16 a[180:181], v75 offset:45056\n"
  "ds_read_b64_tr_b16 a[182:183], v75 offset:47104\n"
  "v_mfma_f32_16x16x32_bf16 v[20:23], a[164:167], a[20:23], v[20:23]\n"
  "s_waitcnt lgkmcnt(6)\n"
  "v_mfma_f32_16x16x32_bf16 v[8:11], a[168:171], a[16:19], v[8:11]\n"
  "ds_read_b64_tr_b16 a[184:185], v76 offset:45056\n"
  "ds_read_b64_tr_b16 a[186:187], v76 offset:47104\n"
  "v_mfma_f32_16x16x32_bf16 v[24:27], a[168:171], a[20:23], v[24:27]\n"
  "s_mov_b32 s58, 4096\n"
  "buffer_load_dwordx4 v[60:63], v73, s[24:27], s58 offen offset:1024 sc1\n"
  "s_waitcnt lgkmcnt(6)\n"
  "v_mfma_f32_16x16x32_bf16 v[12:15], a[172:175], a[16:19], v[12:15]\n"
  "ds_read_b64_tr_b16 a[188:189], v77 offset:45056\n"
  "ds_read_b64_tr_b16 a[190:191], v77 offset:47104\n"
  "v_mfma_f32_16x16x32_bf16 v[28:31], a[172:175], a[20:23], v[28:31]\n"
  "s_waitcnt vmcnt(48)\n"
  "s_barrier\n"
  "s_waitcnt lgkmcnt(6)\n"
  "v_mfma_f32_16x16x32_bf16 v[0:3], a[176:179], a[24:27], v[0:3]\n"
  "ds_read_b64_tr_b16 a[160:161], v74 offset:49152\n"
  "ds_read_b64_tr_b16 a[162:163], v74 offset:51200\n"
  "v_mfma_f32_16x16x32_bf16 v[16:19], a[176:179], a[28:31], v[16:19]\n"
  "s_sub_u32 s20, 0x560000, s65\n"
  "s_add_u32 m0, s22, 16384\n"
  "s_nop 0\n"
  "buffer_load_dwordx4 v72, s[8:11], s20 offen lds\n"
  "s_sub_u32 s20, 0x570000, s66\n"
  "s_add_u32 m0, s22, 20480\n"
  "s_nop 0\n"
  "buffer_load_dwordx4 v72, s[8:11], s20 offen lds\n"
  "s_waitcnt lgkmcnt(6)\n"
  "v_mfma_f32_16x16x32_bf16 v[4:7], a[180:183], a[24:27], v[4:7]\n"
  "ds_read_b64_tr_b16 a[164:165], v75 offset:49152\n"
  "ds_read_b64_tr_b16 a[166:167], v75 offset:51200\n"
  "v_mfma_f32_16x16x32_bf16 v[20:23], a[180:183], a[28:31], v[20:23]\n"
  "s_sub_u32 s20, 0x5a0000, s65\n"
  "s_add_u32 m0, s22, 24576\n"
  "s_nop 0\n"
  "buffer_load_dwordx4 v72, s[8:11], s20 offen lds\n"
  "s_sub_u32 s20, 0x5d0000, s65\n"
  "s_add_u32 m0, s22, 28672\n"
  "s_nop 0\n"
  "buffer_load_dwordx4 v72, s[8:11], s20 offen lds\n"
  "s_waitcnt lgkmcnt(6)\n"
  "v_mfma_f32_16x16x32_bf16 v[8:11], a[184:187], a[24:27], v[8:11]\n"
  "ds_read_b64_tr_b16 a[168:169], v76 offset:49152\n"
  "ds_read_b64_tr_b16 a[170:171], v76 offset:51200\n"
  "v_mfma_f32_16x16x32_bf16 v[24:27], a[184:187], a[28:31], v[24:27]\n"
  "s_sub_u32 s21, 0x5a000, s55\n"
  "buffer_load_dwordx4 a[16:19], v73, s[12:15], s21 offen offset:0 sc1\n"
  "s_waitcnt lgkmcnt(6)\n"
  "v_mfma_f32_16x16x32_bf16 v[12:15], a[188:191], a[24:27], v[12:15]\n"
  "ds_read_b64_tr_b16 a[172:173], v77 offset:49152\n"
  "ds_read_b64_tr_b16 a[174:175], v77 offset:51200\n"
  "v_mfma_f32_16x16x32_bf16 v[28:31], a[188:191], a[28:31], v[28:31]\n"
  "buffer_load_dwordx4 a[20:23], v73, s[12:15], s21 offen offset:1024 sc1\n"
  "buffer_load_dwordx4 v[32:35], v79, s[28:31], 0 offen offset:0 sc1\n"
  "s_waitcnt lgkmcnt(6)\n"
  "v_mfma_f32_16x16x32_bf16 v[0:3], a[160:163], a[32:35], v[0:3]\n"
  "ds_read_b64_tr_b16 a[176:177], v74 offset:53248\n"
  "ds_read_b64_tr_b16 a[178:179], v74 offset:55296\n"
  "v_mfma_f32_16x16x32_bf16 v[16:19], a[160:163], a[36:39], v[16:19]\n"
  "s_sub_u32 s21, 0x5d000, s55\n"
  "buffer_load_dwordx4 a[24:27], v73, s[12:15], s21 offen offset:0 sc1\n"
  "s_waitcnt lgkmcnt(6)\n"
  "v_mfma_f32_16x16x32_bf16 v[4:7], a[164:167], a[32:35], v[4:7]\n"
  "ds_read_b64_tr_b16 a[180:181], v75 offset:53248\n"
  "ds_read_b64_tr_b16 a[182:183], v75 offset:55296\n"
  "v_mfma_f32_16x16x32_bf16 v[20:23], a[164:167], a[36:39], v[20:23]\n"
  "buffer_load_dwordx4 a[28:31], v73, s[12:15], s21 offen offset:1024 sc1\n"
  "s_waitcnt lgkmcnt(6)\n"
  "v_mfma_f32_16x16x32_bf16 v[8:11], a[168:171], a[32:35], v[8:11]\n"
  "ds_read_b64_tr_b16 a[184:185], v76 offset:53248\n"
  "ds_read_b64_tr_b16 a[186:187], v76 offset:55296\n"
  "v_mfma_f32_16x16x32_bf16 v[24:27], a[168:171], a[36:39], v[24:27]\n"
  "buffer_load_dwordx4 v[36:39], v79, s[28:31], 0 offen offset:64 sc1\n"
  "s_waitcnt lgkmcnt(6)\n"
  "v_mfma_f32_16x16x32_bf16 v[12:15], a[172:175], a[32:35], v[12:15]\n"
  "ds_read_b64_tr_b16 a[188:189], v77 offset:53248\n"
  "ds_read_b64_tr_b16 a[190:191], v77 offset:55296\n"
  "v_mfma_f32_16x16x32_bf16 v[28:31], a[172:175], a[36:39], v[28:31]\n"
  "s_waitcnt lgkmcnt(6)\n"
  "v_mfma_f32_16x16x32_bf16 v[0:3], a[176:179], a[40:43], v[0:3]\n"
  "ds_read_b64_tr_b16 a[160:161], v74 offset:57344\n"
  "ds_read_b64_tr_b16 a[162:163], v74 offset:59392\n"
  "v_mfma_f32_16x16x32_bf16 v[16:19], a[176:179], a[44:47], v[16:19]\n"
  "buffer_load_dwordx4 v[40:43], v79, s[28:31], 0 offen offset:128 sc1\n"
  "s_waitcnt lgkmcnt(6)\n"
  "v_mfma_f32_16x16x32_bf16 v[4:7], a[180:183], a[40:43], v[4:7]\n"
  "ds_read_b64_tr_b16 a[164:165], v75 offset:57344\n"
  "ds_read_b64_tr_b16 a[166:167], v75 offset:59392\n"
  "v_mfma_f32_16x16x32_bf16 v[20:23], a[180:183], a[44:47], v[20:23]\n"
  "buffer_load_dwordx4 v[44:47], v79, s[28:31], 0 offen offset:192 sc1\n"
  "s_waitcnt lgkmcnt(6)\n"
  "v_mfma_f32_16x16x32_bf16 v[8:11], a[184:187], a[40:43], v[8:11]\n"
  "ds_read_b64_tr_b16 a[168:169], v76 offset:57344\n"
  "ds_read_b64_tr_b16 a[170:171], v76 offset:59392\n"
  "v_mfma_f32_16x16x32_bf16 v[24:27], a[184:187], a[44:47], v[24:27]\n"
  "s_waitcnt lgkmcnt(6)\n"
  "v_mfma_f32_16x16x32_bf16 v[12:15], a[188:191], a[40:43], v[12:15]\n"
  "ds_read_b64_tr_b16 a[172:173], v77 offset:57344\n"
  "ds_read_b64_tr_b16 a[174:175], v77 offset:59392\n"
  "v_mfma_f32_16x16x32_bf16 v[28:31], a[188:191], a[44:47], v[28:31]\n"
  "s_waitcnt vmcnt(58) lgkmcnt(6)\n"
  "v_mfma_f32_16x16x32_bf16 v[0:3], a[160:163], a[48:51], v[0:3]\n"
  "ds_read_b64_tr_b16 a[176:177], v74 offset:61440\n"
  "ds_read_b64_tr_b16 a[178:179], v74 offset:63488\n"
  "v_mfma_f32_16x16x32_bf16 v[16:19], a[160:163], a[52:55], v[16:19]\n"
  "s_add_u32 s21, s55, 0x5e000\n"
  "buffer_load_dwordx4 a[32:35], v73, s[12:15], s21 offen offset:0 sc1\n"
  "s_waitcnt lgkmcnt(6)\n"
  "v_mfma_f32_16x16x32_bf16 v[4:7], a[164:167], a[48:51], v[4:7]\n"
  "ds_read_b64_tr_b16 a[180:181], v75 offset:61440\n"
  "ds_read_b64_tr_b16 a[182:183], v75 offset:63488\n"
  "v_mfma_f32_16x16x32_bf16 v[20:23], a[164:167], a[52:55], v[20:23]\n"
  "buffer_load_dwordx4 a[36:39], v73, s[12:15], s21 offen offset:1024 sc1\n"
  "s_waitcnt lgkmcnt(6)\n"
  "v_mfma_f32_16x16x32_bf16 v[8:11], a[168:171], a[48:51], v[8:11]\n"
  "ds_read_b64_tr_b16 a[184:185], v76 offset:61440\n"
  "ds_read_b64_tr_b16 a[186:187], v76 offset:63488\n"
  "v_mfma_f32_16x16x32_bf16 v[24:27], a[168:171], a[52:55], v[24:27]\n"
  "s_waitcnt lgkmcnt(6)\n"
  "v_mfma_f32_16x16x32_bf16 v[12:15], a[172:175], a[48:51], v[12:15]\n"
  "ds_read_b64_tr_b16 a[188:189], v77 offset:61440\n"
  "ds_read_b64_tr_b16 a[190:191], v77 offset:63488\n"
  "v_mfma_f32_16x16x32_bf16 v[28:31], a[172:175], a[52:55], v[28:31]\n"
  "s_waitcnt vmcnt(50)\n"
  "s_barrier\n"
  "s_waitcnt lgkmcnt(6)\n"
  "v_mfma_f32_16x16x32_bf16 v[0:3], a[176:179], a[56:59], v[0:3]\n"
  "ds_read_b64_tr_b16 a[160:161], v80 offset:0\n"
  "ds_read_b64_tr_b16 a[162:163], v80 offset:2048\n"
  "v_mfma_f32_16x16x32_bf16 v[16:19], a[176:179], a[60:63], v[16:19]\n"
  "s_add_u32 s20, s65, 0x5e0000\n"
  "s_add_u32 m0, s22, 32768\n"
  "s_nop 0\n"
  "buffer_load_dwordx4 v72, s[8:11], s20 offen lds\n"
  "s_add_u32 s20, s65, 0x610000\n"
  "s_add_u32 m0, s22, 36864\n"
  "s_nop 0\n"
  "buffer_load_dwordx4 v72, s[8:11], s20 offen lds\n"
  "s_waitcnt lgkmcnt(6)\n"
  "v_mfma_f32_16x16x32_bf16 v[4:7], a[180:183], a[56:59], v[4:7]\n"
  "ds_read_b64_tr_b16 a[164:165], v81 offset:0\n"
  "ds_read_b64_tr_b16 a[166:167], v81 offset:2048\n"
  "v_mfma_f32_16x16x32_bf16 v[20:23], a[180:183], a[60:63], v[20:23]\n"
  "s_add_u32 s20, s66, 0x640000\n"
  "s_add_u32 m0, s22, 40960\n"
  "s_nop 0\n"
  "buffer_load_dwordx4 v72, s[8:11], s20 offen lds\n"
  "s_add_u32 s20, s65, 0x650000\n"
  "s_add_u32 m0, s22, 45056\n"
  "s_nop 0\n"
  "buffer_load_dwordx4 v72, s[8:11], s20 offen lds\n"
  "s_waitcnt lgkmcnt(6)\n"
  "v_mfma_f32_16x16x32_bf16 v[8:11], a[184:187], a[56:59], v[8:11]\n"
  "ds_read_b64_tr_b16 a[168:169], v82 offset:0\n"
  "ds_read_b64_tr_b16 a[170:171], v82 offset:2048\n"
  "v_mfma_f32_16x16x32_bf16 v[24:27], a[184:187], a[60:63], v[24:27]\n"
  "s_mov_b32 s58, 24576\n"
  "buffer_load_dwordx4 v[84:87], v79, s[28:31], s58 offen offset:0 sc1\n"
  "s_waitcnt lgkmcnt(6)\n"
  "v_mfma_f32_16x16x32_bf16 v[12:15], a[188:191], a[56:59], v[12:15]\n"
  "ds_read_b64_tr_b16 a[172:173], v83 offset:0\n"
  "ds_read_b64_tr_b16 a[174:175], v83 offset:2048\n"
  "v_mfma_f32_16x16x32_bf16 v[28:31], a[188:191], a[60:63], v[28:31]\n"
  "s_mov_b32 s58, 24576\n"
  "buffer_load_dwordx4 v[88:91], v79, s[28:31], s58 offen offset:64 sc1\n"
  "s_mov_b32 s58, 24576\n"
  "buffer_load_dwordx4 v[92:95], v79, s[28:31], s58 offen offset:128 sc1\n"
  "s_waitcnt lgkmcnt(6)\n"
  "v_mfma_f32_16x16x32_bf16 v[0:3], a[160:163], a[64:67], v[0:3]\n"
  "ds_read_b64_tr_b16 a[176:177], v80 offset:4096\n"
  "ds_read_b64_tr_b16 a[178:179], v80 offset:6144\n"
  "v_mfma_f32_16x16x32_bf16 v[16:19], a[160:163], a[68:71], v[16:19]\n"
  "s_waitcnt lgkmcnt(6)\n"
  "v_mfma_f32_16x16x32_bf16 v[4:7], a[164:167], a[64:67], v[4:7]\n"
  "ds_read_b64_tr_b16 a[180:181], v81 offset:4096\n"
  "ds_read_b64_tr_b16 a[182:183], v81 offset:6144\n"
  "v_mfma_f32_16x16x32_bf16 v[20:23], a[164:167], a[68:71], v[20:23]\n"
  "s_mov_b32 s58, 24576\n"
  "buffer_load_dwordx4 v[96:99], v79, s[28:31], s58 offen offset:192 sc1\n"
  "s_waitcnt lgkmcnt(6)\n"
  "v_mfma_f32_16x16x32_bf16 v[8:11], a[168:171], a[64:67], v[8:11]\n"
  "ds_read_b64_tr_b16 a[184:185], v82 offset:4096\n"
  "ds_read_b64_tr_b16 a[186:187], v82 offset:6144\n"
  "v_mfma_f32_16x16x32_bf16 v[24:27], a[168:171], a[68:71], v[24:27]\n"
  "s_add_u32 s21, s55, 0x61000\n"
  "buffer_load_dwordx4 a[40:43], v73, s[12:15], s21 offen offset:0 sc1\n"
  "s_waitcnt lgkmcnt(6)\n"
  "v_mfma_f32_16x16x32_bf16 v[12:15], a[172:175], a[64:67], v[12:15]\n"
  "ds_read_b64_tr_b16 a[188:189], v83 offset:4096\n"
  "ds_read_b64_tr_b16 a[190:191], v83 offset:6144\n"
  "v_mfma_f32_16x16x32_bf16 v[28:31], a[172:175], a[68:71], v[28:31]\n"
  "buffer_load_dwordx4 a[44:47], v73, s[12:15], s21 offen offset:1024 sc1\n"
  "s_waitcnt lgkmcnt(6)\n"
  "v_mfma_f32_16x16x32_bf16 v[0:3], a[176:179], a[72:75], v[0:3]\n"
  "ds_read_b64_tr_b16 a[160:161], v80 offset:8192\n"
  "ds_read_b64_tr_b16 a[162:163], v80 offset:10240\n"
  "v_mfma_f32_16x16x32_bf16 v[16:19], a[176:179], a[76:79], v[16:19]\n"
  "s_waitcnt lgkmcnt(6)\n"
  "v_mfma_f32_16x16x32_bf16 v[4:7], a[180:183], a[72:75], v[4:7]\n"
  "ds_read_b64_tr_b16 a[164:165], v81 offset:8192\n"
  "ds_read_b64_tr_b16 a[166:167], v81 offset:10240\n"
  "v_mfma_f32_16x16x32_bf16 v[20:23], a[180:183], a[76:79], v[20:23]\n"
  "s_waitcnt lgkmcnt(6)\n"
  "v_mfma_f32_16x16x32_bf16 v[8:11], a[184:187], a[72:75], v[8:11]\n"
  "ds_read_b64_tr_b16 a[168:169], v82 offset:8192\n"
  "ds_read_b64_tr_b16 a[170:171], v82 offset:10240\n"
  "v_mfma_f32_16x16x32_bf16 v[24:27], a[184:187], a[76:79], v[24:27]\n"
  "s_waitcnt lgkmcnt(6)\n"
  "v_mfma_f32_16x16x32_bf16 v[12:15], a[188:191], a[72:75], v[12:15]\n"
  "ds_read_b64_tr_b16 a[172:173], v83 offset:8192\n"
  "ds_read_b64_tr_b16 a[174:175], v83 offset:10240\n"
  "v_mfma_f32_16x16x32_bf16 v[28:31], a[188:191], a[76:79], v[28:31]\n"
  "s_waitcnt vmcnt(58) lgkmcnt(6)\n"
  "v_mfma_f32_16x16x32_bf16 v[0:3], a[160:163], a[80:83], v[0:3]\n"
  "ds_read_b64_tr_b16 a[176:177], v80 offset:12288\n"
  "ds_read_b64_tr_b16 a[178:179], v80 offset:14336\n"
  "v_mfma_f32_16x16x32_bf16 v[16:19], a[160:163], a[84:87], v[16:19]\n"
  "s_add_u32 s21, s56, 0x64000\n"
  "buffer_load_dwordx4 a[48:51], v73, s[12:15], s21 offen offset:0 sc1\n"
  "s_waitcnt lgkmcnt(6)\n"
  "v_mfma_f32_16x16x32_bf16 v[4:7], a[164:167], a[80:83], v[4:7]\n"
  "ds_read_b64_tr_b16 a[180:181], v81 offset:12288\n"
  "ds_read_b64_tr_b16 a[182:183], v81 offset:14336\n"
  "v_mfma_f32_16x16x32_bf16 v[20:23], a[164:167], a[84:87], v[20:23]\n"
  "buffer_load_dwordx4 a[52:55], v73, s[12:15], s21 offen offset:1024 sc1\n"
  "s_waitcnt lgkmcnt(6)\n"
  "v_mfma_f32_16x16x32_bf16 v[8:11], a[168:171], a[80:83], v[8:11]\n"
  "ds_read_b64_tr_b16 a[184:185], v82 offset:12288\n"
  "ds_read_b64_tr_b16 a[186:187], v82 offset:14336\n"
  "v_mfma_f32_16x16x32_bf16 v[24:27], a[168:171], a[84:87], v[24:27]\n"
  "s_waitcnt lgkmcnt(6)\n"
  "v_mfma_f32_16x16x32_bf16 v[12:15], a[172:175], a[80:83], v[12:15]\n"
  "ds_read_b64_tr_b16 a[188:189], v83 offset:12288\n"
  "ds_read_b64_tr_b16 a[190:191], v83 offset:14336\n"
  "v_mfma_f32_16x16x32_bf16 v[28:31], a[172:175], a[84:87], v[28:31]\n"
  "s_waitcnt vmcnt(50)\n"
  "s_barrier\n"
  "s_waitcnt lgkmcnt(6)\n"
  "v_mfma_f32_16x16x32_bf16 v[0:3], a[176:179], a[88:91], v[0:3]\n"
  "ds_read_b64_tr_b16 a[160:161], v80 offset:16384\n"
  "ds_read_b64_tr_b16 a[162:163], v80 offset:18432\n"
  "v_mfma_f32_16x16x32_bf16 v[16:19], a[176:179], a[92:95], v[16:19]\n"
  "s_sub_u32 s20, 0x680000, s65\n"
  "s_add_u32 m0, s22, 49152\n"
  "s_nop 0\n"
  "buffer_load_dwordx4 v72, s[8:11], s20 offen lds\n"
  "s_sub_u32 s20, 0x6b0000, s65\n"
  "s_add_u32 m0, s22, 53248\n"
  "s_nop 0\n"
  "buffer_load_dwordx4 v72, s[8:11], s20 offen lds\n"
  "s_waitcnt lgkmcnt(6)\n"
  "v_mfma_f32_16x16x32_bf16 v[4:7], a[180:183], a[88:91], v[4:7]\n"
  "ds_read_b64_tr_b16 a[164:165], v81 offset:16384\n"
  "ds_read_b64_tr_b16 a[166:167], v81 offset:18432\n"
  "v_mfma_f32_16x16x32_bf16 v[20:23], a[180:183], a[92:95], v[20:23]\n"
  "s_sub_u32 s20, 0x6c0000, s66\n"
  "s_add_u32 m0, s22, 57344\n"
  "s_nop 0\n"
  "buffer_load_dwordx4 v72, s[8:11], s20 offen lds\n"
  "s_sub_u32 s20, 0x6f0000, s65\n"
  "s_add_u32 m0, s22, 61440\n"
  "s_nop 0\n"
  "buffer_load_dwordx4 v72, s[8:11], s20 offen lds\n"
  "s_waitcnt lgkmcnt(6)\n"
  "v_mfma_f32_16x16x32_bf16 v[8:11], a[184:187], a[88:91], v[8:11]\n"
  "ds_read_b64_tr_b16 a[168:169], v82 offset:16384\n"
  "ds_read_b64_tr_b16 a[170:171], v82 offset:18432\n"
  "v_mfma_f32_16x16x32_bf16 v[24:27], a[184:187], a[92:95], v[24:27]\n"
  "s_add_u32 s21, s55, 0x65000\n"
  "buffer_load_dwordx4 a[56:59], v73, s[12:15], s21 offen offset:0 sc1\n"
  "s_waitcnt lgkmcnt(6)\n"
  "v_mfma_f32_16x16x32_bf16 v[12:15], a[188:191], a[88:91], v[12:15]\n"
  "ds_read_b64_tr_b16 a[172:173], v83 offset:16384\n"
  "ds_read_b64_tr_b16 a[174:175], v83 offset:18432\n"
  "v_mfma_f32_16x16x32_bf16 v[28:31], a[188:191], a[92:95], v[28:31]\n"
  "buffer_load_dwordx4 a[60:63], v73, s[12:15], s21 offen offset:1024 sc1\n"
  "s_sub_u32 s21, 0x68000, s55\n"
  "buffer_load_dwordx4 a[64:67], v73, s[12:15], s21 offen offset:0 sc1\n"
  "s_waitcnt lgkmcnt(6)\n"
  "v_mfma_f32_16x16x32_bf16 v[0:3], a[160:163], a[96:99], v[0:3]\n"
  "ds_read_b64_tr_b16 a[176:177], v80 offset:20480\n"
  "ds_read_b64_tr_b16 a[178:179], v80 offset:22528\n"
  "v_mfma_f32_16x16x32_bf16 v[16:19], a[160:163], a[100:103], v[16:19]\n"
  "s_waitcnt lgkmcnt(6)\n"
  "v_mfma_f32_16x16x32_bf16 v[4:7], a[164:167], a[96:99], v[4:7]\n"
  "ds_read_b64_tr_b16 a[180:181], v81 offset:20480\n"
  "ds_read_b64_tr_b16 a[182:183], v81 offset:22528\n"
  "v_mfma_f32_16x16x32_bf16 v[20:23], a[164:167], a[100:103], v[20:23]\n"
  "buffer_load_dwordx4 a[68:71], v73, s[12:15], s21 offen offset:1024 sc1\n"
  "s_waitcnt lgkmcnt(6)\n"
  "v_mfma_f32_16x16x32_bf16 v[8:11], a[168:171], a[96:99], v[8:11]\n"
  "ds_read_b64_tr_b16 a[184:185], v82 offset:20480\n"
  "ds_read_b64_tr_b16 a[186:187], v82 offset:22528\n"
  "v_mfma_f32_16x16x32_bf16 v[24:27], a[168:171], a[100:103], v[24:27]\n"
  "s_sub_u32 s21, 0x6b000, s55\n"
  "buffer_load_dwordx4 a[72:75], v73, s[12:15], s21 offen offset:0 sc1\n"
  "s_waitcnt lgkmcnt(6)\n"
  "v_mfma_f32_16x16x32_bf16 v[12:15], a[172:175], a[96:99], v[12:15]\n"
  "ds_read_b64_tr_b16 a[188:189], v83 offset:20480\n"
  "ds_read_b64_tr_b16 a[190:191], v83 offset:22528\n"
  "v_mfma_f32_16x16x32_bf16 v[28:31], a[172:175], a[100:103], v[28:31]\n"
  "buffer_load_dwordx4 a[76:79], v73, s[12:15], s21 offen offset:1024 sc1\n"
  "s_waitcnt lgkmcnt(6)\n"
  "v_mfma_f32_16x16x32_bf16 v[0:3], a[176:179], a[104:107], v[0:3]\n"
  "ds_read_b64_tr_b16 a[160:161], v80 offset:24576\n"
  "ds_read_b64_tr_b16 a[162:163], v80 offset:26624\n"
  "v_mfma_f32_16x16x32_bf16 v[16:19], a[176:179], a[108:111], v[16:19]\n"
  "s_waitcnt lgkmcnt(6)\n"
  "v_mfma_f32_16x16x32_bf16 v[4:7], a[180:183], a[104:107], v[4:7]\n"
  "ds_read_b64_tr_b16 a[164:165], v81 offset:24576\n"
  "ds_read_b64_tr_b16 a[166:167], v81 offset:26624\n"
  "v_mfma_f32_16x16x32_bf16 v[20:23], a[180:183], a[108:111], v[20:23]\n"
  "s_waitcnt lgkmcnt(6)\n"
  "v_mfma_f32_16x16x32_bf16 v[8:11], a[184:187], a[104:107], v[8:11]\n"
  "ds_read_b64_tr_b16 a[168:169], v82 offset:24576\n"
  "ds_read_b64_tr_b16 a[170:171], v82 offset:26624\n"
  "v_mfma_f32_16x16x32_bf16 v[24:27], a[184:187], a[108:111], v[24:27]\n"
  "s_waitcnt lgkmcnt(6)\n"
  "v_mfma_f32_16x16x32_bf16 v[12:15], a[188:191], a[104:107], v[12:15]\n"
  "ds_read_b64_tr_b16 a[172:173], v83 offset:24576\n"
  "ds_read_b64_tr_b16 a[174:175], v83 offset:26624\n"
  "v_mfma_f32_16x16x32_bf16 v[28:31], a[188:191], a[108:111], v[28:31]\n"
  "s_waitcnt vmcnt(58) lgkmcnt(6)\n"
  "v_mfma_f32_16x16x32_bf16 v[0:3], a[160:163], a[112:115], v[0:3]\n"
  "ds_read_b64_tr_b16 a[176:177], v80 offset:28672\n"
  "ds_read_b64_tr_b16 a[178:179], v80 offset:30720\n"
  "v_mfma_f32_16x16x32_bf16 v[16:19], a[160:163], a[116:119], v[16:19]\n"
  "s_sub_u32 s21, 0x6c000, s56\n"
  "buffer_load_dwordx4 a[80:83], v73, s[12:15], s21 offen offset:0 sc1\n"
  "s_waitcnt lgkmcnt(6)\n"
  "v_mfma_f32_16x16x32_bf16 v[4:7], a[164:167], a[112:115], v[4:7]\n"
  "ds_read_b64_tr_b16 a[180:181], v81 offset:28672\n"
  "ds_read_b64_tr_b16 a[182:183], v81 offset:30720\n"
  "v_mfma_f32_16x16x32_bf16 v[20:23], a[164:167], a[116:119], v[20:23]\n"
  "buffer_load_dwordx4 a[84:87], v73, s[12:15], s21 offen offset:1024 sc1\n"
  "s_waitcnt lgkmcnt(6)\n"
  "v_mfma_f32_16x16x32_bf16 v[8:11], a[168:171], a[112:115], v[8:11]\n"
  "ds_read_b64_tr_b16 a[184:185], v82 offset:28672\n"
  "ds_read_b64_tr_b16 a[186:187], v82 offset:30720\n"
  "v_mfma_f32_16x16x32_bf16 v[24:27], a[168:171], a[116:119], v[24:27]\n"
  "s_waitcnt lgkmcnt(6)\n"
  "v_mfma_f32_16x16x32_bf16 v[12:15], a[172:175], a[112:115], v[12:15]\n"
  "ds_read_b64_tr_b16 a[188:189], v83 offset:28672\n"
  "ds_read_b64_tr_b16 a[190:191], v83 offset:30720\n"
  "v_mfma_f32_16x16x32_bf16 v[28:31], a[172:175], a[116:119], v[28:31]\n"
  "s_waitcnt vmcnt(50)\n"
  "s_barrier\n"
  "s_waitcnt lgkmcnt(6)\n"
  "v_mfma_f32_16x16x32_bf16 v[0:3], a[176:179], a[120:123], v[0:3]\n"
  "ds_read_b64_tr_b16 a[160:161], v74 offset:0\n"
  "ds_read_b64_tr_b16 a[162:163], v74 offset:2048\n"
  "v_mfma_f32_16x16x32_bf16 v[16:19], a[176:179], a[124:127], v[16:19]\n"
  "s_sub_u32 s21, 0x6f000, s55\n"
  "buffer_load_dwordx4 a[88:91], v73, s[12:15], s21 offen offset:0 sc1\n"
  "buffer_load_dwordx4 a[92:95], v73, s[12:15], s21 offen offset:1024 sc1\n"
  "s_waitcnt lgkmcnt(6)\n"
  "v_mfma_f32_16x16x32_bf16 v[4:7], a[180:183], a[120:123], v[4:7]\n"
  "ds_read_b64_tr_b16 a[164:165], v75 offset:0\n"
  "ds_read_b64_tr_b16 a[166:167], v75 offset:2048\n"
  "v_mfma_f32_16x16x32_bf16 v[20:23], a[180:183], a[124:127], v[20:23]\n"
  "s_waitcnt lgkmcnt(6)\n"
  "v_mfma_f32_16x16x32_bf16 v[8:11], a[184:187], a[120:123], v[8:11]\n"
  "ds_read_b64_tr_b16 a[168:169], v76 offset:0\n"
  "ds_read_b64_tr_b16 a[170:171], v76 offset:2048\n"
  "v_mfma_f32_16x16x32_bf16 v[24:27], a[184:187], a[124:127], v[24:27]\n"
  "s_waitcnt lgkmcnt(6)\n"
  "v_mfma_f32_16x16x32_bf16 v[12:15], a[188:191], a[120:123], v[12:15]\n"
  "ds_read_b64_tr_b16 a[172:173], v77 offset:0\n"
  "ds_read_b64_tr_b16 a[174:175], v77 offset:2048\n"
  "v_mfma_f32_16x16x32_bf16 v[28:31], a[188:191], a[124:127], v[28:31]\n"
  "s_waitcnt lgkmcnt(6)\n"
  "v_mfma_f32_16x16x32_bf16 v[0:3], a[160:163], a[128:131], v[0:3]\n"
  "ds_read_b64_tr_b16 a[176:177], v74 offset:4096\n"
  "ds_read_b64_tr_b16 a[178:179], v74 offset:6144\n"
  "v_mfma_f32_16x16x32_bf16 v[16:19], a[160:163], a[132:135], v[16:19]\n"
  "s_waitcnt lgkmcnt(6)\n"
  "v_mfma_f32_16x16x32_bf16 v[4:7], a[164:167], a[128:131], v[4:7]\n"
  "ds_read_b64_tr_b16 a[180:181], v75 offset:4096\n"
  "ds_read_b64_tr_b16 a[182:183], v75 offset:6144\n"
  "v_mfma_f32_16x16x32_bf16 v[20:23], a[164:167], a[132:135], v[20:23]\n"
  "s_waitcnt lgkmcnt(6)\n"
  "v_mfma_f32_16x16x32_bf16 v[8:11], a[168:171], a[128:131], v[8:11]\n"
  "ds_read_b64_tr_b16 a[184:185], v76 offset:4096\n"
  "ds_read_b64_tr_b16 a[186:187], v76 offset:6144\n"
  "v_mfma_f32_16x16x32_bf16 v[24:27], a[168:171], a[132:135], v[24:27]\n"
  "s_waitcnt lgkmcnt(6)\n"
  "v_mfma_f32_16x16x32_bf16 v[12:15], a[172:175], a[128:131], v[12:15]\n"
  "ds_read_b64_tr_b16 a[188:189], v77 offset:4096\n"
  "ds_read_b64_tr_b16 a[190:191], v77 offset:6144\n"
  "v_mfma_f32_16x16x32_bf16 v[28:31], a[172:175], a[132:135], v[28:31]\n"
  "s_waitcnt lgkmcnt(6)\n"
  "v_mfma_f32_16x16x32_bf16 v[0:3], a[176:179], a[136:139], v[0:3]\n"
  "ds_read_b64_tr_b16 a[160:161], v74 offset:8192\n"
  "ds_read_b64_tr_b16 a[162:163], v74 offset:10240\n"
  "v_mfma_f32_16x16x32_bf16 v[16:19], a[176:179], a[140:143], v[16:19]\n"
  "s_waitcnt lgkmcnt(6)\n"
  "v_mfma_f32_16x16x32_bf16 v[4:7], a[180:183], a[136:139], v[4:7]\n"
  "ds_read_b64_tr_b16 a[164:165], v75 offset:8192\n"
  "ds_read_b64_tr_b16 a[166:167], v75 offset:10240\n"
  "v_mfma_f32_16x16x32_bf16 v[20:23], a[180:183], a[140:143], v[20:23]\n"
  "s_waitcnt lgkmcnt(6)\n"
  "v_mfma_f32_16x16x32_bf16 v[8:11], a[184:187], a[136:139], v[8:11]\n"
  "ds_read_b64_tr_b16 a[168:169], v76 offset:8192\n"
  "ds_read_b64_tr_b16 a[170:171], v76 offset:10240\n"
  "v_mfma_f32_16x16x32_bf16 v[24:27], a[184:187], a[140:143], v[24:27]\n"
  "s_waitcnt lgkmcnt(6)\n"
  "v_mfma_f32_16x16x32_bf16 v[12:15], a[188:191], a[136:139], v[12:15]\n"
  "ds_read_b64_tr_b16 a[172:173], v77 offset:8192\n"
  "ds_read_b64_tr_b16 a[174:175], v77 offset:10240\n"
  "v_mfma_f32_16x16x32_bf16 v[28:31], a[188:191], a[140:143], v[28:31]\n"
  "s_waitcnt vmcnt(50) lgkmcnt(6)\n"
  "v_mfma_f32_16x16x32_bf16 v[0:3], a[160:163], a[144:147], v[0:3]\n"
  "ds_read_b64_tr_b16 a[176:177], v74 offset:12288\n"
  "ds_read_b64_tr_b16 a[178:179], v74 offset:14336\n"
  "v_mfma_f32_16x16x32_bf16 v[16:19], a[160:163], a[148:151], v[16:19]\n"
  "s_waitcnt lgkmcnt(6)\n"
  "v_mfma_f32_16x16x32_bf16 v[4:7], a[164:167], a[144:147], v[4:7]\n"
  "ds_read_b64_tr_b16 a[180:181], v75 offset:12288\n"
  "ds_read_b64_tr_b16 a[182:183], v75 offset:14336\n"
  "v_mfma_f32_16x16x32_bf16 v[20:23], a[164:167], a[148:151], v[20:23]\n"
  "s_waitcnt lgkmcnt(6)\n"
  "v_mfma_f32_16x16x32_bf16 v[8:11], a[168:171], a[144:147], v[8:11]\n"
  "ds_read_b64_tr_b16 a[184:185], v76 offset:12288\n"
  "ds_read_b64_tr_b16 a[186:187], v76 offset:14336\n"
  "v_mfma_f32_16x16x32_bf16 v[24:27], a[168:171], a[148:151], v[24:27]\n"
  "s_waitcnt lgkmcnt(6)\n"
  "v_mfma_f32_16x16x32_bf16 v[12:15], a[172:175], a[144:147], v[12:15]\n"
  "ds_read_b64_tr_b16 a[188:189], v77 offset:12288\n"
  "ds_read_b64_tr_b16 a[190:191], v77 offset:14336\n"
  "v_mfma_f32_16x16x32_bf16 v[28:31], a[172:175], a[148:151], v[28:31]\n"
  "s_waitcnt vmcnt(36)\n"
  "s_barrier\n"
  "s_waitcnt lgkmcnt(6)\n"
  "v_mfma_f32_16x16x32_bf16 v[0:3], a[176:179], a[152:155], v[0:3]\n"
  "ds_read_b64_tr_b16 a[160:161], v74 offset:16384\n"
  "ds_read_b64_tr_b16 a[162:163], v74 offset:18432\n"
  "v_mfma_f32_16x16x32_bf16 v[16:19], a[176:179], a[156:159], v[16:19]\n"
  "s_waitcnt lgkmcnt(6)\n"
  "v_mfma_f32_16x16x32_bf16 v[4:7], a[180:183], a[152:155], v[4:7]\n"
  "ds_read_b64_tr_b16 a[164:165], v75 offset:16384\n"
  "ds_read_b64_tr_b16 a[166:167], v75 offset:18432\n"
  "v_mfma_f32_16x16x32_bf16 v[20:23], a[180:183], a[156:159], v[20:23]\n"
  "s_waitcnt lgkmcnt(6)\n"
  "v_mfma_f32_16x16x32_bf16 v[8:11], a[184:187], a[152:155], v[8:11]\n"
  "ds_read_b64_tr_b16 a[168:169], v76 offset:16384\n"
  "ds_read_b64_tr_b16 a[170:171], v76 offset:18432\n"
  "v_mfma_f32_16x16x32_bf16 v[24:27], a[184:187], a[156:159], v[24:27]\n"
  "s_waitcnt lgkmcnt(6)\n"
  "v_mfma_f32_16x16x32_bf16 v[12:15], a[188:191], a[152:155], v[12:15]\n"
  "ds_read_b64_tr_b16 a[172:173], v77 offset:16384\n"
  "ds_read_b64_tr_b16 a[174:175], v77 offset:18432\n"
  "v_mfma_f32_16x16x32_bf16 v[28:31], a[188:191], a[156:159], v[28:31]\n"
  "s_waitcnt lgkmcnt(6)\n"
  "v_mfma_f32_16x16x32_bf16 v[0:3], a[160:163], a[0:3], v[0:3]\n"
  "ds_read_b64_tr_b16 a[176:177], v74 offset:20480\n"
  "ds_read_b64_tr_b16 a[178:179], v74 offset:22528\n"
  "v_mfma_f32_16x16x32_bf16 v[16:19], a[160:163], a[4:7], v[16:19]\n"
  "s_waitcnt lgkmcnt(6)\n"
  "v_mfma_f32_16x16x32_bf16 v[4:7], a[164:167], a[0:3], v[4:7]\n"
  "ds_read_b64_tr_b16 a[180:181], v75 offset:20480\n"
  "ds_read_b64_tr_b16 a[182:183], v75 offset:22528\n"
  "v_mfma_f32_16x16x32_bf16 v[20:23], a[164:167], a[4:7], v[20:23]\n"
  "s_waitcnt lgkmcnt(6)\n"
  "v_mfma_f32_16x16x32_bf16 v[8:11], a[168:171], a[0:3], v[8:11]\n"
  "ds_read_b64_tr_b16 a[184:185], v76 offset:20480\n"
  "ds_read_b64_tr_b16 a[186:187], v76 offset:22528\n"
  "v_mfma_f32_16x16x32_bf16 v[24:27], a[168:171], a[4:7], v[24:27]\n"
  "s_waitcnt lgkmcnt(6)\n"
  "v_mfma_f32_16x16x32_bf16 v[12:15], a[172:175], a[0:3], v[12:15]\n"
  "ds_read_b64_tr_b16 a[188:189], v77 offset:20480\n"
  "ds_read_b64_tr_b16 a[190:191], v77 offset:22528\n"
  "v_mfma_f32_16x16x32_bf16 v[28:31], a[172:175], a[4:7], v[28:31]\n"
  "s_waitcnt lgkmcnt(6)\n"
  "v_mfma_f32_16x16x32_bf16 v[0:3], a[176:179], a[8:11], v[0:3]\n"
  "ds_read_b64_tr_b16 a[160:161], v74 offset:24576\n"
  "ds_read_b64_tr_b16 a[162:163], v74 offset:26624\n"
  "v_mfma_f32_16x16x32_bf16 v[16:19], a[176:179], a[12:15], v[16:19]\n"
  "s_waitcnt lgkmcnt(6)\n"
  "v_mfma_f32_16x16x32_bf16 v[4:7], a[180:183], a[8:11], v[4:7]\n"
  "ds_read_b64_tr_b16 a[164:165], v75 offset:24576\n"
  "ds_read_b64_tr_b16 a[166:167], v75 offset:26624\n"
  "v_mfma_f32_16x16x32_bf16 v[20:23], a[180:183], a[12:15], v[20:23]\n"
  "s_waitcnt lgkmcnt(6)\n"
  "v_mfma_f32_16x16x32_bf16 v[8:11], a[184:187], a[8:11], v[8:11]\n"
  "ds_read_b64_tr_b16 a[168:169], v76 offset:24576\n"
  "ds_read_b64_tr_b16 a[170:171], v76 offset:26624\n"
  "v_mfma_f32_16x16x32_bf16 v[24:27], a[184:187], a[12:15], v[24:27]\n"
  "s_waitcnt lgkmcnt(6)\n"
  "v_mfma_f32_16x16x32_bf16 v[12:15], a[188:191], a[8:11], v[12:15]\n"
  "ds_read_b64_tr_b16 a[172:173], v77 offset:24576\n"
  "ds_read_b64_tr_b16 a[174:175], v77 offset:26624\n"
  "v_mfma_f32_16x16x32_bf16 v[28:31], a[188:191], a[12:15], v[28:31]\n"
  "s_waitcnt vmcnt(34) lgkmcnt(6)\n"
  "v_mfma_f32_16x16x32_bf16 v[0:3], a[160:163], a[16:19], v[0:3]\n"
  "ds_read_b64_tr_b16 a[176:177], v74 offset:28672\n"
  "ds_read_b64_tr_b16 a[178:179], v74 offset:30720\n"
  "v_mfma_f32_16x16x32_bf16 v[16:19], a[160:163], a[20:23], v[16:19]\n"
  "s_waitcnt lgkmcnt(6)\n"
  "v_mfma_f32_16x16x32_bf16 v[4:7], a[164:167], a[16:19], v[4:7]\n"
  "ds_read_b64_tr_b16 a[180:181], v75 offset:28672\n"
  "ds_read_b64_tr_b16 a[182:183], v75 offset:30720\n"
  "v_mfma_f32_16x16x32_bf16 v[20:23], a[164:167], a[20:23], v[20:23]\n"
  "s_waitcnt lgkmcnt(6)\n"
  "v_mfma_f32_16x16x32_bf16 v[8:11], a[168:171], a[16:19], v[8:11]\n"
  "ds_read_b64_tr_b16 a[184:185], v76 offset:28672\n"
  "ds_read_b64_tr_b16 a[186:187], v76 offset:30720\n"
  "v_mfma_f32_16x16x32_bf16 v[24:27], a[168:171], a[20:23], v[24:27]\n"
  "s_waitcnt lgkmcnt(6)\n"
  "v_mfma_f32_16x16x32_bf16 v[12:15], a[172:175], a[16:19], v[12:15]\n"
  "ds_read_b64_tr_b16 a[188:189], v77 offset:28672\n"
  "ds_read_b64_tr_b16 a[190:191], v77 offset:30720\n"
  "v_mfma_f32_16x16x32_bf16 v[28:31], a[172:175], a[20:23], v[28:31]\n"
  "s_waitcnt vmcnt(22)\n"
  "s_barrier\n"
  "s_waitcnt lgkmcnt(6)\n"
  "v_mfma_f32_16x16x32_bf16 v[0:3], a[176:179], a[24:27], v[0:3]\n"
  "ds_read_b64_tr_b16 a[160:161], v74 offset:32768\n"
  "ds_read_b64_tr_b16 a[162:163], v74 offset:34816\n"
  "v_mfma_f32_16x16x32_bf16 v[16:19], a[176:179], a[28:31], v[16:19]\n"
  "s_waitcnt lgkmcnt(6)\n"
  "v_mfma_f32_16x16x32_bf16 v[4:7], a[180:183], a[24:27], v[4:7]\n"
  "ds_read_b64_tr_b16 a[164:165], v75 offset:32768\n"
  "ds_read_b64_tr_b16 a[166:167], v75 offset:34816\n"
  "v_mfma_f32_16x16x32_bf16 v[20:23], a[180:183], a[28:31], v[20:23]\n"
  "s_waitcnt lgkmcnt(6)\n"
  "v_mfma_f32_16x16x32_bf16 v[8:11], a[184:187], a[24:27], v[8:11]\n"
  "ds_read_b64_tr_b16 a[168:169], v76 offset:32768\n"
  "ds_read_b64_tr_b16 a[170:171], v76 offset:34816\n"
  "v_mfma_f32_16x16x32_bf16 v[24:27], a[184:187], a[28:31], v[24:27]\n"
  "s_waitcnt lgkmcnt(6)\n"
  "v_mfma_f32_16x16x32_bf16 v[12:15], a[188:191], a[24:27], v[12:15]\n"
  "ds_read_b64_tr_b16 a[172:173], v77 offset:32768\n"
  "ds_read_b64_tr_b16 a[174:175], v77 offset:34816\n"
  "v_mfma_f32_16x16x32_bf16 v[28:31], a[188:191], a[28:31], v[28:31]\n"
  "s_waitcnt lgkmcnt(6)\n"
  "v_mfma_f32_16x16x32_bf16 v[0:3], a[160:163], a[32:35], v[0:3]\n"
  "ds_read_b64_tr_b16 a[176:177], v74 offset:36864\n"
  "ds_read_b64_tr_b16 a[178:179], v74 offset:38912\n"
  "v_mfma_f32_16x16x32_bf16 v[16:19], a[160:163], a[36:39], v[16:19]\n"
  "s_waitcnt lgkmcnt(6)\n"
  "v_mfma_f32_16x16x32_bf16 v[4:7], a[164:167], a[32:35], v[4:7]\n"
  "ds_read_b64_tr_b16 a[180:181], v75 offset:36864\n"
  "ds_read_b64_tr_b16 a[182:183], v75 offset:38912\n"
  "v_mfma_f32_16x16x32_bf16 v[20:23], a[164:167], a[36:39], v[20:23]\n"
  "s_waitcnt lgkmcnt(6)\n"
  "v_mfma_f32_16x16x32_bf16 v[8:11], a[168:171], a[32:35], v[8:11]\n"
  "ds_read_b64_tr_b16 a[184:185], v76 offset:36864\n"
  "ds_read_b64_tr_b16 a[186:187], v76 offset:38912\n"
  "v_mfma_f32_16x16x32_bf16 v[24:27], a[168:171], a[36:39], v[24:27]\n"
  "s_waitcnt lgkmcnt(6)\n"
  "v_mfma_f32_16x16x32_bf16 v[12:15], a[172:175], a[32:35], v[12:15]\n"
  "ds_read_b64_tr_b16 a[188:189], v77 offset:36864\n"
  "ds_read_b64_tr_b16 a[190:191], v77 offset:38912\n"
  "v_mfma_f32_16x16x32_bf16 v[28:31], a[172:175], a[36:39], v[28:31]\n"
  "s_waitcnt vmcnt(16) lgkmcnt(6)\n"
  "v_mfma_f32_16x16x32_bf16 v[0:3], a[176:179], a[40:43], v[0:3]\n"
  "ds_read_b64_tr_b16 a[160:161], v74 offset:40960\n"
  "ds_read_b64_tr_b16 a[162:163], v74 offset:43008\n"
  "v_mfma_f32_16x16x32_bf16 v[16:19], a[176:179], a[44:47], v[16:19]\n"
  "s_waitcnt lgkmcnt(6)\n"
  "v_mfma_f32_16x16x32_bf16 v[4:7], a[180:183], a[40:43], v[4:7]\n"
  "ds_read_b64_tr_b16 a[164:165], v75 offset:40960\n"
  "ds_read_b64_tr_b16 a[166:167], v75 offset:43008\n"
  "v_mfma_f32_16x16x32_bf16 v[20:23], a[180:183], a[44:47], v[20:23]\n"
  "s_waitcnt lgkmcnt(6)\n"
  "v_mfma_f32_16x16x32_bf16 v[8:11], a[184:187], a[40:43], v[8:11]\n"
  "ds_read_b64_tr_b16 a[168:169], v76 offset:40960\n"
  "ds_read_b64_tr_b16 a[170:171], v76 offset:43008\n"
  "v_mfma_f32_16x16x32_bf16 v[24:27], a[184:187], a[44:47], v[24:27]\n"
  "s_waitcnt lgkmcnt(6)\n"
  "v_mfma_f32_16x16x32_bf16 v[12:15], a[188:191], a[40:43], v[12:15]\n"
  "ds_read_b64_tr_b16 a[172:173], v77 offset:40960\n"
  "ds_read_b64_tr_b16 a[174:175], v77 offset:43008\n"
  "v_mfma_f32_16x16x32_bf16 v[28:31], a[188:191], a[44:47], v[28:31]\n"
  "s_waitcnt vmcnt(14) lgkmcnt(6)\n"
  "v_mfma_f32_16x16x32_bf16 v[0:3], a[160:163], a[48:51], v[0:3]\n"
  "ds_read_b64_tr_b16 a[176:177], v74 offset:45056\n"
  "ds_read_b64_tr_b16 a[178:179], v74 offset:47104\n"
  "v_mfma_f32_16x16x32_bf16 v[16:19], a[160:163], a[52:55], v[16:19]\n"
  "s_waitcnt lgkmcnt(6)\n"
  "v_mfma_f32_16x16x32_bf16 v[4:7], a[164:167], a[48:51], v[4:7]\n"
  "ds_read_b64_tr_b16 a[180:181], v75 offset:45056\n"
  "ds_read_b64_tr_b16 a[182:183], v75 offset:47104\n"
  "v_mfma_f32_16x16x32_bf16 v[20:23], a[164:167], a[52:55], v[20:23]\n"
  "s_waitcnt lgkmcnt(6)\n"
  "v_mfma_f32_16x16x32_bf16 v[8:11], a[168:171], a[48:51], v[8:11]\n"
  "ds_read_b64_tr_b16 a[184:185], v76 offset:45056\n"
  "ds_read_b64_tr_b16 a[186:187], v76 offset:47104\n"
  "v_mfma_f32_16x16x32_bf16 v[24:27], a[168:171], a[52:55], v[24:27]\n"
  "s_waitcnt lgkmcnt(6)\n"
  "v_mfma_f32_16x16x32_bf16 v[12:15], a[172:175], a[48:51], v[12:15]\n"
  "ds_read_b64_tr_b16 a[188:189], v77 offset:45056\n"
  "ds_read_b64_tr_b16 a[190:191], v77 offset:47104\n"
  "v_mfma_f32_16x16x32_bf16 v[28:31], a[172:175], a[52:55], v[28:31]\n"
  "s_waitcnt vmcnt(10)\n"
  "s_barrier\n"
  "s_waitcnt vmcnt(8) lgkmcnt(6)\n"
  "v_mfma_f32_16x16x32_bf16 v[0:3], a[176:179], a[56:59], v[0:3]\n"
  "ds_read_b64_tr_b16 a[160:161], v74 offset:49152\n"
  "ds_read_b64_tr_b16 a[162:163], v74 offset:51200\n"
  "v_mfma_f32_16x16x32_bf16 v[16:19], a[176:179], a[60:63], v[16:19]\n"
  "s_waitcnt lgkmcnt(6)\n"
  "v_mfma_f32_16x16x32_bf16 v[4:7], a[180:183], a[56:59], v[4:7]\n"
  "ds_read_b64_tr_b16 a[164:165], v75 offset:49152\n"
  "ds_read_b64_tr_b16 a[166:167], v75 offset:51200\n"
  "v_mfma_f32_16x16x32_bf16 v[20:23], a[180:183], a[60:63], v[20:23]\n"
  "s_waitcnt lgkmcnt(6)\n"
  "v_mfma_f32_16x16x32_bf16 v[8:11], a[184:187], a[56:59], v[8:11]\n"
  "ds_read_b64_tr_b16 a[168:169], v76 offset:49152\n"
  "ds_read_b64_tr_b16 a[170:171], v76 offset:51200\n"
  "v_mfma_f32_16x16x32_bf16 v[24:27], a[184:187], a[60:63], v[24:27]\n"
  "s_waitcnt lgkmcnt(6)\n"
  "v_mfma_f32_16x16x32_bf16 v[12:15], a[188:191], a[56:59], v[12:15]\n"
  "ds_read_b64_tr_b16 a[172:173], v77 offset:49152\n"
  "ds_read_b64_tr_b16 a[174:175], v77 offset:51200\n"
  "v_mfma_f32_16x16x32_bf16 v[28:31], a[188:191], a[60:63], v[28:31]\n"
  "s_waitcnt vmcnt(6) lgkmcnt(6)\n"
  "v_mfma_f32_16x16x32_bf16 v[0:3], a[160:163], a[64:67], v[0:3]\n"
  "ds_read_b64_tr_b16 a[176:177], v74 offset:53248\n"
  "ds_read_b64_tr_b16 a[178:179], v74 offset:55296\n"
  "v_mfma_f32_16x16x32_bf16 v[16:19], a[160:163], a[68:71], v[16:19]\n"
  "s_waitcnt lgkmcnt(6)\n"
  "v_mfma_f32_16x16x32_bf16 v[4:7], a[164:167], a[64:67], v[4:7]\n"
  "ds_read_b64_tr_b16 a[180:181], v75 offset:53248\n"
  "ds_read_b64_tr_b16 a[182:183], v75 offset:55296\n"
  "v_mfma_f32_16x16x32_bf16 v[20:23], a[164:167], a[68:71], v[20:23]\n"
  "s_waitcnt lgkmcnt(6)\n"
  "v_mfma_f32_16x16x32_bf16 v[8:11], a[168:171], a[64:67], v[8:11]\n"
  "ds_read_b64_tr_b16 a[184:185], v76 offset:53248\n"
  "ds_read_b64_tr_b16 a[186:187], v76 offset:55296\n"
  "v_mfma_f32_16x16x32_bf16 v[24:27], a[168:171], a[68:71], v[24:27]\n"
  "s_waitcnt lgkmcnt(6)\n"
  "v_mfma_f32_16x16x32_bf16 v[12:15], a[172:175], a[64:67], v[12:15]\n"
  "ds_read_b64_tr_b16 a[188:189], v77 offset:53248\n"
  "ds_read_b64_tr_b16 a[190:191], v77 offset:55296\n"
  "v_mfma_f32_16x16x32_bf16 v[28:31], a[172:175], a[68:71], v[28:31]\n"
  "s_waitcnt vmcnt(4) lgkmcnt(6)\n"
  "v_mfma_f32_16x16x32_bf16 v[0:3], a[176:179], a[72:75], v[0:3]\n"
  "ds_read_b64_tr_b16 a[160:161], v74 offset:57344\n"
  "ds_read_b64_tr_b16 a[162:163], v74 offset:59392\n"
  "v_mfma_f32_16x16x32_bf16 v[16:19], a[176:179], a[76:79], v[16:19]\n"
  "s_waitcnt lgkmcnt(6)\n"
  "v_mfma_f32_16x16x32_bf16 v[4:7], a[180:183], a[72:75], v[4:7]\n"
  "ds_read_b64_tr_b16 a[164:165], v75 offset:57344\n"
  "ds_read_b64_tr_b16 a[166:167], v75 offset:59392\n"
  "v_mfma_f32_16x16x32_bf16 v[20:23], a[180:183], a[76:79], v[20:23]\n"
  "s_waitcnt lgkmcnt(6)\n"
  "v_mfma_f32_16x16x32_bf16 v[8:11], a[184:187], a[72:75], v[8:11]\n"
  "ds_read_b64_tr_b16 a[168:169], v76 offset:57344\n"
  "ds_read_b64_tr_b16 a[170:171], v76 offset:59392\n"
  "v_mfma_f32_16x16x32_bf16 v[24:27], a[184:187], a[76:79], v[24:27]\n"
  "s_waitcnt lgkmcnt(6)\n"
  "v_mfma_f32_16x16x32_bf16 v[12:15], a[188:191], a[72:75], v[12:15]\n"
  "ds_read_b64_tr_b16 a[172:173], v77 offset:57344\n"
  "ds_read_b64_tr_b16 a[174:175], v77 offset:59392\n"
  "v_mfma_f32_16x16x32_bf16 v[28:31], a[188:191], a[76:79], v[28:31]\n"
  "s_waitcnt vmcnt(2) lgkmcnt(6)\n"
  "v_mfma_f32_16x16x32_bf16 v[0:3], a[160:163], a[80:83], v[0:3]\n"
  "ds_read_b64_tr_b16 a[176:177], v74 offset:61440\n"
  "ds_read_b64_tr_b16 a[178:179], v74 offset:63488\n"
  "v_mfma_f32_16x16x32_bf16 v[16:19], a[160:163], a[84:87], v[16:19]\n"
  "s_waitcnt lgkmcnt(6)\n"
  "v_mfma_f32_16x16x32_bf16 v[4:7], a[164:167], a[80:83], v[4:7]\n"
  "ds_read_b64_tr_b16 a[180:181], v75 offset:61440\n"
  "ds_read_b64_tr_b16 a[182:183], v75 offset:63488\n"
  "v_mfma_f32_16x16x32_bf16 v[20:23], a[164:167], a[84:87], v[20:23]\n"
  "s_waitcnt lgkmcnt(6)\n"
  "v_mfma_f32_16x16x32_bf16 v[8:11], a[168:171], a[80:83], v[8:11]\n"
  "ds_read_b64_tr_b16 a[184:185], v76 offset:61440\n"
  "ds_read_b64_tr_b16 a[186:187], v76 offset:63488\n"
  "v_mfma_f32_16x16x32_bf16 v[24:27], a[168:171], a[84:87], v[24:27]\n"
  "s_waitcnt lgkmcnt(6)\n"
  "v_mfma_f32_16x16x32_bf16 v[12:15], a[172:175], a[80:83], v[12:15]\n"
  "ds_read_b64_tr_b16 a[188:189], v77 offset:61440\n"
  "ds_read_b64_tr_b16 a[190:191], v77 offset:63488\n"
  "v_mfma_f32_16x16x32_bf16 v[28:31], a[172:175], a[84:87], v[28:31]\n"
  "s_waitcnt vmcnt(0) lgkmcnt(6)\n"
  "v_mfma_f32_16x16x32_bf16 v[0:3], a[176:179], a[88:91], v[0:3]\n"
  "v_mfma_f32_16x16x32_bf16 v[16:19], a[176:179], a[92:95], v[16:19]\n"
  "s_waitcnt lgkmcnt(4)\n"
  "v_mfma_f32_16x16x32_bf16 v[4:7], a[180:183], a[88:91], v[4:7]\n"
  "v_mfma_f32_16x16x32_bf16 v[20:23], a[180:183], a[92:95], v[20:23]\n"
  "s_waitcnt lgkmcnt(2)\n"
  "v_mfma_f32_16x16x32_bf16 v[8:11], a[184:187], a[88:91], v[8:11]\n"
  "v_mfma_f32_16x16x32_bf16 v[24:27], a[184:187], a[92:95], v[24:27]\n"
  "s_waitcnt lgkmcnt(0)\n"
  "v_mfma_f32_16x16x32_bf16 v[12:15], a[188:191], a[88:91], v[12:15]\n"
  "v_mfma_f32_16x16x32_bf16 v[28:31], a[188:191], a[92:95], v[28:31]\n"
  "s_nop 7\n"
  "v_pk_add_f32 v[32:33], v[32:33], v[84:85]\n"
  "v_pk_add_f32 v[34:35], v[34:35], v[86:87]\n"
  "v_pk_add_f32 v[36:37], v[36:37], v[88:89]\n"
  "v_pk_add_f32 v[38:39], v[38:39], v[90:91]\n"
  "v_pk_add_f32 v[40:41], v[40:41], v[92:93]\n"
  "v_pk_add_f32 v[42:43], v[42:43], v[94:95]\n"
  "v_pk_add_f32 v[44:45], v[44:45], v[96:97]\n"
  "v_pk_add_f32 v[46:47], v[46:47], v[98:99]\n"
  "s_add_u32 s58, s23, 0\n"
  "v_lshlrev_b32 v64, 16, v48\n"
  "v_and_b32 v65, 0xffff0000, v48\n"
  "v_lshlrev_b32 v66, 16, v49\n"
  "v_and_b32 v67, 0xffff0000, v49\n"
  "v_pk_fma_f32 v[0:1], v[0:1], v[32:33], v[64:65]\n"
  "v_pk_fma_f32 v[2:3], v[2:3], v[34:35], v[66:67]\n"
  "v_cvt_pk_bf16_f32 v0, v0, v1\n"
  "v_cvt_pk_bf16_f32 v1, v2, v3\n"
  "v_lshlrev_b32 v68, 16, v50\n"
  "v_and_b32 v69, 0xffff0000, v50\n"
  "v_lshlrev_b32 v70, 16, v51\n"
  "v_and_b32 v71, 0xffff0000, v51\n"
  "v_pk_fma_f32 v[4:5], v[4:5], v[36:37], v[68:69]\n"
  "v_pk_fma_f32 v[6:7], v[6:7], v[38:39], v[70:71]\n"
  "v_cvt_pk_bf16_f32 v2, v4, v5\n"
  "v_cvt_pk_bf16_f32 v3, v6, v7\n"
  "s_nop 1\n"
  "v_permlane16_swap_b32 v0, v2\n"
  "v_permlane16_swap_b32 v1, v3\n"
  "buffer_store_dwordx4 v[0:3], v78, s[16:19], s58 offen offset:0\n"
  "v_lshlrev_b32 v64, 16, v56\n"
  "v_and_b32 v65, 0xffff0000, v56\n"
  "v_lshlrev_b32 v66, 16, v57\n"
  "v_and_b32 v67, 0xffff0000, v57\n"
  "v_pk_fma_f32 v[8:9], v[8:9], v[40:41], v[64:65]\n"
  "v_pk_fma_f32 v[10:11], v[10:11], v[42:43], v[66:67]\n"
  "v_cvt_pk_bf16_f32 v8, v8, v9\n"
  "v_cvt_pk_bf16_f32 v9, v10, v11\n"
  "v_lshlrev_b32 v68, 16, v58\n"
  "v_and_b32 v69, 0xffff0000, v58\n"
  "v_lshlrev_b32 v70, 16, v59\n"
  "v_and_b32 v71, 0xffff0000, v59\n"
  "v_pk_fma_f32 v[12:13], v[12:13], v[44:45], v[68:69]\n"
  "v_pk_fma_f32 v[14:15], v[14:15], v[46:47], v[70:71]\n"
  "v_cvt_pk_bf16_f32 v10, v12, v13\n"
  "v_cvt_pk_bf16_f32 v11, v14, v15\n"
  "s_nop 1\n"
  "v_permlane16_swap_b32 v8, v10\n"
  "v_permlane16_swap_b32 v9, v11\n"
  "buffer_store_dwordx4 v[8:11], v78, s[16:19], s58 offen offset:64\n"
  "s_add_u32 s58, s23, 32768\n"
  "v_lshlrev_b32 v64, 16, v52\n"
  "v_and_b32 v65, 0xffff0000, v52\n"
  "v_lshlrev_b32 v66, 16, v53\n"
  "v_and_b32 v67, 0xffff0000, v53\n"
  "v_pk_fma_f32 v[16:17], v[16:17], v[32:33], v[64:65]\n"
  "v_pk_fma_f32 v[18:19], v[18:19], v[34:35], v[66:67]\n"
  "v_cvt_pk_bf16_f32 v16, v16, v17\n"
  "v_cvt_pk_bf16_f32 v17, v18, v19\n"
  "v_lshlrev_b32 v68, 16, v54\n"
  "v_and_b32 v69, 0xffff0000, v54\n"
  "v_lshlrev_b32 v70, 16, v55\n"
  "v_and_b32 v71, 0xffff0000, v55\n"
  "v_pk_fma_f32 v[20:21], v[20:21], v[36:37], v[68:69]\n"
  "v_pk_fma_f32 v[22:23], v[22:23], v[38:39], v[70:71]\n"
  "v_cvt_pk_bf16_f32 v18, v20, v21\n"
  "v_cvt_pk_bf16_f32 v19, v22, v23\n"
  "s_nop 1\n"
  "v_permlane16_swap_b32 v16, v18\n"
  "v_permlane16_swap_b32 v17, v19\n"
  "buffer_store_dwordx4 v[16:19], v78, s[16:19], s58 offen offset:0\n"
  "v_lshlrev_b32 v64, 16, v60\n"
  "v_and_b32 v65, 0xffff0000, v60\n"
  "v_lshlrev_b32 v66, 16, v61\n"
  "v_and_b32 v67, 0xffff0000, v61\n"
  "v_pk_fma_f32 v[24:25], v[24:25], v[40:41], v[64:65]\n"
  "v_pk_fma_f32 v[26:27], v[26:27], v[42:43], v[66:67]\n"
  "v_cvt_pk_bf16_f32 v24, v24, v25\n"
  "v_cvt_pk_bf16_f32 v25, v26, v27\n"
  "v_lshlrev_b32 v68, 16, v62\n"
  "v_and_b32 v69, 0xffff0000, v62\n"
  "v_lshlrev_b32 v70, 16, v63\n"
  "v_and_b32 v71, 0xffff0000, v63\n"
  "v_pk_fma_f32 v[28:29], v[28:29], v[44:45], v[68:69]\n"
  "v_pk_fma_f32 v[30:31], v[30:31], v[46:47], v[70:71]\n"
  "v_cvt_pk_bf16_f32 v26, v28, v29\n"
  "v_cvt_pk_bf16_f32 v27, v30, v31\n"
  "s_nop 1\n"
  "v_permlane16_swap_b32 v24, v26\n"
  "v_permlane16_swap_b32 v25, v27\n"
  "buffer_store_dwordx4 v[24:27], v78, s[16:19], s58 offen offset:64\n"
  "k4_code_end:\n"
    :: "{v72}"(df4),"{v73}"(l16),"{v74}"(rd0),"{v75}"(rd1),"{v76}"(rd2),"{v77}"(rd3),
       "{v78}"(oo4),"{v79}"(g4*16u),"{v80}"(rd0+65536u),"{v81}"(rd1+65536u),"{v82}"(rd2+65536u),"{v83}"(rd3+65536u),
       "{v103}"(pof),"{v106}"(l16+odd*8u),"{v107}"(ln*128u),
       "{v[144:147]}"(hd0),"{v[148:151]}"(hd1),"{v[152:155]}"(hd2),"{v[156:159]}"(hd3),
       "{v[208:209]}"(he0),"{v[212:213]}"(he1),"{v[216:217]}"(he2),"{v[220:221]}"(he3),
       "{s[8:11]}"(srdW4),"{s[12:15]}"(srdH4),"{s[16:19]}"(srdO4),"{s[24:27]}"(srdX4),"{s[28:31]}"(srdG4),
       "{s22}"(ldsb+w*1024u),"{s23}"(os0),"{s59}"(rot4),"{s[60:61]}"(fpl+4096),"{s[62:63]}"(fpl+8192),
       "{s[68:71]}"(srdO3),"{s72}"(ooff+8192u),"{s73}"(hoff),"{s[74:75]}"(hfl+8192+T*128+fb),"{s[76:77]}"(hfl+4096+T*128+fb)
    : "v0","v1","v2","v3","v4","v5","v6","v7","v8","v9","v10","v11","v12","v13","v14","v15","v16","v17","v18","v19","v20","v21","v22","v23","v24","v25","v26","v27","v28","v29","v30","v31","v32","v33","v34","v35","v36","v37","v38","v39","v40","v41","v42","v43","v44","v45","v46","v47","v48","v49","v50","v51","v52","v53","v54","v55","v56","v57","v58","v59","v60","v61","v62","v63","v64","v65","v66","v67","v68","v69","v70","v71","v84","v85","v86","v87","v88","v89","v90","v91","v92","v93","v94","v95","v96","v97","v98","v99","v104","v105","v108","a0","a1","a2","a3","a4","a5","a6","a7","a8","a9","a10","a11","a12","a13","a14","a15","a16","a17","a18","a19","a20","a21","a22","a23","a24","a25","a26","a27","a28","a29","a30","a31","a32","a33","a34","a35","a36","a37","a38","a39","a40","a41","a42","a43","a44","a45","a46","a47","a48","a49","a50","a51","a52","a53","a54","a55","a56","a57","a58","a59","a60","a61","a62","a63","a64","a65","a66","a67","a68","a69","a70","a71","a72","a73","a74","a75","a76","a77","a78","a79","a80","a81","a82","a83","a84","a85","a86","a87","a88","a89","a90","a91","a92","a93","a94","a95","a96","a97","a98","a99","a100","a101","a102","a103","a104","a105","a106","a107","a108","a109","a110","a111","a112","a113","a114","a115","a116","a117","a118","a119","a120","a121","a122","a123","a124","a125","a126","a127","a128","a129","a130","a131","a132","a133","a134","a135","a136","a137","a138","a139","a140","a141","a142","a143","a144","a145","a146","a147","a148","a149","a150","a151","a152","a153","a154","a155","a156","a157","a158","a159","a160","a161","a162","a163","a164","a165","a166","a167","a168","a169","a170","a171","a172","a173","a174","a175","a176","a177","a178","a179","a180","a181","a182","a183","a184","a185","a186","a187","a188","a189","a190","a191","s20","s21","s58","s64","s54","s55","s56","s57","s65","s66","s67","m0","scc","vcc","memory");
  }
}
#define ST(i)
extern "C" void kernel_launch(void* const* d_in, const int* in_sizes, int n_in, void* d_out, int out_size, void* d_ws, size_t ws_size, hipStream_t stream){
  const bf16_t* q=(const bf16_t*)d_in[0]; const bf16_t* k=(const bf16_t*)d_in[1]; const bf16_t* v=(const bf16_t*)d_in[2];
  const bf16_t* sk=(const bf16_t*)d_in[3]; const bf16_t* ssmk=(const bf16_t*)d_in[4]; const bf16_t* ssmD=(const bf16_t*)d_in[5];
  const bf16_t* wg=(const bf16_t*)d_in[7]; const bf16_t* wu=(const bf16_t*)d_in[8]; const bf16_t* wd=(const bf16_t*)d_in[9];
  const bf16_t* c=(const bf16_t*)d_in[10]; const bf16_t* wm=(const bf16_t*)d_in[11];
  char* ws=(char*)d_ws+WS_BASE;
  float* modp=(float*)ws; float* modf=(float*)(ws+(64<<10)); float2* stats=(float2*)(ws+(128<<10)); unsigned* flags=(unsigned*)(ws+(192<<10));
  bf16_t* kvTr=(bf16_t*)(ws+(1<<20)); char* yg=ws+(6<<20); bf16_t* x1t=(bf16_t*)(ws+(WS_X1<<20)); bf16_t* h2t=(bf16_t*)(ws+(WS_H2<<20)); bf16_t* Ht=(bf16_t*)(ws+(WS_HT<<20));
  for(int r=0;r<RPT0;r++) k0_pre<<<256,256,0,stream>>>(k,v,sk,ssmk,kvTr,flags
    ST(0));
  k123_gateup<<<256,256,0,stream>>>(ssmk,kvTr,ssmD,yg,c,wm,modp,modf,q,x1t,h2t,flags,wg,wu,Ht,wd,(bf16_t*)d_out ST(1) ST(2) ST(3) ST(4));
}
```
